```python
import torch

def _softmax(x, axis=-1):
    return torch.softmax(x, dim=axis)
def _gelu(x):
    return torch.nn.functional.gelu(x, approximate="tanh")
def _silu(x):
    return torch.nn.functional.silu(x)
def _sigmoid(x):
    return torch.sigmoid(x)
def _layernorm(x, axis=-1, eps=1e-5):
    m = x.mean(dim=axis, keepdim=True)
    v = x.var(dim=axis, keepdim=True, unbiased=False)
    return (x - m) / torch.sqrt(v + eps)
def _scatter_add(tgt, idx, src, axis=0):
    return tgt.clone().index_add_(axis, idx, src)
def _segment_sum(src, seg, n):
    out = torch.zeros((n,) + src.shape[1:], dtype=src.dtype)
    return out.index_add_(0, seg, src)
def _pad(x, pw):
    flat = tuple(v for pair in reversed(pw) for v in pair)
    return torch.nn.functional.pad(x, flat)

kernel_name = 'recurrent_scan__grouped_kv_mqa__moe_arch_mod__prompt'

def setup_inputs():

    _w = (lambda a: (lambda t: t.to(torch.bfloat16) if t.is_floating_point() else t)(torch.as_tensor(a)))
    def _A_setup():
        B, S, D, H = 1, 2048, 512, 32
        Dh = D // H
        return {
            "x": _w((lambda *s: torch.randn(*s, dtype=torch.bfloat16) * 0.05)(B, S, D)),
            "wq": _w((lambda *s: torch.randn(*s, dtype=torch.bfloat16) * 0.05)(D, H * Dh)),
            "wk": _w((lambda *s: torch.randn(*s, dtype=torch.bfloat16) * 0.05)(D, Dh)),
            "wv": _w((lambda *s: torch.randn(*s, dtype=torch.bfloat16) * 0.05)(D, Dh)),
            "wo": _w((lambda *s: torch.randn(*s, dtype=torch.bfloat16) * 0.05)(H * Dh, D)),
        }
    _A_in = _A_setup()

    def _B_setup():
        B, S, D, F, C = 1, 2048, 512, 1792, 2
        return {
            "x": _w((lambda *s: torch.randn(*s, dtype=torch.bfloat16) * 0.05)(B, S, D)),
            "w_r": _w((lambda *s: torch.randn(*s, dtype=torch.bfloat16) * 1.6)(D)),
            "w_gate": _w((lambda *s: torch.randn(*s, dtype=torch.bfloat16) * 0.1)(D, F)),
            "w_up": _w((lambda *s: torch.randn(*s, dtype=torch.bfloat16) * 0.1)(D, F)),
            "w_down": _w((lambda *s: torch.randn(*s, dtype=torch.bfloat16) * 0.1)(F, D)),
        }
    _B_in = _B_setup()

    return {
        "A_x": _A_in["x"],
        "A_wq": _A_in["wq"],
        "A_wk": _A_in["wk"],
        "A_wv": _A_in["wv"],
        "A_wo": _A_in["wo"],
        "B_x": _B_in["x"],
        "B_w_r": _B_in["w_r"],
        "B_w_gate": _B_in["w_gate"],
        "B_w_up": _B_in["w_up"],
        "B_w_down": _B_in["w_down"],
    }

def reference(A_x, A_wq, A_wk, A_wv, A_wo, B_x, B_w_r, B_w_gate, B_w_up, B_w_down):
    def _A_ref(x, wq, wk, wv, wo):
        B, S, D = x.shape
        H = 32
        Dh = D // H
        q = (lambda x, *d: x.permute(*d))((x @ wq).reshape(B, S, H, Dh), 0, 2, 1, 3)
        k = x @ wk
        v = x @ wv
        scores = torch.einsum("bhqd,bkd->bhqk", q, k) * (Dh ** -0.5)
        p = _softmax(scores, axis=-1)
        o = torch.einsum("bhqk,bkd->bhqd", p, v)
        return (lambda x, *d: x.permute(*d))(o, 0, 2, 1, 3).reshape(B, S, H * Dh) @ wo

    def _B_ref(x, w_r, w_gate, w_up, w_down):
        B, S, D = x.shape
        C = 2
        r = x @ w_r
        idx = torch.argsort(-r, axis=-1)[:, :C]
        idx_e = (lambda x, n, axis: torch.repeat_interleave(x, n, dim=axis))(idx.reshape(B, C, 1), D, axis=2)
        x_sel = (lambda x, i, axis: torch.take_along_dim(x, i, dim=axis))(x, idx_e, axis=1)
        g = _silu(x_sel @ w_gate)
        u = x_sel @ w_up
        y = (g * u) @ w_down
        r_sel = _sigmoid((lambda x, i, axis: torch.take_along_dim(x, i, dim=axis))(r, idx, axis=1))
        y = y * r_sel.reshape(B, C, 1)
        b_off = (torch.arange(B) * S).reshape(B, 1)
        flat_idx = (idx + b_off).reshape(B * C)
        zero = (x * 0.0).reshape(B * S, D)
        upd = _scatter_add(zero, flat_idx, y.reshape(B * C, D), axis=0)
        return x + upd.reshape(B, S, D)

    x = A_x
    xn = _layernorm(x, axis=-1)
    s = _A_ref(xn, A_wq, A_wk, A_wv, A_wo)
    g = _silu(_B_ref(xn, B_w_r, B_w_gate, B_w_up, B_w_down))
    return x + s * g
```

```cpp
#include <hip/hip_runtime.h>
#include <hip/hip_bf16.h>
#include <stdint.h>

typedef __attribute__((ext_vector_type(2))) __bf16 bf16x2;
typedef __attribute__((ext_vector_type(4))) __bf16 bf16x4;
typedef __attribute__((ext_vector_type(8))) __bf16 bf16x8;
typedef __attribute__((ext_vector_type(16))) __bf16 bf16x16;
typedef __attribute__((ext_vector_type(4))) short s16x4;
typedef __attribute__((ext_vector_type(8))) short s16x8;
typedef __attribute__((ext_vector_type(2))) float f32x2;
typedef __attribute__((ext_vector_type(4))) float f32x4;
typedef __attribute__((ext_vector_type(8))) float f32x8;
typedef __attribute__((ext_vector_type(16))) float f32x16;
typedef __attribute__((ext_vector_type(2))) unsigned u32x2;
typedef __attribute__((ext_vector_type(4))) unsigned u32x4;
typedef unsigned short u16;
#define LAS __attribute__((address_space(3)))
#define DI __device__ __forceinline__

constexpr int S_ = 2048, D_ = 512, F_ = 1792;
constexpr int NU = 224;
constexpr int VGRP = 544;

constexpr size_t WS_Q = 0;
constexpr size_t WS_O = WS_Q + (size_t)S_ * D_ * 2;
constexpr size_t WS_XN = WS_O + (size_t)S_ * D_ * 2;
constexpr size_t WS_K = WS_XN + (size_t)S_ * D_ * 2;
constexpr size_t WS_V = WS_K + (size_t)S_ * 16 * 2;
constexpr size_t WS_STAT = WS_V + 128 * VGRP;
constexpr size_t WS_QN = WS_STAT + (size_t)S_ * 8;
constexpr size_t WS_KN = WS_QN + 256 * 4 * 4;
constexpr size_t WS_CAND = WS_KN + 256 * 4;
constexpr size_t WS_YP = WS_CAND + 256 * 2 * 4;
constexpr size_t WS_END = WS_YP + (size_t)NU * 2 * D_ * 4;

DI float bf_lo(unsigned u) { return __uint_as_float(u << 16); }
DI float bf_hi(unsigned u) { return __uint_as_float(u & 0xffff0000u); }
DI unsigned pack_bf(float a, float b) { f32x2 f = {a, b}; return __builtin_bit_cast(unsigned, __builtin_convertvector(f, bf16x2)); }
DI float rbf(float a) { return (float)(__bf16)a; }
DI float dot2(unsigned a, unsigned b, float c) { return __builtin_amdgcn_fdot2_f32_bf16(__builtin_bit_cast(bf16x2, a), __builtin_bit_cast(bf16x2, b), c, false); }
DI void glds16(const void* g, void* lds_wave_base) { __builtin_amdgcn_global_load_lds((const unsigned*)g, (LAS unsigned*)lds_wave_base, 16, 0, 0); }
DI s16x4 tr_read(const char* p) { return __builtin_amdgcn_ds_read_tr16_b64_v4i16((LAS s16x4*)p); }
DI bf16x8 tr_frag(const char* p0, const char* p1) {
  s16x4 a = tr_read(p0), b = tr_read(p1);
  s16x8 v = __builtin_shufflevector(a, b, 0, 1, 2, 3, 4, 5, 6, 7);
  return __builtin_bit_cast(bf16x8, v);
}
template <int CTRL> DI float dpp_f(float v) { return __int_as_float(__builtin_amdgcn_update_dpp(0, __float_as_int(v), CTRL, 0xf, 0xf, true)); }
template <int CTRL> DI unsigned dpp_u(unsigned v) { return (unsigned)__builtin_amdgcn_update_dpp(0, (int)v, CTRL, 0xf, 0xf, true); }
DI float sum8(float v) {
  v += dpp_f<0xB1>(v);
  v += dpp_f<0x4E>(v);
  v += dpp_f<0x141>(v);
  return v;
}
DI float xor16f(float v) { return __shfl_xor(v, 16); }
DI float xor32f(float v) { return __shfl_xor(v, 32); }
DI float wave_maxf(float v) {
  v = fmaxf(v, dpp_f<0xB1>(v)); v = fmaxf(v, dpp_f<0x4E>(v)); v = fmaxf(v, dpp_f<0x141>(v)); v = fmaxf(v, dpp_f<0x140>(v));
  v = fmaxf(v, xor16f(v)); v = fmaxf(v, xor32f(v));
  return v;
}
DI unsigned wave_maxu(unsigned v) {
  v = max(v, dpp_u<0xB1>(v)); v = max(v, dpp_u<0x4E>(v)); v = max(v, dpp_u<0x141>(v)); v = max(v, dpp_u<0x140>(v));
  v = max(v, (unsigned)__shfl_xor((int)v, 16)); v = max(v, (unsigned)__shfl_xor((int)v, 32));
  return v;
}
DI float silu_f(float v) { return v * __builtin_amdgcn_rcpf(1.0f + __builtin_amdgcn_exp2f(-1.4426950408889634f * v)); }
DI float sigm_f(float v) { return __builtin_amdgcn_rcpf(1.0f + __builtin_amdgcn_exp2f(-1.4426950408889634f * v)); }

DI unsigned make_key(float r_bf, int token) {
  unsigned b = __float_as_uint(r_bf) >> 16;
  b = (b & 0x8000u) ? (~b & 0xffffu) : (b | 0x8000u);
  return (b << 16) | (unsigned)(2047 - token);
}
DI float key_val(unsigned key) {
  unsigned b = key >> 16;
  b = (b & 0x8000u) ? (b & 0x7fffu) : (~b & 0xffffu);
  return __uint_as_float(b << 16);
}
DI int key_tok(unsigned key) { return 2047 - (int)(key & 0xffffu); }

DI void top2(const unsigned* cand, int lane, unsigned& k1, unsigned& k2) {
  u32x4 a = *(const u32x4*)(cand + lane * 8), b = *(const u32x4*)(cand + lane * 8 + 4);
  unsigned m1 = max(a[0], a[1]), m2 = min(a[0], a[1]);
#define UPD(v) { unsigned t_ = min(m1, (v)); m1 = max(m1, (v)); m2 = max(m2, t_); }
  UPD(a[2]) UPD(a[3]) UPD(b[0]) UPD(b[1]) UPD(b[2]) UPD(b[3])
#undef UPD
  k1 = wave_maxu(m1);
  k2 = wave_maxu(m1 == k1 ? m2 : m1);
}

__global__ __launch_bounds__(512, 2) void k1_proj(const u16* __restrict__ x, const u16* __restrict__ wq, const u16* __restrict__ wk,
                                                  const u16* __restrict__ wv, const u16* __restrict__ wr, char* __restrict__ ws) {
  __shared__ __attribute__((aligned(16))) char smem[163840];
  char* XI = smem;
  char* WI = smem + 65536;
  char* KI = smem + 131072;
  const int tid = threadIdx.x, lane = tid & 63;
  const int w = __builtin_amdgcn_readfirstlane(tid >> 6);
  const int b = blockIdx.x, hg = b & 7, tb = b >> 3;
  const int rr = lane >> 3, c = lane & 7;

  u32x4 wrv[8];
  if (w == 6) {
    const u16* p = wr + c * 8;
#pragma unroll
    for (int i = 0; i < 8; ++i) asm volatile("global_load_dwordx4 %0, %1, off offset:%2" : "=v"(wrv[i]) : "v"(p), "i"(i * 128) : "memory");
  }
  u32x4 xv[8];
  {
    const u16* p = x + (size_t)(tb * 64 + w * 8 + rr) * D_ + c * 8;
#pragma unroll
    for (int i = 0; i < 8; ++i) asm volatile("global_load_dwordx4 %0, %1, off offset:%2" : "=v"(xv[i]) : "v"(p), "i"(i * 128) : "memory");
  }
#pragma unroll
  for (int i = 0; i < 4; ++i) {
    const int pc = w * 4 + i;
    const u16* src = (pc < 16 ? wk + pc * 512 : wv + (pc - 16) * 512) + lane * 8;
    glds16(src, KI + pc * 1024);
  }
#pragma unroll
  for (int i = 0; i < 8; ++i) {
    const int pc = i * 8 + w, row = pc * 8 + rr;
    const int sc = c ^ (((row >> 1) & 1) << 2);
    glds16(wq + (size_t)row * D_ + hg * 64 + sc * 8, WI + pc * 1024);
  }
  asm volatile("s_waitcnt vmcnt(12)" : "+v"(xv[0]), "+v"(xv[1]), "+v"(xv[2]), "+v"(xv[3]), "+v"(xv[4]), "+v"(xv[5]), "+v"(xv[6]), "+v"(xv[7]) :: "memory");

  float s = 0.f;
#pragma unroll
  for (int i = 0; i < 8; ++i)
#pragma unroll
    for (int j = 0; j < 4; ++j) s = dot2(xv[i][j], 0x3f803f80u, s);
  const float mean = sum8(s) * (1.0f / 512.0f);
  float v = 0.f;
#pragma unroll
  for (int i = 0; i < 8; ++i)
#pragma unroll
    for (int j = 0; j < 4; ++j) {
      const float a = bf_lo(xv[i][j]) - mean, bb = bf_hi(xv[i][j]) - mean;
      v = fmaf(a, a, v); v = fmaf(bb, bb, v);
    }
  const float var = rbf(sum8(v) * (1.0f / 512.0f));
  const float mb = rbf(mean);
  const float sd = rbf(__builtin_sqrtf(rbf(var + 1e-5f)));
  const float inv = 1.0f / sd;
  const int lrow = w * 8 + rr;
#pragma unroll
  for (int i = 0; i < 8; ++i) {
    u32x4 o;
#pragma unroll
    for (int j = 0; j < 4; ++j) o[j] = pack_bf(rbf(bf_lo(xv[i][j]) - mb) * inv, rbf(bf_hi(xv[i][j]) - mb) * inv);
    *(u32x4*)(XI + lrow * 1024 + (((i * 8 + c) ^ (lrow & 15)) << 4)) = o;
  }
  if (w == hg && c == 0) *(f32x2*)(ws + WS_STAT + (size_t)(tb * 64 + lrow) * 8) = f32x2{mb, sd};
  asm volatile("s_waitcnt vmcnt(0)" ::: "memory");
  __syncthreads();

  const int h = lane >> 5, l31 = lane & 31;
  if (w < 4) {
    const int nt = w & 1, tt = w >> 1;
    const int blk = (lane >> 4) & 1, q = (lane & 15) >> 2, p = lane & 3;
    const int tok = tt * 32 + l31;
    const char* xb = XI + tok * 1024;
    const char* wb = WI + (8 * h + q) * 128 + (((4 * nt + 2 * blk + (p >> 1)) ^ (((q >> 1) & 1) << 2)) << 4) + 8 * (p & 1);
    f32x16 acc = {};
#pragma unroll 8
    for (int ks = 0; ks < 32; ++ks) {
      bf16x8 a = tr_frag(wb + ks * 2048, wb + ks * 2048 + 512);
      bf16x8 bx = *(const bf16x8*)(xb + (((2 * ks + h) ^ (tok & 15)) << 4));
      acc = __builtin_amdgcn_mfma_f32_32x32x16_bf16(a, bx, acc, 0, 0, 0);
    }
    const float cs = 0.25f * 1.4426950408889634f;
    float n0 = 0.f, n1 = 0.f;
#pragma unroll
    for (int r = 0; r < 8; ++r) { acc[r] *= cs; acc[r + 8] *= cs; n0 = fmaf(acc[r], acc[r], n0); n1 = fmaf(acc[r + 8], acc[r + 8], n1); }
    n0 += xor32f(n0); n1 += xor32f(n1);
    const float nm = wave_maxf(fmaxf(n0, n1));
    if (lane == 0) *(float*)(ws + WS_QN + (size_t)(b * 4 + w) * 4) = nm;
    u16* qrow = (u16*)(ws + WS_Q) + (size_t)(tb * 64 + tok) * D_ + hg * 64 + nt * 32;
#pragma unroll
    for (int g = 0; g < 4; g += 2) {
      unsigned a0 = pack_bf(acc[4 * g], acc[4 * g + 1]), a1 = pack_bf(acc[4 * g + 2], acc[4 * g + 3]);
      unsigned b0 = pack_bf(acc[4 * g + 4], acc[4 * g + 5]), b1 = pack_bf(acc[4 * g + 6], acc[4 * g + 7]);
      auto r0 = __builtin_amdgcn_permlane32_swap(a0, b0, false, false);
      auto r1 = __builtin_amdgcn_permlane32_swap(a1, b1, false, false);
      *(u32x4*)(qrow + 8 * g + 8 * h) = u32x4{r0[0], r1[0], r0[1], r1[1]};
    }
  } else if (w < 6) {
    const int g = lane >> 4, q = (lane & 15) >> 2, p = lane & 3;
    const int ltok = hg * 8 + (lane & 7);
    const char* xb = XI + ltok * 1024;
    const char* wb = KI + (w - 4) * 16384 + (8 * g + q) * 32 + p * 8;
    f32x4 acc = {};
#pragma unroll 8
    for (int ks = 0; ks < 16; ++ks) {
      bf16x8 a = tr_frag(wb + ks * 1024, wb + ks * 1024 + 128);
      bf16x8 bx = *(const bf16x8*)(xb + (((4 * ks + g) ^ (ltok & 15)) << 4));
      if (w == 4) acc = __builtin_amdgcn_mfma_f32_16x16x32_bf16(a, bx, acc, 0, 0, 0);
      else        acc = __builtin_amdgcn_mfma_f32_16x16x32_bf16(bx, a, acc, 0, 0, 0);
    }
    const int tok0 = tb * 64 + hg * 8;
    const u32x2 pk = {pack_bf(acc[0], acc[1]), pack_bf(acc[2], acc[3])};
    if (w == 4) {
      float n = acc[0] * acc[0] + acc[1] * acc[1] + acc[2] * acc[2] + acc[3] * acc[3];
      n += xor16f(n); n += xor32f(n);
      n = wave_maxf(n);
      if (lane == 0) *(float*)(ws + WS_KN + (size_t)b * 4) = n;
      if ((lane & 15) < 8) *(u32x2*)(ws + WS_K + (size_t)(tok0 + (lane & 15)) * 32 + g * 8) = pk;
    } else {
      char* vb = ws + WS_V + (size_t)(tok0 >> 4) * VGRP + ((tok0 >> 3) & 1) * 16;
      if (g < 2) *(u32x2*)(vb + (lane & 15) * 32 + g * 8) = pk;
      if (lane == 32) *(u32x4*)(vb + 16 * 32) = u32x4{0x3f803f80u, 0x3f803f80u, 0x3f803f80u, 0x3f803f80u};
    }
  } else if (w == 6) {
    asm volatile("" : "+v"(wrv[0]), "+v"(wrv[1]), "+v"(wrv[2]), "+v"(wrv[3]), "+v"(wrv[4]), "+v"(wrv[5]), "+v"(wrv[6]), "+v"(wrv[7]));
    const int ltok = hg * 8 + rr, tok = tb * 64 + ltok;
    float r = 0.f;
#pragma unroll
    for (int i = 0; i < 8; ++i) {
      u32x4 xn = *(const u32x4*)(XI + ltok * 1024 + (((i * 8 + c) ^ (ltok & 15)) << 4));
      *(u32x4*)(ws + WS_XN + (size_t)tok * 1024 + (i * 8 + c) * 16) = xn;
#pragma unroll
      for (int j = 0; j < 4; ++j) r = dot2(xn[j], wrv[i][j], r);
    }
    r = rbf(sum8(r));
    const unsigned key = make_key(r, tok);
    const unsigned k1 = wave_maxu(key);
    const unsigned k2 = wave_maxu(key == k1 ? 0u : key);
    if (lane == 0) *(u32x2*)(ws + WS_CAND + (size_t)b * 8) = u32x2{k1, k2};
  }
}

constexpr int FFN_LDS = 16384 + 8 * 1088 + 2048;
DI void ffn_issue(char* L, const u16* wg, const u16* wu, const u16* wd, int u, int lane) {
#pragma unroll
  for (int j = 0; j < 16; ++j) {
    const int k = j * 32 + (lane >> 1);
    glds16(((lane & 1) ? wu : wg) + (size_t)k * F_ + u * 8, L + j * 1024);
  }
#pragma unroll
  for (int f = 0; f < 8; ++f) glds16(wd + (size_t)(u * 8 + f) * D_ + lane * 8, L + 16384 + f * 1088);
}
DI void ffn_compute(char* L, char* ws, int u, int lane) {
  char* XS = L + 16384 + 8 * 1088;
  unsigned k1, k2;
  top2((const unsigned*)(ws + WS_CAND), lane, k1, k2);
  const int t0 = __builtin_amdgcn_readfirstlane(key_tok(k1)), t1 = __builtin_amdgcn_readfirstlane(key_tok(k2));
  glds16(ws + WS_XN + (size_t)t0 * 1024 + lane * 16, XS);
  glds16(ws + WS_XN + (size_t)t1 * 1024 + lane * 16, XS + 1024);
  asm volatile("s_waitcnt vmcnt(0)" ::: "memory");
  __builtin_amdgcn_wave_barrier();
  const int g = lane >> 4, q = (lane & 15) >> 2, p = lane & 3;
  const char* wb = L + (8 * g + q) * 32 + p * 8;
  const char* xb = XS + (lane & 1) * 1024 + g * 16;
  f32x4 acc = {};
#pragma unroll 8
  for (int ks = 0; ks < 16; ++ks) {
    bf16x8 a = tr_frag(wb + ks * 1024, wb + ks * 1024 + 128);
    bf16x8 bx = *(const bf16x8*)(xb + ks * 64);
    acc = __builtin_amdgcn_mfma_f32_16x16x32_bf16(a, bx, acc, 0, 0, 0);
  }
  float hv[4];
#pragma unroll
  for (int r = 0; r < 4; ++r) {
    const float up = xor32f(acc[r]);
    hv[r] = rbf(rbf(silu_f(rbf(acc[r]))) * rbf(up));
  }
  unsigned h0 = pack_bf(hv[0], hv[1]), h1 = pack_bf(hv[2], hv[3]);
  unsigned h2 = (unsigned)__shfl_xor((int)h0, 16), h3 = (unsigned)__shfl_xor((int)h1, 16);
  u32x4 ha = (lane < 2) ? u32x4{h0, h1, h2, h3} : u32x4{0u, 0u, 0u, 0u};
  const bf16x8 A = __builtin_bit_cast(bf16x8, ha);
  const int blk = (lane >> 4) & 1;
  const char* db = L + 16384 + q * 1088 + (2 * blk + (p >> 1)) * 16 + 8 * (p & 1);
  float* yp = (float*)(ws + WS_YP) + (size_t)u * 1024;
#pragma unroll 4
  for (int ct = 0; ct < 16; ++ct) {
    bf16x8 bw = tr_frag(db + ct * 64, db + ct * 64 + 4 * 1088);
    f32x16 z = {};
    z = __builtin_amdgcn_mfma_f32_32x32x16_bf16(A, bw, z, 0, 0, 0);
    if (lane < 32) { yp[ct * 32 + lane] = z[0]; yp[512 + ct * 32 + lane] = z[1]; }
  }
}
__global__ __launch_bounds__(64) void k_ffn(const u16* __restrict__ wg, const u16* __restrict__ wu, const u16* __restrict__ wd, char* __restrict__ ws) {
  __shared__ __attribute__((aligned(16))) char L[FFN_LDS];
  const int b = blockIdx.x, u = (b & 7) * 28 + (b >> 3), lane = threadIdx.x;
  ffn_issue(L, wg, wu, wd, u, lane);
  ffn_compute(L, ws, u, lane);
}

constexpr int PSLOT = 2304;
#define QK(KF, QF, C) __builtin_amdgcn_mfma_f32_32x32x16_bf16((KF), (QF), (C), 0, 0, 0)
DI f32x16 pv2(const bf16x8& va, const bf16x8& vb, const f32x16& p, f32x16 o, int idx) {
  f32x8 lo = __builtin_shufflevector(p, p, 0, 1, 2, 3, 4, 5, 6, 7), hi = __builtin_shufflevector(p, p, 8, 9, 10, 11, 12, 13, 14, 15);
  o = __builtin_amdgcn_smfmac_f32_32x32x32_bf16(va, __builtin_bit_cast(bf16x16, lo), o, idx, 0, 0);
  o = __builtin_amdgcn_smfmac_f32_32x32x32_bf16(vb, __builtin_bit_cast(bf16x16, hi), o, idx, 0, 0);
  return o;
}
DI f32x16 exp16(f32x16 s) {
#pragma unroll
  for (int i = 0; i < 16; ++i) s[i] = __builtin_amdgcn_exp2f(s[i]);
  return s;
}
DI f32x16 splat16(float v) { f32x16 r;
#pragma unroll
  for (int i = 0; i < 16; ++i) r[i] = v;
  return r; }

__global__ __launch_bounds__(512, 2) void k2_attn(char* __restrict__ ws) {
  __shared__ __attribute__((aligned(16))) char smem[8 * 7 * PSLOT + 1024];
  float* rowmax = (float*)(smem + 8 * 7 * PSLOT);
  const int tid = threadIdx.x, lane = tid & 63;
  const int w = __builtin_amdgcn_readfirstlane(tid >> 6);
  const int b = blockIdx.x, hg = b & 7, tb = b >> 3;
  const int h = lane >> 5, l31 = lane & 31;

  const char* qbase = ws + WS_Q + (size_t)(tb * 64 + (l31 >> 2)) * 1024 + hg * 128 + (l31 & 3) * 32 + h * 16;
  bf16x8 qf = *(const bf16x8*)(qbase + (size_t)((w + 1) & 7) * 8192);
  bf16x8 kf[8], va[8], vb[8];
  {
    const char* kb = ws + WS_K + (size_t)(w * 256 + l31) * 32 + h * 16;
    const char* vp = ws + WS_V + (size_t)(w * 16) * VGRP + (min(l31, 16) * 2 + h) * 16;
#pragma unroll
    for (int t = 0; t < 8; ++t) {
      kf[t] = *(const bf16x8*)(kb + t * 1024);
      va[t] = *(const bf16x8*)(vp + (2 * t) * VGRP);
      vb[t] = *(const bf16x8*)(vp + (2 * t + 1) * VGRP);
    }
  }
  const f32x4 qn = *(const f32x4*)(ws + WS_QN + (size_t)b * 16);
  const f32x4 kn = *(const f32x4*)(ws + WS_KN + (size_t)lane * 16);
  const float kmax = wave_maxf(fmaxf(fmaxf(kn[0], kn[1]), fmaxf(kn[2], kn[3])));
  const float qmax = fmaxf(fmaxf(qn[0], qn[1]), fmaxf(qn[2], qn[3]));
  const float bound = __builtin_sqrtf(qmax * kmax) * 1.02f + 0.01f;
  const bool fast = __builtin_amdgcn_readfirstlane(bound <= 48.0f ? 1 : 0) != 0;
  const int idx = (int)0xDDDDDDDDu;
  const bf16x8 zf = {};
  const f32x16 z16 = {};
  f32x16 o;

  if (fast) {
    const f32x16 cc = splat16(-bound);
    f32x16 s0 = QK(kf[0], qf, cc), s1;
    for (int i = 0; i < 8; ++i) {
      const bf16x8 qn2 = *(const bf16x8*)(qbase + (size_t)((w + 2 + i) & 7) * 8192);
      o = QK(zf, zf, z16);
#pragma unroll
      for (int t = 0; t < 8; t += 2) {
        s1 = QK(kf[t + 1], qf, cc);
        s0 = exp16(s0);
        o = pv2(va[t], vb[t], s0, o, idx);
        if (t < 6) s0 = QK(kf[t + 2], qf, cc); else s0 = QK(kf[0], qn2, cc);
        s1 = exp16(s1);
        o = pv2(va[t + 1], vb[t + 1], s1, o, idx);
      }
      qf = qn2;
      if (i < 7) {
        char* sl = smem + (((w + 1 + i) & 7) * 7 + (6 - i)) * PSLOT;
        *(f32x4*)(sl + lane * 16) = f32x4{o[0], o[1], o[2], o[3]};
        *(f32x4*)(sl + 1024 + lane * 16) = f32x4{o[4], o[5], o[6], o[7]};
        *(float*)(sl + 2048 + lane * 4) = o[8];
      }
    }
  } else {
    float* part = (float*)smem;
    for (int j = 0; j < 8; ++j) {
      const bf16x8 q2 = *(const bf16x8*)(qbase + (size_t)j * 8192);
      float m = -3.0e38f;
#pragma unroll
      for (int t = 0; t < 8; ++t) {
        const f32x16 s = QK(kf[t], q2, z16);
#pragma unroll
        for (int r = 0; r < 16; ++r) m = fmaxf(m, s[r]);
      }
      m = fmaxf(m, xor32f(m));
      if (lane < 32) part[w * 256 + j * 32 + lane] = m;
    }
    __syncthreads();
    if (tid < 256) {
      float m = part[tid];
#pragma unroll
      for (int ww = 1; ww < 8; ++ww) m = fmaxf(m, part[ww * 256 + tid]);
      rowmax[tid] = m;
    }
    __syncthreads();
    for (int i = 0; i < 8; ++i) {
      const int j = (w + 1 + i) & 7;
      const bf16x8 q2 = *(const bf16x8*)(qbase + (size_t)j * 8192);
      const f32x16 cc = splat16(-rowmax[j * 32 + l31]);
      o = z16;
#pragma unroll
      for (int t = 0; t < 8; ++t) {
        f32x16 s = exp16(QK(kf[t], q2, cc));
        o = pv2(va[t], vb[t], s, o, idx);
      }
      if (i < 7) {
        char* sl = smem + (j * 7 + (6 - i)) * PSLOT;
        *(f32x4*)(sl + lane * 16) = f32x4{o[0], o[1], o[2], o[3]};
        *(f32x4*)(sl + 1024 + lane * 16) = f32x4{o[4], o[5], o[6], o[7]};
        *(float*)(sl + 2048 + lane * 4) = o[8];
      }
    }
  }
  __syncthreads();
  float a[9];
#pragma unroll
  for (int r = 0; r < 9; ++r) a[r] = o[r];
#pragma unroll
  for (int sidx = 0; sidx < 7; ++sidx) {
    const char* sl = smem + (w * 7 + sidx) * PSLOT;
    const f32x4 p0 = *(const f32x4*)(sl + lane * 16), p1 = *(const f32x4*)(sl + 1024 + lane * 16);
    const float p2 = *(const float*)(sl + 2048 + lane * 4);
#pragma unroll
    for (int r = 0; r < 4; ++r) { a[r] += p0[r]; a[r + 4] += p1[r]; }
    a[8] += p2;
  }
  const float inv = 1.0f / a[8];
  unsigned a0 = pack_bf(a[0] * inv, a[1] * inv), a1 = pack_bf(a[2] * inv, a[3] * inv);
  unsigned b0 = pack_bf(a[4] * inv, a[5] * inv), b1 = pack_bf(a[6] * inv, a[7] * inv);
  auto r0 = __builtin_amdgcn_permlane32_swap(a0, b0, false, false);
  auto r1 = __builtin_amdgcn_permlane32_swap(a1, b1, false, false);
  char* op = ws + WS_O + (size_t)(tb * 64 + w * 8 + (l31 >> 2)) * 1024 + hg * 128 + (l31 & 3) * 32 + h * 16;
  *(u32x4*)op = u32x4{r0[0], r1[0], r0[1], r1[1]};
}

__global__ __launch_bounds__(512, 2) void k3_out(const u16* __restrict__ x, const u16* __restrict__ wo, const char* __restrict__ ws, u16* __restrict__ out) {
  __shared__ __attribute__((aligned(16))) char smem[131072 + 4096 + 512];
  char* OI = smem;
  char* WI = smem + 65536;
  float* YR = (float*)(smem + 131072);
  float* YF = (float*)(smem + 131072 + 4096);
  const int tid = threadIdx.x, lane = tid & 63;
  const int w = __builtin_amdgcn_readfirstlane(tid >> 6);
  const int b = blockIdx.x, cg = b & 7, tb = b >> 3;
  const int rr = lane >> 3, c = lane & 7;
#pragma unroll
  for (int i = 0; i < 8; ++i) {
    const int row = i * 8 + w;
    glds16(ws + WS_O + (size_t)(tb * 64 + row) * 1024 + ((lane ^ (row & 15)) << 4), OI + row * 1024);
  }
#pragma unroll
  for (int i = 0; i < 8; ++i) {
    const int pc = i * 8 + w, row = pc * 8 + rr;
    const int sc = c ^ (((row >> 1) & 1) << 2);
    glds16(wo + (size_t)row * D_ + cg * 64 + sc * 8, WI + pc * 1024);
  }
  unsigned k1, k2;
  top2((const unsigned*)(ws + WS_CAND), lane, k1, k2);
  const int t0 = __builtin_amdgcn_readfirstlane(key_tok(k1)), t1 = __builtin_amdgcn_readfirstlane(key_tok(k2));
  const bool hit0 = (t0 >> 6) == tb, hit1 = (t1 >> 6) == tb;
  if (hit0 || hit1) {
    const float* yp = (const float*)(ws + WS_YP) + cg * 64 + lane;
    float y0 = 0.f, y1 = 0.f;
#pragma unroll 7
    for (int i = 0; i < 28; ++i) {
      const int u = w * 28 + i;
      y0 += yp[(size_t)u * 1024];
      y1 += yp[(size_t)u * 1024 + 512];
    }
    YR[(w * 2 + 0) * 64 + lane] = y0;
    YR[(w * 2 + 1) * 64 + lane] = y1;
    __syncthreads();
    if (tid < 128) {
      float y = 0.f;
#pragma unroll
      for (int ww = 0; ww < 8; ++ww) y += YR[(ww * 2 + (tid >> 6)) * 64 + lane];
      const float rs = rbf(sigm_f(key_val(tid < 64 ? k1 : k2)));
      YF[tid] = rbf(rbf(y) * rs);
    }
  }
  asm volatile("s_waitcnt vmcnt(0)" ::: "memory");
  __syncthreads();
  if (w < 4) {
    const int h = lane >> 5, l31 = lane & 31;
    const int nt = w & 1, tt = w >> 1;
    const int blk = (lane >> 4) & 1, q = (lane & 15) >> 2, p = lane & 3;
    const int tok = tt * 32 + l31, gtok = tb * 64 + tok;
    const u16* xrow = x + (size_t)gtok * D_ + cg * 64 + nt * 32 + 4 * h;
    u32x2 xv[4];
#pragma unroll
    for (int g = 0; g < 4; ++g) xv[g] = *(const u32x2*)(xrow + 8 * g);
    const f32x2 st = *(const f32x2*)(ws + WS_STAT + (size_t)gtok * 8);
    const char* ob = OI + tok * 1024;
    const char* wb = WI + (8 * h + q) * 128 + (((4 * nt + 2 * blk + (p >> 1)) ^ (((q >> 1) & 1) << 2)) << 4) + 8 * (p & 1);
    f32x16 acc = {};
#pragma unroll 8
    for (int ks = 0; ks < 32; ++ks) {
      bf16x8 a = tr_frag(wb + ks * 2048, wb + ks * 2048 + 512);
      bf16x8 bo = *(const bf16x8*)(ob + (((2 * ks + h) ^ (tok & 15)) << 4));
      acc = __builtin_amdgcn_mfma_f32_32x32x16_bf16(a, bo, acc, 0, 0, 0);
    }
    const int sel = (gtok == t0) ? 0 : ((gtok == t1) ? 1 : -1);
    unsigned pk[8];
#pragma unroll
    for (int g = 0; g < 4; ++g) {
      float res[4];
#pragma unroll
      for (int j = 0; j < 4; ++j) {
        const unsigned xu = xv[g][j >> 1];
        const float xf = (j & 1) ? bf_hi(xu) : bf_lo(xu);
        float xn = rbf(rbf(xf - st[0]) / st[1]);
        if (sel >= 0) xn = rbf(xn + YF[sel * 64 + nt * 32 + 8 * g + 4 * h + j]);
        const float gg = rbf(silu_f(xn));
        res[j] = xf + rbf(rbf(acc[4 * g + j]) * gg);
      }
      pk[2 * g] = pack_bf(res[0], res[1]); pk[2 * g + 1] = pack_bf(res[2], res[3]);
    }
    u16* orow = out + (size_t)gtok * D_ + cg * 64 + nt * 32;
#pragma unroll
    for (int g = 0; g < 4; g += 2) {
      auto r0 = __builtin_amdgcn_permlane32_swap(pk[2 * g], pk[2 * g + 2], false, false);
      auto r1 = __builtin_amdgcn_permlane32_swap(pk[2 * g + 1], pk[2 * g + 3], false, false);
      *(u32x4*)(orow + 8 * g + 8 * h) = u32x4{r0[0], r1[0], r0[1], r1[1]};
    }
  }
}

extern "C" void kernel_launch(void* const* d_in, const int* in_sizes, int n_in, void* d_out, int out_size, void* d_ws, size_t ws_size,
                              hipStream_t stream) {
  const u16* x = (const u16*)d_in[0];
  const u16* wq = (const u16*)d_in[1];
  const u16* wk = (const u16*)d_in[2];
  const u16* wv = (const u16*)d_in[3];
  const u16* wo = (const u16*)d_in[4];
  const u16* wr = (const u16*)d_in[6];
  const u16* wg = (const u16*)d_in[7];
  const u16* wu = (const u16*)d_in[8];
  const u16* wd = (const u16*)d_in[9];
  char* ws = (char*)d_ws;
  k1_proj<<<256, 512, 0, stream>>>(x, wq, wk, wv, wr, ws);
  k_ffn<<<NU, 64, 0, stream>>>(wg, wu, wd, ws);
  k2_attn<<<256, 512, 0, stream>>>(ws);
  k3_out<<<256, 512, 0, stream>>>(x, wo, ws, (u16*)d_out);
}
```

```cpp
#include <hip/hip_runtime.h>
#include <hip/hip_bf16.h>
#include <stdint.h>

typedef __attribute__((ext_vector_type(2))) __bf16 bf16x2;
typedef __attribute__((ext_vector_type(8))) __bf16 bf16x8;
typedef __attribute__((ext_vector_type(16))) __bf16 bf16x16;
typedef __attribute__((ext_vector_type(4))) short s16x4;
typedef __attribute__((ext_vector_type(8))) short s16x8;
typedef __attribute__((ext_vector_type(2))) float f32x2;
typedef __attribute__((ext_vector_type(4))) float f32x4;
typedef __attribute__((ext_vector_type(8))) float f32x8;
typedef __attribute__((ext_vector_type(16))) float f32x16;
typedef __attribute__((ext_vector_type(2))) unsigned u32x2;
typedef __attribute__((ext_vector_type(4))) unsigned u32x4;
typedef unsigned short u16;
typedef __amdgpu_buffer_rsrc_t rsrc_t;
#define LAS __attribute__((address_space(3)))
#define DI __device__ __forceinline__

constexpr int S_ = 2048, D_ = 512, F_ = 1792;
constexpr int NU = 224;
constexpr int VHALF = 288;
constexpr int SC1 = 16;
constexpr unsigned SPIN_MAX = 1u << 21;

constexpr unsigned WS_EPOCH = 0;
constexpr unsigned WS_KVF = 1024;
constexpr unsigned WS_CAND = WS_KVF + 8 * 256 * 8;
constexpr unsigned WS_YF = WS_CAND + 8 * 512 * 8;
constexpr unsigned WS_OF = WS_YF + 1024;
constexpr unsigned WS_K = WS_OF + 32 * 64 * 4;
constexpr unsigned WS_V = WS_K + S_ * 32;
constexpr unsigned WS_XN = WS_V + 256 * VHALF;
constexpr unsigned WS_O = WS_XN + S_ * D_ * 2;
constexpr unsigned WS_YP = WS_O + S_ * D_ * 2;
constexpr unsigned WS_END = WS_YP + NU * 2 * D_ * 4;

constexpr int L_XI = 65536;
constexpr int L_WI = 0;
constexpr int L_KI = 131072;
constexpr int PSLOT = 2304;
constexpr int L_Q = 8 * 7 * PSLOT;
constexpr int L_QI = 131072;
constexpr int L_CTL = L_QI + 8192;
constexpr int L_GU = L_CTL + 1024;
constexpr int L_DN = L_GU + 16384;
constexpr int L_TOTAL = 163840;
constexpr int C_CNT = 0;
constexpr int C_SEL = 4;
constexpr int C_K1 = 8, C_K2 = 12;
constexpr int C_QMAX = 16;
constexpr int C_R = 32;
constexpr int C_STAT = 64;
constexpr int C_YF = 576;

#define STAMP(kid, i)

DI float bf_lo(unsigned u) { return __uint_as_float(u << 16); }
DI float bf_hi(unsigned u) { return __uint_as_float(u & 0xffff0000u); }
DI unsigned pack_bf(float a, float b) { f32x2 f = {a, b}; return __builtin_bit_cast(unsigned, __builtin_convertvector(f, bf16x2)); }
DI float rbf(float a) { return (float)(__bf16)a; }
DI float dot2(unsigned a, unsigned b, float c) { return __builtin_amdgcn_fdot2_f32_bf16(__builtin_bit_cast(bf16x2, a), __builtin_bit_cast(bf16x2, b), c, false); }
DI void glds16(const void* sbase, unsigned voff, void* lds_wave_base) {
  unsigned keep; const unsigned dst = (unsigned)(uintptr_t)(LAS char*)lds_wave_base;
  asm volatile("s_mov_b32 %0, m0\n\ts_mov_b32 m0, %2\n\ts_nop 0\n\tglobal_load_lds_dwordx4 %1, %3\n\ts_mov_b32 m0, %0"
               : "=&s"(keep) : "v"(voff), "s"(dst), "s"(sbase) : "memory");
}
DI s16x4 tr_read(const char* p) { return __builtin_amdgcn_ds_read_tr16_b64_v4i16((LAS s16x4*)p); }
DI bf16x8 tr_frag(const char* p0, const char* p1) {
  s16x4 a = tr_read(p0), b = tr_read(p1);
  s16x8 v = __builtin_shufflevector(a, b, 0, 1, 2, 3, 4, 5, 6, 7);
  return __builtin_bit_cast(bf16x8, v);
}
template <int CTRL> DI float dpp_f(float v) { return __int_as_float(__builtin_amdgcn_update_dpp(0, __float_as_int(v), CTRL, 0xf, 0xf, true)); }
template <int CTRL> DI unsigned dpp_u(unsigned v) { return (unsigned)__builtin_amdgcn_update_dpp(0, (int)v, CTRL, 0xf, 0xf, true); }
DI float sum8(float v) {
  v += dpp_f<0xB1>(v); v += dpp_f<0x4E>(v); v += dpp_f<0x141>(v);
  return v;
}
DI float add16(float v) { auto r = __builtin_amdgcn_permlane16_swap(__float_as_uint(v), __float_as_uint(v), false, false); return __uint_as_float(r[0]) + __uint_as_float(r[1]); }
DI float add32(float v) { auto r = __builtin_amdgcn_permlane32_swap(__float_as_uint(v), __float_as_uint(v), false, false); return __uint_as_float(r[0]) + __uint_as_float(r[1]); }
DI float max16(float v) { auto r = __builtin_amdgcn_permlane16_swap(__float_as_uint(v), __float_as_uint(v), false, false); return fmaxf(__uint_as_float(r[0]), __uint_as_float(r[1])); }
DI float max32(float v) { auto r = __builtin_amdgcn_permlane32_swap(__float_as_uint(v), __float_as_uint(v), false, false); return fmaxf(__uint_as_float(r[0]), __uint_as_float(r[1])); }
DI float wave_sum(float v) { v = sum8(v); v += dpp_f<0x140>(v); return add32(add16(v)); }
DI float wave_maxf(float v) {
  v = fmaxf(v, dpp_f<0xB1>(v)); v = fmaxf(v, dpp_f<0x4E>(v)); v = fmaxf(v, dpp_f<0x141>(v)); v = fmaxf(v, dpp_f<0x140>(v));
  return max32(max16(v));
}
DI unsigned wave_maxu(unsigned v) {
  v = max(v, dpp_u<0xB1>(v)); v = max(v, dpp_u<0x4E>(v)); v = max(v, dpp_u<0x141>(v)); v = max(v, dpp_u<0x140>(v));
  auto r = __builtin_amdgcn_permlane16_swap(v, v, false, false); v = max(r[0], r[1]);
  auto q = __builtin_amdgcn_permlane32_swap(v, v, false, false); return max(q[0], q[1]);
}
DI float silu_f(float v) { return v * __builtin_amdgcn_rcpf(1.0f + __builtin_amdgcn_exp2f(-1.4426950408889634f * v)); }
DI float sigm_f(float v) { return __builtin_amdgcn_rcpf(1.0f + __builtin_amdgcn_exp2f(-1.4426950408889634f * v)); }

DI unsigned make_key(float r_bf, int token) {
  unsigned b = __float_as_uint(r_bf) >> 16;
  b = (b & 0x8000u) ? (~b & 0xffffu) : (b | 0x8000u);
  return (b << 16) | (unsigned)(2047 - token);
}
DI float key_val(unsigned key) {
  unsigned b = key >> 16;
  b = (b & 0x8000u) ? (b & 0x7fffu) : (~b & 0xffffu);
  return __uint_as_float(b << 16);
}
DI int key_tok(unsigned key) { return 2047 - (int)(key & 0x7ffu); }

DI u32x4 ld128(rsrc_t rs, unsigned off) { return __builtin_amdgcn_raw_buffer_load_b128(rs, (int)off, 0, SC1); }
DI u32x2 ld64(rsrc_t rs, unsigned off) { return __builtin_amdgcn_raw_buffer_load_b64(rs, (int)off, 0, SC1); }
DI unsigned ld32(rsrc_t rs, unsigned off) { return __builtin_amdgcn_raw_buffer_load_b32(rs, (int)off, 0, SC1); }
DI void st128(rsrc_t rs, unsigned off, u32x4 v) { __builtin_amdgcn_raw_buffer_store_b128(v, rs, (int)off, 0, SC1); }
DI void st64(rsrc_t rs, unsigned off, u32x2 v) { __builtin_amdgcn_raw_buffer_store_b64(v, rs, (int)off, 0, SC1); }
DI void st32(rsrc_t rs, unsigned off, unsigned v) { __builtin_amdgcn_raw_buffer_store_b32(v, rs, (int)off, 0, SC1); }
#define DRAIN() asm volatile("s_waitcnt vmcnt(0)" ::: "memory")
#define CBAR() asm volatile("" ::: "memory")
#define RAW_BARRIER() do { asm volatile("s_waitcnt lgkmcnt(0)" ::: "memory"); __builtin_amdgcn_s_barrier(); asm volatile("" ::: "memory"); } while (0)
DI unsigned lds_ld(const char* p) { return __hip_atomic_load((LAS unsigned*)p, __ATOMIC_RELAXED, __HIP_MEMORY_SCOPE_WORKGROUP); }
DI void lds_wait_ge(const char* p, unsigned want) {
  _Pragma("nounroll") for (unsigned s = 0; s < SPIN_MAX; ++s) {
    if ((unsigned)__builtin_amdgcn_readfirstlane(lds_ld(p)) >= want) break;
    __builtin_amdgcn_s_sleep(1);
  }
  CBAR();
}

#define QK(KF, QF, C) __builtin_amdgcn_mfma_f32_32x32x16_bf16((KF), (QF), (C), 0, 0, 0)
DI f32x16 pv_lo(const bf16x8& va, const f32x16& p, f32x16 o, int idx) {
  f32x8 lo = __builtin_shufflevector(p, p, 0, 1, 2, 3, 4, 5, 6, 7);
  return __builtin_amdgcn_smfmac_f32_32x32x32_bf16(va, __builtin_bit_cast(bf16x16, lo), o, idx, 0, 0);
}
DI f32x16 pv_hi(const bf16x8& vb, const f32x16& p, f32x16 o, int idx) {
  f32x8 hi = __builtin_shufflevector(p, p, 8, 9, 10, 11, 12, 13, 14, 15);
  return __builtin_amdgcn_smfmac_f32_32x32x32_bf16(vb, __builtin_bit_cast(bf16x16, hi), o, idx, 0, 0);
}
DI f32x16 exp16(f32x16 s) {
#pragma unroll
  for (int i = 0; i < 16; ++i) s[i] = __builtin_amdgcn_exp2f(s[i]);
  return s;
}
DI f32x16 splat16(float v) { f32x16 r;
#pragma unroll
  for (int i = 0; i < 16; ++i) r[i] = v;
  return r; }

#define EX(b, i) "v_exp_f32 v[" #b "+" #i "], v[" #b "+" #i "]\n\t"
#define SM(V, b, o) "v_smfmac_f32_32x32x32_bf16 v[240:255], %[" #V "], v[" #b "+" #o ":" #b "+" #o "+7], %[idx]\n\t"
#define ZQ(b, K, Q, n) "s_waitcnt vmcnt(" #n ")\n\tv_mfma_f32_32x32x16_bf16 v[" #b ":" #b "+15], %[" #K "], %[" #Q "], v[192:207]\n\t"
#define TILE(cur, oth, FIRST, K, Q, n, VA) \
  EX(cur, 0) EX(cur, 1) FIRST EX(cur, 2) EX(cur, 3) EX(cur, 4) ZQ(oth, K, Q, n) EX(cur, 5) EX(cur, 6) EX(cur, 7) EX(cur, 8) EX(cur, 9) \
  SM(VA, cur, 0) EX(cur, 10) EX(cur, 11) EX(cur, 12) EX(cur, 13) EX(cur, 14) EX(cur, 15)
#define PASS_ASM \
  TILE(208, 224, "v_mfma_f32_32x32x16_bf16 v[240:255], %[z], %[q], 0\n\t", k1, q, 28, a0) \
  TILE(224, 208, SM(b0, 208, 8), k2, q, 25, a1) \
  TILE(208, 224, SM(b1, 224, 8), k3, q, 22, a2) \
  TILE(224, 208, SM(b2, 208, 8), k4, q, 19, a3) \
  TILE(208, 224, SM(b3, 224, 8), k5, q, 16, a4) \
  TILE(224, 208, SM(b4, 208, 8), k6, q, 13, a5) \
  TILE(208, 224, SM(b5, 224, 8), k7, q, 10, a6) \
  TILE(224, 208, SM(b6, 208, 8), k0, qn, 8, a7) \
  "s_nop 1\n\t" SM(b7, 224, 8) "s_nop 11"
#define SB() __builtin_amdgcn_sched_barrier(0)

__global__ __launch_bounds__(512, 2) void fused_block(const u16* __restrict__ x, const u16* __restrict__ wq, const u16* __restrict__ wk,
                                                      const u16* __restrict__ wv, const u16* __restrict__ wo, const u16* __restrict__ wr,
                                                      const u16* __restrict__ wg, const u16* __restrict__ wu, const u16* __restrict__ wd,
                                                      char* ws, u16* __restrict__ out) {
  __shared__ __attribute__((aligned(16))) char smem[L_TOTAL];
  char* const XI = smem + L_XI;
  char* const WI = smem + L_WI;
  char* const KI = smem + L_KI;
  char* const CTL = smem + L_CTL;
  const int tid = threadIdx.x, lane = tid & 63;
  const int w = __builtin_amdgcn_readfirstlane(tid >> 6);
  const int b = blockIdx.x, g8 = b & 7, tb = g8 + 8 * ((b >> 3) & 3), hg = b >> 5;
  const int rr = lane >> 3, c = lane & 7;
  const int h = lane >> 5, l31 = lane & 31;
  const int tok0 = tb * 64 + hg * 8;
  const int pid = tok0 >> 3;
  const rsrc_t rs = __builtin_amdgcn_make_buffer_rsrc(ws, 0, (int)WS_END, 0x00020000);
  STAMP(0, 0);

  unsigned tag;
  asm volatile("s_load_dword %0, %1, 0x0" : "=s"(tag) : "s"(ws) : "memory");
  const bool kvw = (w == hg);
  u32x4 xo, wrv, xv[8];
  asm volatile("global_load_dwordx4 %0, %1, %2" : "=v"(xo) : "v"(lane * 16), "s"(x + (size_t)(tok0 + w) * D_) : "memory");
  asm volatile("global_load_dwordx4 %0, %1, %2" : "=v"(wrv) : "v"(lane * 16), "s"(wr) : "memory");
#pragma unroll
  for (int i = 0; i < 4; ++i) {
    const int pc = w * 4 + i;
    glds16(pc < 16 ? wk + pc * 512 : wv + (pc - 16) * 512, lane * 16, KI + pc * 1024);
  }
  const unsigned wq_off = rr * 1024 + ((c ^ (((rr >> 1) & 1) << 2)) << 4);
  if (!kvw) {
    const u16* p = x + (size_t)(tb * 64 + w * 8) * D_;
#pragma unroll
    for (int i = 0; i < 8; ++i) asm volatile("global_load_dwordx4 %0, %1, %2 offset:%3" : "=v"(xv[i]) : "v"(rr * 1024 + c * 16), "s"(p), "i"(i * 128) : "memory");
    asm volatile("s_waitcnt vmcnt(12)" ::: "memory");
  } else {
    __builtin_amdgcn_s_setprio(3);
    asm volatile("s_waitcnt vmcnt(4)" ::: "memory");
  }
  asm volatile("" : "+v"(xo), "+v"(wrv));
  STAMP(0, 1);
  unsigned rkey;
  float own_m, own_s;
  {
    float s = 0.f;
#pragma unroll
    for (int j = 0; j < 4; ++j) s = dot2(xo[j], 0x3f803f80u, s);
    const float mean = wave_sum(s) * (1.0f / 512.0f);
    float v = 0.f;
#pragma unroll
    for (int j = 0; j < 4; ++j) { const float a = bf_lo(xo[j]) - mean, bb = bf_hi(xo[j]) - mean; v = fmaf(a, a, v); v = fmaf(bb, bb, v); }
    const float var = rbf(wave_sum(v) * (1.0f / 512.0f));
    own_m = rbf(mean); own_s = rbf(__builtin_amdgcn_sqrtf(rbf(var + 1e-5f)));
    const float inv = __builtin_amdgcn_rcpf(own_s);
    u32x4 o; float r = 0.f;
#pragma unroll
    for (int j = 0; j < 4; ++j) { o[j] = pack_bf(rbf(bf_lo(xo[j]) - own_m) * inv, rbf(bf_hi(xo[j]) - own_m) * inv); r = dot2(o[j], wrv[j], r); }
    const int lrow = hg * 8 + w;
    *(u32x4*)(XI + lrow * 1024 + ((lane ^ (lrow & 15)) << 4)) = o;
    if (!kvw) asm volatile("s_waitcnt vmcnt(8)" ::: "memory");
    else      asm volatile("s_waitcnt vmcnt(0)" ::: "memory");
    asm volatile("s_waitcnt lgkmcnt(0)" : "+s"(tag) :: "memory");
    __builtin_amdgcn_s_barrier();
    CBAR();
    st128(rs, WS_XN + (unsigned)(tok0 + w) * 1024 + lane * 16, o);
    rkey = make_key(rbf(wave_sum(r)), tok0 + w);
  }
  tag += 1u;
  STAMP(0, 2);
  float st_m = 0.f, st_s = 1.f;
  if (kvw) {
    const int g = lane >> 4, q = (lane & 15) >> 2, p = lane & 3;
    const int ltok = hg * 8 + (lane & 7);
    const char* xb = XI + ltok * 1024;
    const char* wb = KI + (8 * g + q) * 32 + p * 8;
    f32x4 ak = {}, av = {};
    bf16x8 fx[2][2], fk[2][2], fv[2][2];
#define KV_LD(bf, k0) _Pragma("unroll") for (int i = 0; i < 2; ++i) { const int ks = (k0) + i; \
      fx[bf][i] = *(const bf16x8*)(xb + (((4 * ks + g) ^ (ltok & 15)) << 4)); \
      fk[bf][i] = tr_frag(wb + ks * 1024, wb + ks * 1024 + 128); \
      fv[bf][i] = tr_frag(wb + 16384 + ks * 1024, wb + 16384 + ks * 1024 + 128); }
    KV_LD(0, 0)
#pragma unroll
    for (int kb = 0; kb < 8; ++kb) {
      if (kb < 7) { KV_LD((kb + 1) & 1, 2 * kb + 2) }
      SB();
#pragma unroll
      for (int i = 0; i < 2; ++i) {
        ak = __builtin_amdgcn_mfma_f32_16x16x32_bf16(fk[kb & 1][i], fx[kb & 1][i], ak, 0, 0, 0);
        av = __builtin_amdgcn_mfma_f32_16x16x32_bf16(fx[kb & 1][i], fv[kb & 1][i], av, 0, 0, 0);
      }
      SB();
    }
    if ((lane & 15) < 8) st64(rs, WS_K + (unsigned)(tok0 + (lane & 15)) * 32 + (((g & 1) << 1) | (g >> 1)) * 8, u32x2{pack_bf(ak[0], ak[1]), pack_bf(ak[2], ak[3])});
    {
      u32x2 pv = {pack_bf(av[0], av[1]), pack_bf(av[2], av[3])};
      unsigned off = (lane & 15) * 16 + g * 8;
      if (lane >= 32) { const unsigned cst = (lane & 2) ? 0u : 0x3f803f80u; pv = u32x2{cst, cst}; off = 256 + (lane & 3) * 8; }
      if (lane < 36) st64(rs, WS_V + (unsigned)pid * VHALF + off, pv);
    }
    float n = ak[0] * ak[0] + ak[1] * ak[1] + ak[2] * ak[2] + ak[3] * ak[3];
    n = wave_maxf(add32(add16(n)));
    if (lane < 2) *(unsigned*)(CTL + lane * 4) = 0u;
    DRAIN();
    if (lane < 8) st64(rs, WS_KVF + (unsigned)(lane * 256 + pid) * 8, u32x2{__float_as_uint(n), tag});
    __builtin_amdgcn_s_setprio(0);
    STAMP(0, 6);
  } else {
    const int wr7 = w - (w > hg ? 1 : 0);
#pragma unroll
    for (int i = 0; i < 9; ++i) { const int pc = i * 7 + wr7; glds16(wq + pc * 8 * D_ + hg * 64, wq_off, WI + pc * 1024); }
    if (wr7 == 0) glds16(wq + 63 * 8 * D_ + hg * 64, wq_off, WI + 63 * 1024);
    asm volatile("s_waitcnt vmcnt(10)" : "+v"(xv[0]), "+v"(xv[1]), "+v"(xv[2]), "+v"(xv[3]), "+v"(xv[4]), "+v"(xv[5]), "+v"(xv[6]), "+v"(xv[7]) :: "memory");
    float s = 0.f;
#pragma unroll
    for (int i = 0; i < 8; ++i)
#pragma unroll
      for (int j = 0; j < 4; ++j) s = dot2(xv[i][j], 0x3f803f80u, s);
    const float mean = sum8(s) * (1.0f / 512.0f);
    float v = 0.f;
#pragma unroll
    for (int i = 0; i < 8; ++i)
#pragma unroll
      for (int j = 0; j < 4; ++j) { const float a = bf_lo(xv[i][j]) - mean, bb = bf_hi(xv[i][j]) - mean; v = fmaf(a, a, v); v = fmaf(bb, bb, v); }
    const float var = rbf(sum8(v) * (1.0f / 512.0f));
    st_m = rbf(mean); st_s = rbf(__builtin_amdgcn_sqrtf(rbf(var + 1e-5f)));
    const float inv = __builtin_amdgcn_rcpf(st_s), nb = -st_m * inv;
    const int lrow = w * 8 + rr;
#pragma unroll
    for (int i = 0; i < 8; ++i) {
      u32x4 o;
#pragma unroll
      for (int j = 0; j < 4; ++j) o[j] = pack_bf(fmaf(bf_lo(xv[i][j]), inv, nb), fmaf(bf_hi(xv[i][j]), inv, nb));
      *(u32x4*)(XI + lrow * 1024 + (((i * 8 + c) ^ (lrow & 15)) << 4)) = o;
    }
    STAMP(0, 3);
    DRAIN();
  }
  RAW_BARRIER();
  STAMP(0, 4);
  if (!kvw && c == 0) *(f32x2*)(CTL + C_STAT + (w * 8 + rr) * 8) = f32x2{st_m, st_s};
  if (lane == 0) { *(unsigned*)(CTL + C_R + w * 4) = rkey; *(f32x2*)(CTL + C_STAT + (hg * 8 + w) * 8) = f32x2{own_m, own_s}; }
  const int blk = (lane >> 4) & 1, q4 = (lane & 15) >> 2, p4 = lane & 3;
  if (w < 4) {
    const int nt = w & 1, tt = w >> 1;
    const int tok = tt * 32 + l31;
    const char* xb = XI + tok * 1024;
    const char* wb = WI + (8 * h + q4) * 128 + (((4 * nt + 2 * blk + (p4 >> 1)) ^ (((q4 >> 1) & 1) << 2)) << 4) + 8 * (p4 & 1);
    f32x16 acc = {};
    bf16x8 fa[2][4], fb[2][4];
#define Q_LD(bf, k0) _Pragma("unroll") for (int i = 0; i < 4; ++i) { const int ks = (k0) + i; \
      fa[bf][i] = tr_frag(wb + ks * 2048, wb + ks * 2048 + 512); \
      fb[bf][i] = *(const bf16x8*)(xb + ((((2 * (ks & 7) + h) ^ (tok & 15)) << 4) + (ks >> 3) * 256)); }
    Q_LD(0, 0)
#pragma unroll
    for (int kb = 0; kb < 8; ++kb) {
      if (kb < 7) { Q_LD((kb + 1) & 1, 4 * kb + 4) }
      SB();
#pragma unroll
      for (int i = 0; i < 4; ++i) acc = __builtin_amdgcn_mfma_f32_32x32x16_bf16(fa[kb & 1][i], fb[kb & 1][i], acc, 0, 0, 0);
      SB();
    }
    const float cs = 0.25f * 1.4426950408889634f;
    float n0 = 0.f, n1 = 0.f;
#pragma unroll
    for (int r = 0; r < 8; ++r) { acc[r] *= cs; acc[r + 8] *= cs; n0 = fmaf(acc[r], acc[r], n0); n1 = fmaf(acc[r + 8], acc[r + 8], n1); }
    char* qi = smem + L_QI + (tt * 4 + nt * 2) * 1024 + lane * 16;
    *(u32x4*)qi = u32x4{pack_bf(acc[0], acc[1]), pack_bf(acc[2], acc[3]), pack_bf(acc[4], acc[5]), pack_bf(acc[6], acc[7])};
    *(u32x4*)(qi + 1024) = u32x4{pack_bf(acc[8], acc[9]), pack_bf(acc[10], acc[11]), pack_bf(acc[12], acc[13]), pack_bf(acc[14], acc[15])};
    const float nm = wave_maxf(fmaxf(add32(n0), add32(n1)));
    if (lane == 0) *(float*)(CTL + C_QMAX + w * 4) = nm;
  }
  asm volatile("s_waitcnt lgkmcnt(0)" ::: "memory");
  if (lane == 0) __hip_atomic_fetch_add((LAS unsigned*)(CTL + C_CNT), 1u, __ATOMIC_RELAXED, __HIP_MEMORY_SCOPE_WORKGROUP);
  STAMP(0, 5);

  float kmax;
  {
    u32x2 f;
    _Pragma("nounroll") for (unsigned s = 0; s < SPIN_MAX; ++s) {
      f = ld64(rs, WS_KVF + (unsigned)(g8 * 256 + w * 32 + l31) * 8);
      if (__all(f[1] == tag)) break;
      __builtin_amdgcn_s_sleep(1);
    }
    CBAR();
    kmax = wave_maxf(__uint_as_float(f[0]));
  }
  STAMP(1, 0);
  bf16x8 kf[8], va[8], vb[8];
  {
    const unsigned kb = WS_K + (unsigned)(w * 256 + l31) * 32 + h * 16;
    const unsigned vp = WS_V + (unsigned)(w * 32 + h) * VHALF + min(l31, 17) * 16;
#pragma unroll
    for (int t = 0; t < 8; ++t) {
      asm volatile("buffer_load_dwordx4 %0, %1, %2, 0 offen offset:%3 sc1" : "=v"(kf[t]) : "v"(kb + (t >> 2) * 4096), "s"(rs), "i"((t & 3) * 1024) : "memory");
      asm volatile("buffer_load_dwordx4 %0, %1, %2, 0 offen offset:%3 sc1" : "=v"(va[t]) : "v"(vp + (t / 3) * 12 * VHALF), "s"(rs), "i"((4 * (t % 3)) * VHALF) : "memory");
      asm volatile("buffer_load_dwordx4 %0, %1, %2, 0 offen offset:%3 sc1" : "=v"(vb[t]) : "v"(vp + (t / 3) * 12 * VHALF), "s"(rs), "i"((4 * (t % 3) + 2) * VHALF) : "memory");
    }
  }
  u32x4 wov[8];
  {
#pragma unroll
    for (int i = 0; i < 8; ++i)
      asm volatile("global_load_dwordx4 %0, %1, %2" : "=v"(wov[i]) : "v"(rr * 1024 + c * 16), "s"(wo + (size_t)(w * 64 + i * 8) * D_ + hg * 64) : "memory");
  }
  lds_wait_ge(CTL + C_CNT, 8u);
  STAMP(1, 1);
  if (w == 4) {
    const unsigned key = lane < 8 ? *(const unsigned*)(CTL + C_R + lane * 4) : 0u;
    const unsigned k1 = wave_maxu(key), k2 = wave_maxu(key == k1 ? 0u : key);
    if (lane < 8) st128(rs, WS_CAND + (unsigned)(lane * 256 + pid) * 16, u32x4{k1, tag, k2, tag});
  }
  const f32x4 qn = *(const f32x4*)(CTL + C_QMAX);
  const float qmax = fmaxf(fmaxf(qn[0], qn[1]), fmaxf(qn[2], qn[3]));
  const float bound = __builtin_amdgcn_sqrtf(qmax * kmax) * 1.02f + 0.01f;
  const bool fast = __builtin_amdgcn_readfirstlane(bound <= 48.0f ? 1 : 0) != 0;
  const int idx = (int)0xDDDDDDDDu;
  bf16x8 zf = {};
  asm volatile("" : "+v"(zf));
  const f32x16 z16 = {};
  const char* qbase = smem + L_QI + lane * 16;
  const int u = (b & 7) * 28 + (b >> 3);
  f32x16 o;
  float shift = bound;
  bf16x8 qf = *(const bf16x8*)(qbase + ((w + 1) & 7) * 1024);
  bf16x8 qn1 = *(const bf16x8*)(qbase + ((w + 2) & 7) * 1024);
  f32x16 cc = splat16(-bound);
  f32x16 s0;
#define FIRST_QK() asm volatile("s_waitcnt vmcnt(31)\n\ts_nop 1\n\tv_mfma_f32_32x32x16_bf16 v[208:223], %[k0], %[q], v[192:207]\n\ts_nop 11" \
               : "={v[208:223]}"(s0), "+{v[192:207]}"(cc) : [k0] "v"(kf[0]), [q] "v"(qf))
  FIRST_QK();

#pragma nounroll
  for (int i = 0; i < 8; ++i) {
    if (w == 0 && i == 1 && b < NU) {
      int ln = lane; asm volatile("" : "+v"(ln));
#pragma unroll
      for (int j = 0; j < 16; ++j) glds16((j < 8 ? wg : wu) + (j & 7) * 64 * F_ + u * 8, ln * (F_ * 2), smem + L_GU + j * 1024);
    }
    if (w == 0 && i == 2) {
      int lane = tid; asm volatile("" : "+v"(lane));
      const int h = lane >> 5, l31 = lane & 31, blk = (lane >> 4) & 1, q4 = (lane & 15) >> 2, p4 = lane & 3;
      unsigned m1 = 0, m2 = 0;
      _Pragma("nounroll") for (unsigned s = 0; s < SPIN_MAX; ++s) {
        bool ok = true; m1 = 0; m2 = 0;
#pragma unroll
        for (int j = 0; j < 4; ++j) {
          const u32x4 cd = ld128(rs, WS_CAND + (unsigned)(g8 * 256 + j * 64 + lane) * 16);
          ok = ok && cd[1] == tag && cd[3] == tag;
          m2 = max(max(m2, cd[2]), min(m1, cd[0])); m1 = max(m1, cd[0]);
        }
        if (__all(ok)) break;
        __builtin_amdgcn_s_sleep(1);
      }
      CBAR();
      const unsigned k1 = __builtin_amdgcn_readfirstlane(wave_maxu(m1));
      const unsigned k2 = __builtin_amdgcn_readfirstlane(wave_maxu(m1 == k1 ? m2 : m1));
      if (lane == 0) { *(u32x2*)(CTL + C_K1) = u32x2{k1, k2}; asm volatile("s_waitcnt lgkmcnt(0)" ::: "memory"); *(volatile LAS unsigned*)(CTL + C_SEL) = 1u; }
      if (b < NU) {
        const int g = lane >> 4;
        const unsigned xoff = WS_XN + (unsigned)key_tok((lane & 1) ? k2 : k1) * 1024 + g * 16;
        const char* wb = smem + L_GU + (p4 >> 1) * 8192 + (8 * g + q4) * 16 + (p4 & 1) * 8;
        f32x4 acc = {};
        DRAIN();
#pragma unroll
        for (int k0 = 0; k0 < 16; k0 += 4) {
          u32x4 bx[4];
#pragma unroll
          for (int j = 0; j < 4; ++j) bx[j] = ld128(rs, xoff + (k0 + j) * 64);
#pragma unroll
          for (int j = 0; j < 4; ++j) {
            const bf16x8 a = tr_frag(wb + (k0 + j) * 512, wb + (k0 + j) * 512 + 64);
            acc = __builtin_amdgcn_mfma_f32_16x16x32_bf16(a, __builtin_bit_cast(bf16x8, bx[j]), acc, 0, 0, 0);
          }
        }
        asm volatile("s_waitcnt lgkmcnt(0)" ::: "memory");
#pragma unroll
        for (int f = 0; f < 8; ++f) glds16(wd + (u * 8 + f) * D_, lane * 16, smem + L_GU + f * 1088);
        float hv[4];
#pragma unroll
        for (int r = 0; r < 4; ++r) {
          auto sw = __builtin_amdgcn_permlane32_swap(__float_as_uint(acc[r]), __float_as_uint(acc[r]), false, false);
          hv[r] = rbf(rbf(silu_f(rbf(__uint_as_float(sw[0])))) * rbf(__uint_as_float(sw[1])));
        }
        const unsigned h0 = pack_bf(hv[0], hv[1]), h1 = pack_bf(hv[2], hv[3]);
        auto e0 = __builtin_amdgcn_permlane16_swap(h0, h0, false, false);
        auto e1 = __builtin_amdgcn_permlane16_swap(h1, h1, false, false);
        const u32x4 ha = (lane < 2) ? u32x4{e0[0], e1[0], e0[1], e1[1]} : u32x4{0u, 0u, 0u, 0u};
        const bf16x8 A = __builtin_bit_cast(bf16x8, ha);
        const char* db = smem + L_GU + q4 * 1088 + (2 * blk + (p4 >> 1)) * 16 + 8 * (p4 & 1);
        DRAIN();
#pragma unroll 4
        for (int ct = 0; ct < 16; ++ct) {
          const bf16x8 bw = tr_frag(db + ct * 64, db + ct * 64 + 4 * 1088);
          const f32x16 z = QK(A, bw, z16);
          auto sw = __builtin_amdgcn_permlane32_swap(__float_as_uint(z[0]), __float_as_uint(z[1]), false, false);
          st32(rs, WS_YP + (unsigned)u * 4096 + h * 2048 + (ct * 32 + l31) * 4, sw[0]);
        }
        DRAIN();
        if (lane == 0) st32(rs, WS_YF + u * 4, tag);
      }
    }
      STAMP(3, i);
    const bf16x8 qn2 = *(const bf16x8*)(qbase + ((w + 3 + i) & 7) * 1024);
    if (!fast) {
      asm volatile("s_waitcnt vmcnt(0)" ::: "memory");
      SB();
      float m = -3.0e38f;
#pragma unroll
      for (int t = 0; t < 8; ++t) {
        const f32x16 sx = QK(kf[t], qf, z16);
#pragma unroll
        for (int r = 0; r < 16; ++r) m = fmaxf(m, sx[r]);
        SB();
      }
      shift = max32(m);
      cc = splat16(-shift);
      FIRST_QK();
    }
    asm volatile(PASS_ASM
                 : "+{v[208:223]}"(s0), "=&{v[240:255]}"(o), "+{v[192:207]}"(cc)
                 : [k0] "v"(kf[0]), [k1] "v"(kf[1]), [k2] "v"(kf[2]), [k3] "v"(kf[3]), [k4] "v"(kf[4]), [k5] "v"(kf[5]), [k6] "v"(kf[6]), [k7] "v"(kf[7]),
                   [a0] "v"(va[0]), [a1] "v"(va[1]), [a2] "v"(va[2]), [a3] "v"(va[3]), [a4] "v"(va[4]), [a5] "v"(va[5]), [a6] "v"(va[6]), [a7] "v"(va[7]),
                   [b0] "v"(vb[0]), [b1] "v"(vb[1]), [b2] "v"(vb[2]), [b3] "v"(vb[3]), [b4] "v"(vb[4]), [b5] "v"(vb[5]), [b6] "v"(vb[6]), [b7] "v"(vb[7]),
                   [q] "v"(qf), [qn] "v"(qn1), [z] "v"(zf), [idx] "v"(idx)
                 : "v224", "v225", "v226", "v227", "v228", "v229", "v230", "v231", "v232", "v233", "v234", "v235", "v236", "v237", "v238", "v239");
    qf = qn1; qn1 = qn2;
    if (i < 7) {
      char* sl = smem + (((w + 1 + i) & 7) * 7 + (6 - i)) * PSLOT;
      *(f32x4*)(sl + lane * 16) = f32x4{o[0], o[1], o[2], o[3]};
      *(f32x4*)(sl + 1024 + lane * 16) = f32x4{o[4], o[5], o[6], o[7]};
      *(float*)(sl + 2048 + lane * 4) = h ? shift : o[8];
    }
  }
  STAMP(1, 2);
  RAW_BARRIER();
  STAMP(1, 3);
  {
    float a[8], pl[7], pm[7];
    auto sw = __builtin_amdgcn_permlane32_swap(__float_as_uint(o[8]), __float_as_uint(o[8]), false, false);
    float l = __uint_as_float(sw[0]);
    float mx = shift;
#pragma unroll
    for (int sidx = 0; sidx < 7; ++sidx) {
      const char* sl = smem + (w * 7 + sidx) * PSLOT + 2048 + l31 * 4;
      pl[sidx] = *(const float*)sl; pm[sidx] = *(const float*)(sl + 128);
      mx = fmaxf(mx, pm[sidx]);
    }
    const float f0 = __builtin_amdgcn_exp2f(shift - mx);
    l *= f0;
#pragma unroll
    for (int r = 0; r < 8; ++r) a[r] = o[r] * f0;
#pragma unroll
    for (int sidx = 0; sidx < 7; ++sidx) {
      const char* sl = smem + (w * 7 + sidx) * PSLOT;
      const f32x4 p0 = *(const f32x4*)(sl + lane * 16), p1 = *(const f32x4*)(sl + 1024 + lane * 16);
      const float f = __builtin_amdgcn_exp2f(pm[sidx] - mx);
#pragma unroll
      for (int r = 0; r < 4; ++r) { a[r] = fmaf(p0[r], f, a[r]); a[r + 4] = fmaf(p1[r], f, a[r + 4]); }
      l = fmaf(pl[sidx], f, l);
    }
    const float inv = __builtin_amdgcn_rcpf(l);
    const unsigned fr = (unsigned)((tb * 2 + (w >> 2)) * 32 + hg * 4 + (w & 3));
    st128(rs, WS_O + fr * 1024 + lane * 16,
          u32x4{pack_bf(a[0] * inv, a[1] * inv), pack_bf(a[2] * inv, a[3] * inv), pack_bf(a[4] * inv, a[5] * inv), pack_bf(a[6] * inv, a[7] * inv)});
    DRAIN();
    if (lane == 0) st32(rs, WS_OF + fr * 4, tag);
  }
  STAMP(1, 4);
#pragma unroll
  for (int i = 0; i < 8; ++i) {
    const int row = i * 8 + rr;
    *(u32x4*)(smem + w * (7 * PSLOT) + row * 128 + ((c ^ (((row >> 1) & 1) << 2)) << 4)) = wov[i];
  }
  if (b == 0 && tid == 0) st32(rs, WS_EPOCH, tag);
  const int etok = tid >> 3, gtok = tb * 64 + etok;
  const u32x4 xe = *(const u32x4*)(x + (size_t)gtok * D_ + hg * 64 + c * 8);
  const unsigned k1 = *(const unsigned*)(CTL + C_K1), k2 = *(const unsigned*)(CTL + C_K2);
  const int t0 = __builtin_amdgcn_readfirstlane(key_tok(k1)), t1 = __builtin_amdgcn_readfirstlane(key_tok(k2));
  float* YR = (float*)(smem + L_GU);
  const bool hit = (t0 >> 6) == tb || (t1 >> 6) == tb;
  if (hit) {
    _Pragma("nounroll") for (unsigned s = 0; s < SPIN_MAX; ++s) {
      const u32x4 f = ld128(rs, WS_YF + min(lane, 55) * 16);
      if (__all(f[0] == tag && f[1] == tag && f[2] == tag && f[3] == tag)) break;
      __builtin_amdgcn_s_sleep(1);
    }
    CBAR();
    f32x4 y = {};
    const unsigned yo = WS_YP + (unsigned)(w * 28 + h) * 4096 + blk * 2048 + (hg * 64 + (lane & 15) * 4) * 4;
#pragma unroll
    for (int i = 0; i < 14; ++i) y += __builtin_bit_cast(f32x4, ld128(rs, yo + i * 8192));
#pragma unroll
    for (int j = 0; j < 4; ++j) y[j] = add32(y[j]);
    if (lane < 32) *(f32x4*)(YR + (w * 2 + blk) * 64 + (lane & 15) * 4) = y;
  }
  RAW_BARRIER();
  float gate[8];
  {
    const f32x2 st = *(const f32x2*)(CTL + C_STAT + etok * 8);
    const float inv = __builtin_amdgcn_rcpf(st[1]);
    const int sel = (gtok == t0) ? 0 : ((gtok == t1) ? 1 : -1);
    float upd[8];
#pragma unroll
    for (int j = 0; j < 8; ++j) upd[j] = 0.f;
    if (hit && sel >= 0) {
      const float rsg = rbf(sigm_f(key_val(sel ? k2 : k1)));
#pragma unroll
      for (int j = 0; j < 8; ++j) {
        float y = 0.f;
#pragma unroll
        for (int ww = 0; ww < 8; ++ww) y += YR[(ww * 2 + sel) * 64 + c * 8 + j];
        upd[j] = rbf(rbf(y) * rsg);
      }
    }
#pragma unroll
    for (int j = 0; j < 8; ++j) {
      const float xf = (j & 1) ? bf_hi(xe[j >> 1]) : bf_lo(xe[j >> 1]);
      const float xn = rbf(rbf(rbf(xf - st[0]) * inv) + upd[j]);
      gate[j] = rbf(silu_f(xn));
    }
  }
  STAMP(2, 0);
  {
    const int tt = w >> 2, kq = w & 3;
    bf16x8 fa[2][8];
#pragma unroll
    for (int i = 0; i < 8; ++i) {
      const int piece = 2 * kq + (i >> 2), row0 = (i & 3) * 16 + 4 * h + q4;
#pragma unroll
      for (int nt = 0; nt < 2; ++nt) {
        const char* wb = smem + piece * (7 * PSLOT) + row0 * 128 + (((4 * nt + 2 * blk + (p4 >> 1)) ^ (((q4 >> 1) & 1) << 2)) << 4) + 8 * (p4 & 1);
        fa[nt][i] = tr_frag(wb, wb + 8 * 128);
      }
    }
    RAW_BARRIER();
    const unsigned fr0 = (unsigned)((tb * 2 + tt) * 32 + kq * 8);
    _Pragma("nounroll") for (unsigned s = 0; s < SPIN_MAX; ++s) {
      const unsigned f = ld32(rs, WS_OF + (fr0 + (lane & 7)) * 4);
      if (__all(f == tag)) break;
      __builtin_amdgcn_s_sleep(1);
    }
    CBAR();
    STAMP(2, 1);
    u32x4 fb[8];
#pragma unroll
    for (int i = 0; i < 8; ++i) fb[i] = ld128(rs, WS_O + (fr0 + i) * 1024 + lane * 16);
    f32x16 acc0 = {}, acc1 = {};
#pragma unroll
    for (int i = 0; i < 8; ++i) {
      acc0 = QK(fa[0][i], __builtin_bit_cast(bf16x8, fb[i]), acc0);
      acc1 = QK(fa[1][i], __builtin_bit_cast(bf16x8, fb[i]), acc1);
    }
    STAMP(2, 2);
    char* pr = smem + (kq * 64 + tt * 32 + l31) * 272 + h * 16;
#pragma unroll
    for (int g = 0; g < 4; ++g) {
      *(f32x4*)(pr + g * 32) = f32x4{acc0[4 * g], acc0[4 * g + 1], acc0[4 * g + 2], acc0[4 * g + 3]};
      *(f32x4*)(pr + 128 + g * 32) = f32x4{acc1[4 * g], acc1[4 * g + 1], acc1[4 * g + 2], acc1[4 * g + 3]};
    }
  }
  RAW_BARRIER();
  {
    f32x4 s0v = {}, s1v = {};
#pragma unroll
    for (int kq = 0; kq < 4; ++kq) {
      const char* pr = smem + (kq * 64 + etok) * 272 + c * 32;
      s0v += *(const f32x4*)pr; s1v += *(const f32x4*)(pr + 16);
    }
    float res[8];
#pragma unroll
    for (int j = 0; j < 8; ++j) {
      const float xf = (j & 1) ? bf_hi(xe[j >> 1]) : bf_lo(xe[j >> 1]);
      const float sv = rbf(j < 4 ? s0v[j & 3] : s1v[j & 3]);
      res[j] = xf + rbf(sv * gate[j]);
    }
    *(u32x4*)(out + (size_t)gtok * D_ + hg * 64 + c * 8) =
        u32x4{pack_bf(res[0], res[1]), pack_bf(res[2], res[3]), pack_bf(res[4], res[5]), pack_bf(res[6], res[7])};
  }
  STAMP(2, 3);
}

extern "C" void kernel_launch(void* const* d_in, const int* in_sizes, int n_in, void* d_out, int out_size, void* d_ws, size_t ws_size,
                              hipStream_t stream) {
  fused_block<<<256, 512, 0, stream>>>((const u16*)d_in[0], (const u16*)d_in[1], (const u16*)d_in[2], (const u16*)d_in[3], (const u16*)d_in[4],
                                       (const u16*)d_in[6], (const u16*)d_in[7], (const u16*)d_in[8], (const u16*)d_in[9], (char*)d_ws, (u16*)d_out);
}
```

```cpp
#include <hip/hip_runtime.h>
#include <hip/hip_bf16.h>
#include <stdint.h>

typedef __attribute__((ext_vector_type(2))) __bf16 bf16x2;
typedef __attribute__((ext_vector_type(8))) __bf16 bf16x8;
typedef __attribute__((ext_vector_type(4))) short s16x4;
typedef __attribute__((ext_vector_type(8))) short s16x8;
typedef __attribute__((ext_vector_type(2))) float f32x2;
typedef __attribute__((ext_vector_type(4))) float f32x4;
typedef __attribute__((ext_vector_type(16))) float f32x16;
typedef __attribute__((ext_vector_type(32))) float f32x32;
typedef __attribute__((ext_vector_type(2))) unsigned u32x2;
typedef __attribute__((ext_vector_type(4))) unsigned u32x4;
typedef unsigned short u16;
typedef __amdgpu_buffer_rsrc_t rsrc_t;
#define LAS __attribute__((address_space(3)))
#define DI __device__ __forceinline__

constexpr int S_ = 2048, D_ = 512, F_ = 1792;
constexpr int NU = 224;
constexpr int VHALF = 256;
constexpr int SC1 = 16;
constexpr unsigned SPIN_MAX = 1u << 18;

constexpr unsigned WS_EPOCH = 0;
constexpr unsigned WS_KVF = 1024;
constexpr unsigned WS_CAND = WS_KVF + 8 * 256 * 8;
constexpr unsigned WS_YF = WS_CAND + 8 * 512 * 8;
constexpr unsigned WS_YG = WS_YF + 8 * 1024;
constexpr unsigned WS_K = WS_YG + 1024 * 8;
constexpr unsigned WS_V = WS_K + S_ * 32;
constexpr unsigned WS_YP = WS_V + 256 * VHALF;
constexpr unsigned WS_P1 = WS_YP + NU * 2 * D_ * 4;
constexpr unsigned WS_O = WS_P1 + NU * 2048;
constexpr unsigned WS_END = WS_O + S_ * D_ * 2;

constexpr int L_WI = 0;
constexpr int L_XI = 65536;
constexpr int L_KI = 131072;
constexpr int PSLOT = 2304;
constexpr int PREG = 16384;
constexpr int L_XS = 7 * PREG;
constexpr int L_GU = 131072;
constexpr int L_QI = 147456;
constexpr int L_CTL = L_QI + 8192;
constexpr int L_DN = L_CTL + 1024;
constexpr int L_DN2 = 7 * PREG + PSLOT + 128;
constexpr int L_HS = 0;
constexpr int L_TOTAL = L_DN + 6 * 1088;
constexpr int C_CNT = 0;
constexpr int C_SEL = 4;
constexpr int C_K1 = 8, C_K2 = 12;
constexpr int C_QMAX = 16;
constexpr int C_R = 32;
constexpr int C_STAT = 64;

#define STAMP(kid, i)

DI float bf_lo(unsigned u) { return __uint_as_float(u << 16); }
DI float bf_hi(unsigned u) { return __uint_as_float(u & 0xffff0000u); }
DI unsigned pack_bf(float a, float b) { f32x2 f = {a, b}; return __builtin_bit_cast(unsigned, __builtin_convertvector(f, bf16x2)); }
DI float rbf(float a) { return (float)(__bf16)a; }
DI float dot2(unsigned a, unsigned b, float c) { return __builtin_amdgcn_fdot2_f32_bf16(__builtin_bit_cast(bf16x2, a), __builtin_bit_cast(bf16x2, b), c, false); }
DI void glds16(const void* sbase, unsigned voff, void* lds_wave_base) {
  unsigned keep; const unsigned dst = (unsigned)(uintptr_t)(LAS char*)lds_wave_base;
  asm volatile("s_mov_b32 %0, m0\n\ts_mov_b32 m0, %2\n\ts_nop 0\n\tglobal_load_lds_dwordx4 %1, %3\n\ts_mov_b32 m0, %0"
               : "=&s"(keep) : "v"(voff), "s"(dst), "s"(sbase) : "memory");
}
#define GLD128(dst, voff, sbase) asm volatile("global_load_dwordx4 %0, %1, %2" : "=v"(dst) : "v"(voff), "s"(sbase) : "memory")
DI const char* opq(const char* p) { unsigned a = (unsigned)(uintptr_t)(LAS char*)p; asm volatile("" : "+v"(a)); return (const char*)(LAS char*)(uintptr_t)a; }
DI s16x4 tr_read(const char* p) { return __builtin_amdgcn_ds_read_tr16_b64_v4i16((LAS s16x4*)p); }
DI bf16x8 tr_frag(const char* p0, const char* p1) {
  s16x4 a = tr_read(p0), b = tr_read(p1);
  s16x8 v = __builtin_shufflevector(a, b, 0, 1, 2, 3, 4, 5, 6, 7);
  return __builtin_bit_cast(bf16x8, v);
}
template <int CTRL> DI float dpp_f(float v) { return __int_as_float(__builtin_amdgcn_update_dpp(0, __float_as_int(v), CTRL, 0xf, 0xf, true)); }
template <int CTRL> DI unsigned dpp_u(unsigned v) { return (unsigned)__builtin_amdgcn_update_dpp(0, (int)v, CTRL, 0xf, 0xf, true); }
DI float sum8(float v) {
  v += dpp_f<0xB1>(v); v += dpp_f<0x4E>(v); v += dpp_f<0x141>(v);
  return v;
}
DI float add16(float v) { auto r = __builtin_amdgcn_permlane16_swap(__float_as_uint(v), __float_as_uint(v), false, false); return __uint_as_float(r[0]) + __uint_as_float(r[1]); }
DI float add32(float v) { auto r = __builtin_amdgcn_permlane32_swap(__float_as_uint(v), __float_as_uint(v), false, false); return __uint_as_float(r[0]) + __uint_as_float(r[1]); }
DI float max16(float v) { auto r = __builtin_amdgcn_permlane16_swap(__float_as_uint(v), __float_as_uint(v), false, false); return fmaxf(__uint_as_float(r[0]), __uint_as_float(r[1])); }
DI float max32(float v) { auto r = __builtin_amdgcn_permlane32_swap(__float_as_uint(v), __float_as_uint(v), false, false); return fmaxf(__uint_as_float(r[0]), __uint_as_float(r[1])); }
DI float sum32(float v) { v = sum8(v); v += dpp_f<0x140>(v); return add16(v); }
DI float wave_sum(float v) { return add32(sum32(v)); }
DI float wave_maxf(float v) {
  v = fmaxf(v, dpp_f<0xB1>(v)); v = fmaxf(v, dpp_f<0x4E>(v)); v = fmaxf(v, dpp_f<0x141>(v)); v = fmaxf(v, dpp_f<0x140>(v));
  return max32(max16(v));
}
DI unsigned wave_maxu(unsigned v) {
  v = max(v, dpp_u<0xB1>(v)); v = max(v, dpp_u<0x4E>(v)); v = max(v, dpp_u<0x141>(v)); v = max(v, dpp_u<0x140>(v));
  auto r = __builtin_amdgcn_permlane16_swap(v, v, false, false); v = max(r[0], r[1]);
  auto q = __builtin_amdgcn_permlane32_swap(v, v, false, false); return max(q[0], q[1]);
}
DI float wave_maxpos(float v) { return __uint_as_float(wave_maxu(__float_as_uint(v))); }
DI float silu_f(float v) { return v * __builtin_amdgcn_rcpf(1.0f + __builtin_amdgcn_exp2f(-1.4426950408889634f * v)); }
DI float sigm_f(float v) { return __builtin_amdgcn_rcpf(1.0f + __builtin_amdgcn_exp2f(-1.4426950408889634f * v)); }
DI float ln_sd(float var) { return rbf(__builtin_amdgcn_sqrtf(rbf(rbf(var) + 1e-5f))); }
DI unsigned ln_pair(unsigned xu, float mb, float inv) { return pack_bf(rbf(bf_lo(xu) - mb) * inv, rbf(bf_hi(xu) - mb) * inv); }

DI unsigned make_key(float r_bf, int token) {
  unsigned b = __float_as_uint(r_bf) >> 16;
  b = (b & 0x8000u) ? (~b & 0xffffu) : (b | 0x8000u);
  return (b << 16) | (unsigned)(2047 - token);
}
DI float key_val(unsigned key) {
  unsigned b = key >> 16;
  b = (b & 0x8000u) ? (b & 0x7fffu) : (~b & 0xffffu);
  return __uint_as_float(b << 16);
}
DI int key_tok(unsigned key) { return 2047 - (int)(key & 0x7ffu); }

DI u32x4 ld128(rsrc_t rs, unsigned off) { return __builtin_amdgcn_raw_buffer_load_b128(rs, (int)off, 0, SC1); }
DI u32x2 ld64(rsrc_t rs, unsigned off) { return __builtin_amdgcn_raw_buffer_load_b64(rs, (int)off, 0, SC1); }
DI unsigned ld32(rsrc_t rs, unsigned off) { return __builtin_amdgcn_raw_buffer_load_b32(rs, (int)off, 0, SC1); }
DI void st128(rsrc_t rs, unsigned off, u32x4 v) { __builtin_amdgcn_raw_buffer_store_b128(v, rs, (int)off, 0, SC1); }
DI void st64(rsrc_t rs, unsigned off, u32x2 v) { __builtin_amdgcn_raw_buffer_store_b64(v, rs, (int)off, 0, SC1); }
DI void st32(rsrc_t rs, unsigned off, unsigned v) { __builtin_amdgcn_raw_buffer_store_b32(v, rs, (int)off, 0, SC1); }
#define DRAIN() asm volatile("s_waitcnt vmcnt(0)" ::: "memory")
#define VMWAIT(n) asm volatile("s_waitcnt vmcnt(" #n ")" ::: "memory")
#define CBAR() asm volatile("" ::: "memory")
#define RAW_BARRIER() do { asm volatile("s_waitcnt lgkmcnt(0)" ::: "memory"); __builtin_amdgcn_s_barrier(); asm volatile("" ::: "memory"); } while (0)
#define SPIN _Pragma("nounroll") for (unsigned s_ = 0; s_ < SPIN_MAX; ++s_)
DI unsigned lds_ld(const char* p) { return __hip_atomic_load((LAS unsigned*)p, __ATOMIC_RELAXED, __HIP_MEMORY_SCOPE_WORKGROUP); }
DI void lds_wait_ge(const char* p, unsigned want) {
  SPIN {
    if ((unsigned)__builtin_amdgcn_readfirstlane(lds_ld(p)) >= want) break;
    __builtin_amdgcn_s_sleep(1);
  }
  CBAR();
}
#define QK(KF, QF, C) __builtin_amdgcn_mfma_f32_32x32x16_bf16((KF), (QF), (C), 0, 0, 0)
DI f32x16 splat16(float v) { f32x16 r;
#pragma unroll
  for (int i = 0; i < 16; ++i) r[i] = v;
  return r; }
#define SB() __builtin_amdgcn_sched_barrier(0)
#define SGB(mask, n) __builtin_amdgcn_sched_group_barrier(mask, n, 0)

#define VCONST_ASM \
  "v_mov_b32 v132, %[cst]\n\t" "v_mov_b32 v133, %[cst]\n\t" "v_mov_b32 v134, %[cst]\n\t" "v_mov_b32 v135, %[cst]\n\t" "v_mov_b32 v140, %[cst]\n\t" \
  "v_mov_b32 v141, %[cst]\n\t" "v_mov_b32 v142, %[cst]\n\t" "v_mov_b32 v143, %[cst]\n\t" "v_mov_b32 v148, %[cst]\n\t" "v_mov_b32 v149, %[cst]\n\t" \
  "v_mov_b32 v150, %[cst]\n\t" "v_mov_b32 v151, %[cst]\n\t" "v_mov_b32 v156, %[cst]\n\t" "v_mov_b32 v157, %[cst]\n\t" "v_mov_b32 v158, %[cst]\n\t" \
  "v_mov_b32 v159, %[cst]\n\t" "v_mov_b32 v164, %[cst]\n\t" "v_mov_b32 v165, %[cst]\n\t" "v_mov_b32 v166, %[cst]\n\t" "v_mov_b32 v167, %[cst]\n\t" \
  "v_mov_b32 v172, %[cst]\n\t" "v_mov_b32 v173, %[cst]\n\t" "v_mov_b32 v174, %[cst]\n\t" "v_mov_b32 v175, %[cst]\n\t" "v_mov_b32 v180, %[cst]\n\t" \
  "v_mov_b32 v181, %[cst]\n\t" "v_mov_b32 v182, %[cst]\n\t" "v_mov_b32 v183, %[cst]\n\t" "v_mov_b32 v188, %[cst]\n\t" "v_mov_b32 v189, %[cst]\n\t" \
  "v_mov_b32 v190, %[cst]\n\t" "v_mov_b32 v191, %[cst]\n\t" ""
#define LOADS_ASM \
  "buffer_load_dwordx4 v[96:99], %[ka0], %[rs], 0 offen offset:0 sc1\n\t" "buffer_load_dwordx4 v[128:131], %[va0], %[rs], 0 offen offset:0 sc1\n\t" \
  "buffer_load_dwordx4 v[100:103], %[ka0], %[rs], 0 offen offset:1024 sc1\n\t" "buffer_load_dwordx4 v[136:139], %[va0], %[rs], 0 offen offset:1024 sc1\n\t" \
  "buffer_load_dwordx4 v[104:107], %[ka0], %[rs], 0 offen offset:2048 sc1\n\t" "buffer_load_dwordx4 v[144:147], %[va0], %[rs], 0 offen offset:2048 sc1\n\t" \
  "buffer_load_dwordx4 v[108:111], %[ka0], %[rs], 0 offen offset:3072 sc1\n\t" "buffer_load_dwordx4 v[152:155], %[va0], %[rs], 0 offen offset:3072 sc1\n\t" \
  "buffer_load_dwordx4 v[112:115], %[ka1], %[rs], 0 offen offset:0 sc1\n\t" "buffer_load_dwordx4 v[160:163], %[va1], %[rs], 0 offen offset:0 sc1\n\t" \
  "buffer_load_dwordx4 v[116:119], %[ka1], %[rs], 0 offen offset:1024 sc1\n\t" "buffer_load_dwordx4 v[168:171], %[va1], %[rs], 0 offen offset:1024 sc1\n\t" \
  "buffer_load_dwordx4 v[120:123], %[ka1], %[rs], 0 offen offset:2048 sc1\n\t" "buffer_load_dwordx4 v[176:179], %[va1], %[rs], 0 offen offset:2048 sc1\n\t" \
  "buffer_load_dwordx4 v[124:127], %[ka1], %[rs], 0 offen offset:3072 sc1\n\t" "buffer_load_dwordx4 v[184:187], %[va1], %[rs], 0 offen offset:3072 sc1\n\t" ""
#define SWAPS_ASM \
  "v_permlane16_swap_b32 v128, v132\n\t" "v_permlane16_swap_b32 v129, v133\n\t" "v_permlane16_swap_b32 v130, v134\n\t" "v_permlane16_swap_b32 v131, v135\n\t" \
  "v_permlane16_swap_b32 v136, v140\n\t" "v_permlane16_swap_b32 v137, v141\n\t" "v_permlane16_swap_b32 v138, v142\n\t" "v_permlane16_swap_b32 v139, v143\n\t" \
  "v_permlane16_swap_b32 v144, v148\n\t" "v_permlane16_swap_b32 v145, v149\n\t" "v_permlane16_swap_b32 v146, v150\n\t" "v_permlane16_swap_b32 v147, v151\n\t" \
  "v_permlane16_swap_b32 v152, v156\n\t" "v_permlane16_swap_b32 v153, v157\n\t" "v_permlane16_swap_b32 v154, v158\n\t" "v_permlane16_swap_b32 v155, v159\n\t" \
  "v_permlane16_swap_b32 v160, v164\n\t" "v_permlane16_swap_b32 v161, v165\n\t" "v_permlane16_swap_b32 v162, v166\n\t" "v_permlane16_swap_b32 v163, v167\n\t" \
  "v_permlane16_swap_b32 v168, v172\n\t" "v_permlane16_swap_b32 v169, v173\n\t" "v_permlane16_swap_b32 v170, v174\n\t" "v_permlane16_swap_b32 v171, v175\n\t" \
  "v_permlane16_swap_b32 v176, v180\n\t" "v_permlane16_swap_b32 v177, v181\n\t" "v_permlane16_swap_b32 v178, v182\n\t" "v_permlane16_swap_b32 v179, v183\n\t" \
  "v_permlane16_swap_b32 v184, v188\n\t" "v_permlane16_swap_b32 v185, v189\n\t" "v_permlane16_swap_b32 v186, v190\n\t" "v_permlane16_swap_b32 v187, v191\n\t" ""
#define PROLOG0_ASM \
  "s_waitcnt vmcnt(15)\n\t" "s_nop 1\n\t" "v_mfma_f32_32x32x16_bf16 v[208:223], v[96:99], %[q], v[192:207]\n\t" "s_waitcnt vmcnt(13)\n\t" \
  "v_mfma_f32_32x32x16_bf16 v[224:239], v[100:103], %[q], v[192:207]\n\t" "v_permlane16_swap_b32 v128, v132\n\t" "v_permlane16_swap_b32 v129, v133\n\t" \
  "v_permlane16_swap_b32 v130, v134\n\t" "v_permlane16_swap_b32 v131, v135\n\t" "s_nop 6\n\t" "v_exp_f32 v208, v208\n\t" "v_exp_f32 v209, v209\n\t" \
  "v_exp_f32 v210, v210\n\t" "v_exp_f32 v211, v211\n\t" "v_exp_f32 v212, v212\n\t" "v_exp_f32 v213, v213\n\t" "v_exp_f32 v214, v214\n\t" "v_exp_f32 v215, v215\n\t" \
  "v_exp_f32 v216, v216\n\t" "v_exp_f32 v217, v217\n\t" "v_exp_f32 v218, v218\n\t" "v_exp_f32 v219, v219\n\t" "v_exp_f32 v220, v220\n\t" ""
#define PROLOG_ASM \
  "s_nop 1\n\t" "v_mfma_f32_32x32x16_bf16 v[208:223], v[96:99], %[q], v[192:207]\n\t" "v_mfma_f32_32x32x16_bf16 v[224:239], v[100:103], %[q], v[192:207]\n\t" \
  "s_nop 10\n\t" "v_exp_f32 v208, v208\n\t" "v_exp_f32 v209, v209\n\t" "v_exp_f32 v210, v210\n\t" "v_exp_f32 v211, v211\n\t" "v_exp_f32 v212, v212\n\t" \
  "v_exp_f32 v213, v213\n\t" "v_exp_f32 v214, v214\n\t" "v_exp_f32 v215, v215\n\t" "v_exp_f32 v216, v216\n\t" "v_exp_f32 v217, v217\n\t" "v_exp_f32 v218, v218\n\t" \
  "v_exp_f32 v219, v219\n\t" "v_exp_f32 v220, v220\n\t" ""
#define PASS0_ASM \
  "v_mfma_f32_32x32x16_bf16 v[240:255], %[z], %[z], 0\n\t" "v_exp_f32 v221, v221\n\t" "v_exp_f32 v222, v222\n\t" "v_exp_f32 v223, v223\n\t" \
  "v_smfmac_f32_32x32x32_bf16 v[240:255], v[128:131], v[208:215], %[idx]\n\t" "v_exp_f32 v224, v224\n\t" "v_exp_f32 v225, v225\n\t" \
  "v_smfmac_f32_32x32x32_bf16 v[240:255], v[132:135], v[216:223], %[idx]\n\t" "v_exp_f32 v226, v226\n\t" "v_exp_f32 v227, v227\n\t" "v_exp_f32 v228, v228\n\t" \
  "s_waitcnt vmcnt(11)\n\t" "v_mfma_f32_32x32x16_bf16 v[208:223], v[104:107], %[q], v[192:207]\n\t" "v_permlane16_swap_b32 v136, v140\n\t" \
  "v_permlane16_swap_b32 v137, v141\n\t" "v_permlane16_swap_b32 v138, v142\n\t" "v_permlane16_swap_b32 v139, v143\n\t" "v_exp_f32 v229, v229\n\t" \
  "v_exp_f32 v230, v230\n\t" "v_exp_f32 v231, v231\n\t" "v_exp_f32 v232, v232\n\t" "v_exp_f32 v233, v233\n\t" \
  "v_smfmac_f32_32x32x32_bf16 v[240:255], v[136:139], v[224:231], %[idx]\n\t" "v_exp_f32 v234, v234\n\t" "v_exp_f32 v235, v235\n\t" "v_exp_f32 v236, v236\n\t" \
  "v_exp_f32 v237, v237\n\t" "v_exp_f32 v238, v238\n\t" "v_exp_f32 v239, v239\n\t" "v_exp_f32 v208, v208\n\t" "v_exp_f32 v209, v209\n\t" \
  "v_smfmac_f32_32x32x32_bf16 v[240:255], v[140:143], v[232:239], %[idx]\n\t" "v_exp_f32 v210, v210\n\t" "v_exp_f32 v211, v211\n\t" "v_exp_f32 v212, v212\n\t" \
  "s_waitcnt vmcnt(9)\n\t" "v_mfma_f32_32x32x16_bf16 v[224:239], v[108:111], %[q], v[192:207]\n\t" "v_permlane16_swap_b32 v144, v148\n\t" \
  "v_permlane16_swap_b32 v145, v149\n\t" "v_permlane16_swap_b32 v146, v150\n\t" "v_permlane16_swap_b32 v147, v151\n\t" "v_exp_f32 v213, v213\n\t" \
  "v_exp_f32 v214, v214\n\t" "v_exp_f32 v215, v215\n\t" "v_exp_f32 v216, v216\n\t" "v_exp_f32 v217, v217\n\t" \
  "v_smfmac_f32_32x32x32_bf16 v[240:255], v[144:147], v[208:215], %[idx]\n\t" "v_exp_f32 v218, v218\n\t" "v_exp_f32 v219, v219\n\t" "v_exp_f32 v220, v220\n\t" \
  "v_exp_f32 v221, v221\n\t" "v_exp_f32 v222, v222\n\t" "v_exp_f32 v223, v223\n\t" "v_exp_f32 v224, v224\n\t" "v_exp_f32 v225, v225\n\t" \
  "v_smfmac_f32_32x32x32_bf16 v[240:255], v[148:151], v[216:223], %[idx]\n\t" "v_exp_f32 v226, v226\n\t" "v_exp_f32 v227, v227\n\t" "v_exp_f32 v228, v228\n\t" \
  "s_waitcnt vmcnt(7)\n\t" "v_mfma_f32_32x32x16_bf16 v[208:223], v[112:115], %[q], v[192:207]\n\t" "v_permlane16_swap_b32 v152, v156\n\t" \
  "v_permlane16_swap_b32 v153, v157\n\t" "v_permlane16_swap_b32 v154, v158\n\t" "v_permlane16_swap_b32 v155, v159\n\t" "v_exp_f32 v229, v229\n\t" \
  "v_exp_f32 v230, v230\n\t" "v_exp_f32 v231, v231\n\t" "v_exp_f32 v232, v232\n\t" "v_exp_f32 v233, v233\n\t" \
  "v_smfmac_f32_32x32x32_bf16 v[240:255], v[152:155], v[224:231], %[idx]\n\t" "v_exp_f32 v234, v234\n\t" "v_exp_f32 v235, v235\n\t" "v_exp_f32 v236, v236\n\t" \
  "v_exp_f32 v237, v237\n\t" "v_exp_f32 v238, v238\n\t" "v_exp_f32 v239, v239\n\t" "v_exp_f32 v208, v208\n\t" "v_exp_f32 v209, v209\n\t" \
  "v_smfmac_f32_32x32x32_bf16 v[240:255], v[156:159], v[232:239], %[idx]\n\t" "v_exp_f32 v210, v210\n\t" "v_exp_f32 v211, v211\n\t" "v_exp_f32 v212, v212\n\t" \
  "s_waitcnt vmcnt(5)\n\t" "v_mfma_f32_32x32x16_bf16 v[224:239], v[116:119], %[q], v[192:207]\n\t" "v_permlane16_swap_b32 v160, v164\n\t" \
  "v_permlane16_swap_b32 v161, v165\n\t" "v_permlane16_swap_b32 v162, v166\n\t" "v_permlane16_swap_b32 v163, v167\n\t" "v_exp_f32 v213, v213\n\t" \
  "v_exp_f32 v214, v214\n\t" "v_exp_f32 v215, v215\n\t" "v_exp_f32 v216, v216\n\t" "v_exp_f32 v217, v217\n\t" \
  "v_smfmac_f32_32x32x32_bf16 v[240:255], v[160:163], v[208:215], %[idx]\n\t" "v_exp_f32 v218, v218\n\t" "v_exp_f32 v219, v219\n\t" "v_exp_f32 v220, v220\n\t" \
  "v_exp_f32 v221, v221\n\t" "v_exp_f32 v222, v222\n\t" "v_exp_f32 v223, v223\n\t" "v_exp_f32 v224, v224\n\t" "v_exp_f32 v225, v225\n\t" \
  "v_smfmac_f32_32x32x32_bf16 v[240:255], v[164:167], v[216:223], %[idx]\n\t" "v_exp_f32 v226, v226\n\t" "v_exp_f32 v227, v227\n\t" "v_exp_f32 v228, v228\n\t" \
  "s_waitcnt vmcnt(3)\n\t" "v_mfma_f32_32x32x16_bf16 v[208:223], v[120:123], %[q], v[192:207]\n\t" "v_permlane16_swap_b32 v168, v172\n\t" \
  "v_permlane16_swap_b32 v169, v173\n\t" "v_permlane16_swap_b32 v170, v174\n\t" "v_permlane16_swap_b32 v171, v175\n\t" "v_exp_f32 v229, v229\n\t" \
  "v_exp_f32 v230, v230\n\t" "v_exp_f32 v231, v231\n\t" "v_exp_f32 v232, v232\n\t" "v_exp_f32 v233, v233\n\t" \
  "v_smfmac_f32_32x32x32_bf16 v[240:255], v[168:171], v[224:231], %[idx]\n\t" "v_exp_f32 v234, v234\n\t" "v_exp_f32 v235, v235\n\t" "v_exp_f32 v236, v236\n\t" \
  "v_exp_f32 v237, v237\n\t" "v_exp_f32 v238, v238\n\t" "v_exp_f32 v239, v239\n\t" "v_exp_f32 v208, v208\n\t" "v_exp_f32 v209, v209\n\t" \
  "v_smfmac_f32_32x32x32_bf16 v[240:255], v[172:175], v[232:239], %[idx]\n\t" "v_exp_f32 v210, v210\n\t" "v_exp_f32 v211, v211\n\t" "v_exp_f32 v212, v212\n\t" \
  "s_waitcnt vmcnt(1)\n\t" "v_mfma_f32_32x32x16_bf16 v[224:239], v[124:127], %[q], v[192:207]\n\t" "ds_read_b128 %[q], %[qa]\n\t" "v_permlane16_swap_b32 v176, v180\n\t" \
  "v_permlane16_swap_b32 v177, v181\n\t" "v_permlane16_swap_b32 v178, v182\n\t" "v_permlane16_swap_b32 v179, v183\n\t" "v_exp_f32 v213, v213\n\t" \
  "v_exp_f32 v214, v214\n\t" "v_exp_f32 v215, v215\n\t" "v_exp_f32 v216, v216\n\t" "v_exp_f32 v217, v217\n\t" \
  "v_smfmac_f32_32x32x32_bf16 v[240:255], v[176:179], v[208:215], %[idx]\n\t" "v_exp_f32 v218, v218\n\t" "v_exp_f32 v219, v219\n\t" "v_exp_f32 v220, v220\n\t" \
  "v_exp_f32 v221, v221\n\t" "v_exp_f32 v222, v222\n\t" "v_exp_f32 v223, v223\n\t" "v_exp_f32 v224, v224\n\t" "v_exp_f32 v225, v225\n\t" \
  "v_smfmac_f32_32x32x32_bf16 v[240:255], v[180:183], v[216:223], %[idx]\n\t" "v_exp_f32 v226, v226\n\t" "v_exp_f32 v227, v227\n\t" "v_exp_f32 v228, v228\n\t" \
  "s_waitcnt vmcnt(0) lgkmcnt(0)\n\t" "v_mfma_f32_32x32x16_bf16 v[208:223], v[96:99], %[q], v[192:207]\n\t" "v_permlane16_swap_b32 v184, v188\n\t" \
  "v_permlane16_swap_b32 v185, v189\n\t" "v_permlane16_swap_b32 v186, v190\n\t" "v_permlane16_swap_b32 v187, v191\n\t" "v_exp_f32 v229, v229\n\t" \
  "v_exp_f32 v230, v230\n\t" "v_exp_f32 v231, v231\n\t" "v_exp_f32 v232, v232\n\t" "v_exp_f32 v233, v233\n\t" \
  "v_smfmac_f32_32x32x32_bf16 v[240:255], v[184:187], v[224:231], %[idx]\n\t" "v_exp_f32 v234, v234\n\t" "v_exp_f32 v235, v235\n\t" "v_exp_f32 v236, v236\n\t" \
  "v_exp_f32 v237, v237\n\t" "v_exp_f32 v238, v238\n\t" "v_exp_f32 v239, v239\n\t" "v_exp_f32 v208, v208\n\t" "v_exp_f32 v209, v209\n\t" \
  "v_smfmac_f32_32x32x32_bf16 v[240:255], v[188:191], v[232:239], %[idx]\n\t" "v_exp_f32 v210, v210\n\t" "v_exp_f32 v211, v211\n\t" "v_exp_f32 v212, v212\n\t" \
  "v_mfma_f32_32x32x16_bf16 v[224:239], v[100:103], %[q], v[192:207]\n\t" "v_exp_f32 v213, v213\n\t" "v_exp_f32 v214, v214\n\t" "v_exp_f32 v215, v215\n\t" \
  "v_exp_f32 v216, v216\n\t" "v_exp_f32 v217, v217\n\t" "v_exp_f32 v218, v218\n\t" "v_exp_f32 v219, v219\n\t" "v_exp_f32 v220, v220\n\t" ""
#define PASSN_ASM \
  "v_mfma_f32_32x32x16_bf16 v[240:255], %[z], %[z], 0\n\t" "v_exp_f32 v221, v221\n\t" "v_exp_f32 v222, v222\n\t" "v_exp_f32 v223, v223\n\t" \
  "v_smfmac_f32_32x32x32_bf16 v[240:255], v[128:131], v[208:215], %[idx]\n\t" "v_exp_f32 v224, v224\n\t" "v_exp_f32 v225, v225\n\t" \
  "v_smfmac_f32_32x32x32_bf16 v[240:255], v[132:135], v[216:223], %[idx]\n\t" "v_exp_f32 v226, v226\n\t" "v_exp_f32 v227, v227\n\t" "v_exp_f32 v228, v228\n\t" \
  "v_mfma_f32_32x32x16_bf16 v[208:223], v[104:107], %[q], v[192:207]\n\t" "v_exp_f32 v229, v229\n\t" "v_exp_f32 v230, v230\n\t" "v_exp_f32 v231, v231\n\t" \
  "v_exp_f32 v232, v232\n\t" "v_exp_f32 v233, v233\n\t" "v_smfmac_f32_32x32x32_bf16 v[240:255], v[136:139], v[224:231], %[idx]\n\t" "v_exp_f32 v234, v234\n\t" \
  "v_exp_f32 v235, v235\n\t" "v_exp_f32 v236, v236\n\t" "v_exp_f32 v237, v237\n\t" "v_exp_f32 v238, v238\n\t" "v_exp_f32 v239, v239\n\t" "v_exp_f32 v208, v208\n\t" \
  "v_exp_f32 v209, v209\n\t" "v_smfmac_f32_32x32x32_bf16 v[240:255], v[140:143], v[232:239], %[idx]\n\t" "v_exp_f32 v210, v210\n\t" "v_exp_f32 v211, v211\n\t" \
  "v_exp_f32 v212, v212\n\t" "v_mfma_f32_32x32x16_bf16 v[224:239], v[108:111], %[q], v[192:207]\n\t" "v_exp_f32 v213, v213\n\t" "v_exp_f32 v214, v214\n\t" \
  "v_exp_f32 v215, v215\n\t" "v_exp_f32 v216, v216\n\t" "v_exp_f32 v217, v217\n\t" "v_smfmac_f32_32x32x32_bf16 v[240:255], v[144:147], v[208:215], %[idx]\n\t" \
  "v_exp_f32 v218, v218\n\t" "v_exp_f32 v219, v219\n\t" "v_exp_f32 v220, v220\n\t" "v_exp_f32 v221, v221\n\t" "v_exp_f32 v222, v222\n\t" "v_exp_f32 v223, v223\n\t" \
  "v_exp_f32 v224, v224\n\t" "v_exp_f32 v225, v225\n\t" "v_smfmac_f32_32x32x32_bf16 v[240:255], v[148:151], v[216:223], %[idx]\n\t" "v_exp_f32 v226, v226\n\t" \
  "v_exp_f32 v227, v227\n\t" "v_exp_f32 v228, v228\n\t" "v_mfma_f32_32x32x16_bf16 v[208:223], v[112:115], %[q], v[192:207]\n\t" "v_exp_f32 v229, v229\n\t" \
  "v_exp_f32 v230, v230\n\t" "v_exp_f32 v231, v231\n\t" "v_exp_f32 v232, v232\n\t" "v_exp_f32 v233, v233\n\t" \
  "v_smfmac_f32_32x32x32_bf16 v[240:255], v[152:155], v[224:231], %[idx]\n\t" "v_exp_f32 v234, v234\n\t" "v_exp_f32 v235, v235\n\t" "v_exp_f32 v236, v236\n\t" \
  "v_exp_f32 v237, v237\n\t" "v_exp_f32 v238, v238\n\t" "v_exp_f32 v239, v239\n\t" "v_exp_f32 v208, v208\n\t" "v_exp_f32 v209, v209\n\t" \
  "v_smfmac_f32_32x32x32_bf16 v[240:255], v[156:159], v[232:239], %[idx]\n\t" "v_exp_f32 v210, v210\n\t" "v_exp_f32 v211, v211\n\t" "v_exp_f32 v212, v212\n\t" \
  "v_mfma_f32_32x32x16_bf16 v[224:239], v[116:119], %[q], v[192:207]\n\t" "v_exp_f32 v213, v213\n\t" "v_exp_f32 v214, v214\n\t" "v_exp_f32 v215, v215\n\t" \
  "v_exp_f32 v216, v216\n\t" "v_exp_f32 v217, v217\n\t" "v_smfmac_f32_32x32x32_bf16 v[240:255], v[160:163], v[208:215], %[idx]\n\t" "v_exp_f32 v218, v218\n\t" \
  "v_exp_f32 v219, v219\n\t" "v_exp_f32 v220, v220\n\t" "v_exp_f32 v221, v221\n\t" "v_exp_f32 v222, v222\n\t" "v_exp_f32 v223, v223\n\t" "v_exp_f32 v224, v224\n\t" \
  "v_exp_f32 v225, v225\n\t" "v_smfmac_f32_32x32x32_bf16 v[240:255], v[164:167], v[216:223], %[idx]\n\t" "v_exp_f32 v226, v226\n\t" "v_exp_f32 v227, v227\n\t" \
  "v_exp_f32 v228, v228\n\t" "v_mfma_f32_32x32x16_bf16 v[208:223], v[120:123], %[q], v[192:207]\n\t" "v_exp_f32 v229, v229\n\t" "v_exp_f32 v230, v230\n\t" \
  "v_exp_f32 v231, v231\n\t" "v_exp_f32 v232, v232\n\t" "v_exp_f32 v233, v233\n\t" "v_smfmac_f32_32x32x32_bf16 v[240:255], v[168:171], v[224:231], %[idx]\n\t" \
  "v_exp_f32 v234, v234\n\t" "v_exp_f32 v235, v235\n\t" "v_exp_f32 v236, v236\n\t" "v_exp_f32 v237, v237\n\t" "v_exp_f32 v238, v238\n\t" "v_exp_f32 v239, v239\n\t" \
  "v_exp_f32 v208, v208\n\t" "v_exp_f32 v209, v209\n\t" "v_smfmac_f32_32x32x32_bf16 v[240:255], v[172:175], v[232:239], %[idx]\n\t" "v_exp_f32 v210, v210\n\t" \
  "v_exp_f32 v211, v211\n\t" "v_exp_f32 v212, v212\n\t" "v_mfma_f32_32x32x16_bf16 v[224:239], v[124:127], %[q], v[192:207]\n\t" "ds_read_b128 %[q], %[qa]\n\t" \
  "v_exp_f32 v213, v213\n\t" "v_exp_f32 v214, v214\n\t" "v_exp_f32 v215, v215\n\t" "v_exp_f32 v216, v216\n\t" "v_exp_f32 v217, v217\n\t" \
  "v_smfmac_f32_32x32x32_bf16 v[240:255], v[176:179], v[208:215], %[idx]\n\t" "v_exp_f32 v218, v218\n\t" "v_exp_f32 v219, v219\n\t" "v_exp_f32 v220, v220\n\t" \
  "v_exp_f32 v221, v221\n\t" "v_exp_f32 v222, v222\n\t" "v_exp_f32 v223, v223\n\t" "v_exp_f32 v224, v224\n\t" "v_exp_f32 v225, v225\n\t" \
  "v_smfmac_f32_32x32x32_bf16 v[240:255], v[180:183], v[216:223], %[idx]\n\t" "v_exp_f32 v226, v226\n\t" "v_exp_f32 v227, v227\n\t" "v_exp_f32 v228, v228\n\t" \
  "s_waitcnt lgkmcnt(0)\n\t" "v_mfma_f32_32x32x16_bf16 v[208:223], v[96:99], %[q], v[192:207]\n\t" "v_exp_f32 v229, v229\n\t" "v_exp_f32 v230, v230\n\t" \
  "v_exp_f32 v231, v231\n\t" "v_exp_f32 v232, v232\n\t" "v_exp_f32 v233, v233\n\t" "v_smfmac_f32_32x32x32_bf16 v[240:255], v[184:187], v[224:231], %[idx]\n\t" \
  "v_exp_f32 v234, v234\n\t" "v_exp_f32 v235, v235\n\t" "v_exp_f32 v236, v236\n\t" "v_exp_f32 v237, v237\n\t" "v_exp_f32 v238, v238\n\t" "v_exp_f32 v239, v239\n\t" \
  "v_exp_f32 v208, v208\n\t" "v_exp_f32 v209, v209\n\t" "v_smfmac_f32_32x32x32_bf16 v[240:255], v[188:191], v[232:239], %[idx]\n\t" "v_exp_f32 v210, v210\n\t" \
  "v_exp_f32 v211, v211\n\t" "v_exp_f32 v212, v212\n\t" "v_mfma_f32_32x32x16_bf16 v[224:239], v[100:103], %[q], v[192:207]\n\t" "v_exp_f32 v213, v213\n\t" \
  "v_exp_f32 v214, v214\n\t" "v_exp_f32 v215, v215\n\t" "v_exp_f32 v216, v216\n\t" "v_exp_f32 v217, v217\n\t" "v_exp_f32 v218, v218\n\t" "v_exp_f32 v219, v219\n\t" \
  "v_exp_f32 v220, v220\n\t" ""
#define ROWMAX_ASM \
  "s_nop 1\n\t" "v_mfma_f32_32x32x16_bf16 v[224:239], v[96:99], %[q], 0\n\t" "s_nop 11\n\t" "v_max3_f32 %[m], %[m], v224, v225\n\t" \
  "v_max3_f32 %[m], %[m], v226, v227\n\t" "v_max3_f32 %[m], %[m], v228, v229\n\t" "v_max3_f32 %[m], %[m], v230, v231\n\t" "v_max3_f32 %[m], %[m], v232, v233\n\t" \
  "v_max3_f32 %[m], %[m], v234, v235\n\t" "v_max3_f32 %[m], %[m], v236, v237\n\t" "v_max3_f32 %[m], %[m], v238, v239\n\t" \
  "v_mfma_f32_32x32x16_bf16 v[224:239], v[100:103], %[q], 0\n\t" "s_nop 11\n\t" "v_max3_f32 %[m], %[m], v224, v225\n\t" "v_max3_f32 %[m], %[m], v226, v227\n\t" \
  "v_max3_f32 %[m], %[m], v228, v229\n\t" "v_max3_f32 %[m], %[m], v230, v231\n\t" "v_max3_f32 %[m], %[m], v232, v233\n\t" "v_max3_f32 %[m], %[m], v234, v235\n\t" \
  "v_max3_f32 %[m], %[m], v236, v237\n\t" "v_max3_f32 %[m], %[m], v238, v239\n\t" "v_mfma_f32_32x32x16_bf16 v[224:239], v[104:107], %[q], 0\n\t" "s_nop 11\n\t" \
  "v_max3_f32 %[m], %[m], v224, v225\n\t" "v_max3_f32 %[m], %[m], v226, v227\n\t" "v_max3_f32 %[m], %[m], v228, v229\n\t" "v_max3_f32 %[m], %[m], v230, v231\n\t" \
  "v_max3_f32 %[m], %[m], v232, v233\n\t" "v_max3_f32 %[m], %[m], v234, v235\n\t" "v_max3_f32 %[m], %[m], v236, v237\n\t" "v_max3_f32 %[m], %[m], v238, v239\n\t" \
  "v_mfma_f32_32x32x16_bf16 v[224:239], v[108:111], %[q], 0\n\t" "s_nop 11\n\t" "v_max3_f32 %[m], %[m], v224, v225\n\t" "v_max3_f32 %[m], %[m], v226, v227\n\t" \
  "v_max3_f32 %[m], %[m], v228, v229\n\t" "v_max3_f32 %[m], %[m], v230, v231\n\t" "v_max3_f32 %[m], %[m], v232, v233\n\t" "v_max3_f32 %[m], %[m], v234, v235\n\t" \
  "v_max3_f32 %[m], %[m], v236, v237\n\t" "v_max3_f32 %[m], %[m], v238, v239\n\t" "v_mfma_f32_32x32x16_bf16 v[224:239], v[112:115], %[q], 0\n\t" "s_nop 11\n\t" \
  "v_max3_f32 %[m], %[m], v224, v225\n\t" "v_max3_f32 %[m], %[m], v226, v227\n\t" "v_max3_f32 %[m], %[m], v228, v229\n\t" "v_max3_f32 %[m], %[m], v230, v231\n\t" \
  "v_max3_f32 %[m], %[m], v232, v233\n\t" "v_max3_f32 %[m], %[m], v234, v235\n\t" "v_max3_f32 %[m], %[m], v236, v237\n\t" "v_max3_f32 %[m], %[m], v238, v239\n\t" \
  "v_mfma_f32_32x32x16_bf16 v[224:239], v[116:119], %[q], 0\n\t" "s_nop 11\n\t" "v_max3_f32 %[m], %[m], v224, v225\n\t" "v_max3_f32 %[m], %[m], v226, v227\n\t" \
  "v_max3_f32 %[m], %[m], v228, v229\n\t" "v_max3_f32 %[m], %[m], v230, v231\n\t" "v_max3_f32 %[m], %[m], v232, v233\n\t" "v_max3_f32 %[m], %[m], v234, v235\n\t" \
  "v_max3_f32 %[m], %[m], v236, v237\n\t" "v_max3_f32 %[m], %[m], v238, v239\n\t" "v_mfma_f32_32x32x16_bf16 v[224:239], v[120:123], %[q], 0\n\t" "s_nop 11\n\t" \
  "v_max3_f32 %[m], %[m], v224, v225\n\t" "v_max3_f32 %[m], %[m], v226, v227\n\t" "v_max3_f32 %[m], %[m], v228, v229\n\t" "v_max3_f32 %[m], %[m], v230, v231\n\t" \
  "v_max3_f32 %[m], %[m], v232, v233\n\t" "v_max3_f32 %[m], %[m], v234, v235\n\t" "v_max3_f32 %[m], %[m], v236, v237\n\t" "v_max3_f32 %[m], %[m], v238, v239\n\t" \
  "v_mfma_f32_32x32x16_bf16 v[224:239], v[124:127], %[q], 0\n\t" "s_nop 11\n\t" "v_max3_f32 %[m], %[m], v224, v225\n\t" "v_max3_f32 %[m], %[m], v226, v227\n\t" \
  "v_max3_f32 %[m], %[m], v228, v229\n\t" "v_max3_f32 %[m], %[m], v230, v231\n\t" "v_max3_f32 %[m], %[m], v232, v233\n\t" "v_max3_f32 %[m], %[m], v234, v235\n\t" \
  "v_max3_f32 %[m], %[m], v236, v237\n\t" "v_max3_f32 %[m], %[m], v238, v239\n\t" ""
#define POST_ASM \
  "s_add_u32 %[i], %[i], 1\n\t" "s_cmp_eq_u32 %[i], 8\n\t" "s_cbranch_scc1 .Lfb_out%=\n\t" "v_cndmask_b32 %[t1], %[sh], v248, %[lo]\n\t" \
  "v_add_u32 %[t2], %[so], %[l16]\n\t" "ds_write_b128 %[t2], v[240:243]\n\t" "ds_write_b128 %[t2], v[244:247] offset:1024\n\t" "v_add_u32 %[t2], %[so], %[l4]\n\t" \
  "ds_write_b32 %[t2], %[t1] offset:2048\n\t" "s_add_u32 %[so], %[so], 14080\n\t" "s_and_b32 %[so], %[so], 0x1ffff\n\t" "s_add_u32 %[qo], %[qo], 1024\n\t" \
  "s_and_b32 %[qo], %[qo], 0x1fff\n\t" "v_add_u32 %[qa], %[qo], %[qb]\n\t" "s_cmp_lt_u32 %[i], %[stop]\n\t" ""

#define KV_OUT "=&{v[96:127]}"(kv0), "=&{v[128:159]}"(kv1), "=&{v[160:191]}"(kv2)
#define KV_IO "+{v[96:127]}"(kv0), "+{v[128:159]}"(kv1), "+{v[160:191]}"(kv2)
#define KV_IN "{v[96:127]}"(kv0), "{v[128:159]}"(kv1), "{v[160:191]}"(kv2)
#define KV_ADDR [ka0] "v"(kb), [ka1] "v"(kb + 4096), [va0] "v"(vp), [va1] "v"(vp + 4096), [rs] "s"(rs)

__global__ __launch_bounds__(512, 2) void fused_block(const u16* __restrict__ x, const u16* __restrict__ wq, const u16* __restrict__ wk,
                                                      const u16* __restrict__ wv, const u16* __restrict__ wo, const u16* __restrict__ wr,
                                                      const u16* __restrict__ wg, const u16* __restrict__ wu, const u16* __restrict__ wd,
                                                      char* ws, u16* __restrict__ out) {
  __shared__ __attribute__((aligned(16))) char smem[L_TOTAL];
  char* const XI = smem + L_XI;
  char* const WI = smem + L_WI;
  char* const KI = smem + L_KI;
  char* const CTL = smem + L_CTL;
  const int tid = threadIdx.x, lane = tid & 63;
  const int w = __builtin_amdgcn_readfirstlane(tid >> 6);
  const int b = blockIdx.x, g8 = b & 7, tb = b & 31, hg = b >> 5;
  const int rr = lane >> 3, c = lane & 7;
  const int h = lane >> 5, l31 = lane & 31;
  const int isV = hg & 1, lr0 = (hg >> 1) * 16;
  const int pid = tb * 8 + hg;
  const int u = (b & 7) * 28 + (b >> 3);
  const rsrc_t rs = __builtin_amdgcn_make_buffer_rsrc(ws, 0, (int)WS_END, 0x00020000);
  STAMP(0, 0);

  unsigned tag;
  asm volatile("s_load_dword %0, %1, 0x0" : "=s"(tag) : "s"(ws) : "memory");
  const bool kvw = (w == (hg & 6));
  const bool ldw = (w == (hg | 1));
  u32x4 xo0, xo1, wr0, wr1, xe, xv[8];
  f32x32 wov;
  unsigned pf0;
  {
    const u16* p = x + (size_t)(tb * 64 + lr0 + 2 * w) * D_;
    GLD128(xo0, h * 1024 + l31 * 16, p);
    GLD128(xo1, h * 1024 + l31 * 16 + 512, p);
  }
#pragma unroll
  for (int i = 0; i < 2; ++i) glds16((isV ? wv : wk) + (w * 2 + i) * 512, lane * 16, KI + (w * 2 + i) * 1024);
  {
    unsigned long long pc; unsigned len;
    asm volatile("s_getpc_b64 %0\n.Lfb_begin:\n\ts_mov_b32 %1, .Lfb_end-.Lfb_begin" : "=s"(pc), "=s"(len));
    unsigned long long ta = pc + min((unsigned)(b >> 3) * 1280u + (unsigned)lane * 128u, len);
    if (lane == 10) ta = (unsigned long long)(out + (size_t)tb * 64 * D_);
    if (lane > 10) ta = (unsigned long long)ws + (lane == 11 ? WS_YP + b * 5376 : (lane == 12 ? WS_K : WS_KVF));
    asm volatile("global_load_dword %0, %1, off" : "=v"(pf0) : "v"(ta) : "memory");
  }
  GLD128(wr0, l31 * 16, wr);
  GLD128(wr1, l31 * 16 + 512, wr);
  const unsigned row_off = rr * 1024 + c * 16;
  const unsigned wq_off = rr * 1024 + ((c ^ (((rr >> 1) & 1) << 2)) << 4);
  const u16* xrows = x + (size_t)(tb * 64 + w * 8) * D_;
  if (kvw) {
    __builtin_amdgcn_s_setprio(3);
#pragma unroll
    for (int j = 0; j < 8; ++j)
      st128(rs, WS_O + (unsigned)((tb * 2 + (j >> 2)) * 32 + hg * 4 + (j & 3)) * 1024 + lane * 16, u32x4{0xffffffffu, 0xffffffffu, 0xffffffffu, 0xffffffffu});
  } else if (ldw) {
#pragma unroll
    for (int pc = 0; pc < 8; ++pc) glds16(wq + pc * 8 * D_ + hg * 64, wq_off, WI + pc * 1024);
  } else {
#pragma unroll
    for (int i = 0; i < 8; ++i) asm volatile("global_load_dwordx4 %0, %1, %2 offset:%3" : "=v"(xv[i]) : "v"(row_off), "s"(xrows), "i"(i * 128) : "memory");
  }
  VMWAIT(13);
  asm volatile("" : "+v"(xo0), "+v"(xo1));
  STAMP(0, 1);
  {
    float s = 0.f;
#pragma unroll
    for (int j = 0; j < 4; ++j) { s = dot2(xo0[j], 0x3f803f80u, s); s = dot2(xo1[j], 0x3f803f80u, s); }
    const float mean = sum32(s) * (1.0f / 512.0f);
    float v = 0.f;
#pragma unroll
    for (int j = 0; j < 4; ++j) {
      const float a0 = bf_lo(xo0[j]) - mean, a1 = bf_hi(xo0[j]) - mean, a2 = bf_lo(xo1[j]) - mean, a3 = bf_hi(xo1[j]) - mean;
      v = fmaf(a0, a0, v); v = fmaf(a1, a1, v); v = fmaf(a2, a2, v); v = fmaf(a3, a3, v);
    }
    const float mb = rbf(mean), sd = ln_sd(sum32(v) * (1.0f / 512.0f)), inv = __builtin_amdgcn_rcpf(sd);
#pragma unroll
    for (int j = 0; j < 4; ++j) { xo0[j] = ln_pair(xo0[j], mb, inv); xo1[j] = ln_pair(xo1[j], mb, inv); }
    const int lrow = lr0 + 2 * w + h;
    char* xr = XI + lrow * 1024 + ((l31 ^ (lrow & 15)) << 4);
    *(u32x4*)xr = xo0; *(u32x4*)(xr + 512) = xo1;
    if (l31 == 0) *(f32x2*)(CTL + C_STAT + lrow * 8) = f32x2{mb, sd};
    if (tid < 2) *(unsigned*)(CTL + tid * 4) = 0u;
    VMWAIT(11);
    asm volatile("s_waitcnt lgkmcnt(0)" : "+s"(tag) :: "memory");
    __builtin_amdgcn_s_barrier();
    CBAR();
  }
  tag += 1u;
  STAMP(0, 2);
  if (kvw) {
    const int g = lane >> 4, q = (lane & 15) >> 2, p = lane & 3;
    const char* xb = XI + (lr0 + (lane & 15)) * 1024;
    const char* wb = opq(KI + (8 * g + q) * 32 + p * 8);
    f32x4 acc = {}, act = {};
    bf16x8 fx[2][4], fw[2][4];
#define KV_LD(bf, k0) _Pragma("unroll") for (int i = 0; i < 4; ++i) { const int ks = (k0) + i; \
      fx[bf][i] = *(const bf16x8*)(xb + ((((4 * i + g) ^ (lane & 15)) << 4) + (ks >> 2) * 256)); \
      fw[bf][i] = tr_frag(wb + ks * 1024, wb + ks * 1024 + 128); }
    KV_LD(0, 0)
#pragma unroll
    for (int kb = 0; kb < 4; ++kb) {
      if (kb < 3) { KV_LD((kb + 1) & 1, 4 * kb + 4) }
      SB();
#pragma unroll
      for (int i = 0; i < 4; ++i) {
        acc = __builtin_amdgcn_mfma_f32_16x16x32_bf16(fw[kb & 1][i], fx[kb & 1][i], acc, 0, 0, 0);
        act = __builtin_amdgcn_mfma_f32_16x16x32_bf16(fx[kb & 1][i], fw[kb & 1][i], act, 0, 0, 0);
      }
      SB();
    }
    if (isV) acc = act;
    const u32x2 pk = {pack_bf(acc[0], acc[1]), pack_bf(acc[2], acc[3])};
    float n = 0.f;
    if (!isV) {
      st64(rs, WS_K + (unsigned)(tb * 64 + lr0 + (lane & 15)) * 32 + (((g & 1) << 1) | (g >> 1)) * 8, pk);
      n = wave_maxpos(add32(add16(acc[0] * acc[0] + acc[1] * acc[1] + acc[2] * acc[2] + acc[3] * acc[3])));
    } else {
      st64(rs, WS_V + (unsigned)(pid - 1 + (g >> 1)) * VHALF + (lane & 15) * 16 + (g & 1) * 8, pk);
    }
    DRAIN();
    if (lane < 8) st64(rs, WS_KVF + (unsigned)(lane * 256 + pid) * 8, u32x2{__float_as_uint(n), tag});
    __builtin_amdgcn_s_setprio(0);
    STAMP(0, 6);
  } else if (ldw) {
#pragma unroll 4
    for (int pc = 8; pc < 28; ++pc) glds16(wq + pc * 8 * D_ + hg * 64, wq_off, WI + pc * 1024);
  } else {
    const int w6 = w - (w > hg ? 2 : 0);
#pragma unroll
    for (int i = 0; i < 6; ++i) { const int pc = 28 + i * 6 + w6; glds16(wq + pc * 8 * D_ + hg * 64, wq_off, WI + pc * 1024); }
    asm volatile("s_waitcnt vmcnt(6)" : "+v"(xv[0]), "+v"(xv[1]), "+v"(xv[2]), "+v"(xv[3]), "+v"(xv[4]), "+v"(xv[5]), "+v"(xv[6]), "+v"(xv[7]) :: "memory");
    float s = 0.f;
#pragma unroll
    for (int i = 0; i < 8; ++i)
#pragma unroll
      for (int j = 0; j < 4; ++j) s = dot2(xv[i][j], 0x3f803f80u, s);
    const float mean = sum8(s) * (1.0f / 512.0f);
    float v = 0.f;
#pragma unroll
    for (int i = 0; i < 8; ++i)
#pragma unroll
      for (int j = 0; j < 4; ++j) { const float a = bf_lo(xv[i][j]) - mean, bb = bf_hi(xv[i][j]) - mean; v = fmaf(a, a, v); v = fmaf(bb, bb, v); }
    const float st_m = rbf(mean), st_s = ln_sd(sum8(v) * (1.0f / 512.0f));
    const float inv = __builtin_amdgcn_rcpf(st_s), nb = -st_m * inv;
    const int lrow = w * 8 + rr;
#pragma unroll
    for (int i = 0; i < 8; ++i) {
      u32x4 o;
#pragma unroll
      for (int j = 0; j < 4; ++j) o[j] = pack_bf(fmaf(bf_lo(xv[i][j]), inv, nb), fmaf(bf_hi(xv[i][j]), inv, nb));
      *(u32x4*)(XI + lrow * 1024 + (((i * 8 + c) ^ (lrow & 15)) << 4)) = o;
    }
    if (c == 0) *(f32x2*)(CTL + C_STAT + lrow * 8) = f32x2{st_m, st_s};
    STAMP(0, 3);
  }
  VMWAIT(28);
  asm volatile("" : "+v"(pf0), "+v"(wr0), "+v"(wr1));
  if ((w >> 2) == isV) {
    float r = 0.f;
#pragma unroll
    for (int j = 0; j < 4; ++j) { r = dot2(xo0[j], wr0[j], r); r = dot2(xo1[j], wr1[j], r); }
    const unsigned rkey = make_key(rbf(sum32(r)), pid * 8 + 2 * (w & 3) + h);
    if (l31 == 0) *(unsigned*)(CTL + C_R + (2 * (w & 3) + h) * 4) = rkey;
  }
  DRAIN();
  RAW_BARRIER();
  STAMP(0, 4);
  if (w == 4) {
    const unsigned key = lane < 8 ? *(const unsigned*)(CTL + C_R + lane * 4) : 0u;
    const unsigned k1 = wave_maxu(key), k2 = wave_maxu(key == k1 ? 0u : key);
    if (lane < 8) st128(rs, WS_CAND + (unsigned)(lane * 256 + pid) * 16, u32x4{k1, tag, k2, tag});
  }
  const int blk = (lane >> 4) & 1, q4 = (lane & 15) >> 2, p4 = lane & 3;
  if (w < 4) {
    const int nt = w & 1, tt = w >> 1;
    const int tok = tt * 32 + l31;
    const char* xb = XI + tok * 1024;
    const char* wb = WI + (8 * h + q4) * 128 + (((4 * nt + 2 * blk + (p4 >> 1)) ^ (((q4 >> 1) & 1) << 2)) << 4) + 8 * (p4 & 1);
    f32x16 acc = {};
    bf16x8 fa[2][4], fb[2][4];
#define Q_LD(bf, k0) _Pragma("unroll") for (int i = 0; i < 4; ++i) { const int ks = (k0) + i; \
      fa[bf][i] = tr_frag(wb + ks * 2048, wb + ks * 2048 + 512); \
      fb[bf][i] = *(const bf16x8*)(xb + ((((2 * (ks & 7) + h) ^ (tok & 15)) << 4) + (ks >> 3) * 256)); }
    Q_LD(0, 0)
    SB();
#pragma unroll
    for (int kb = 0; kb < 8; ++kb) {
      if (kb < 7) { Q_LD((kb + 1) & 1, 4 * kb + 4) }
#pragma unroll
      for (int i = 0; i < 4; ++i) acc = __builtin_amdgcn_mfma_f32_32x32x16_bf16(fa[kb & 1][i], fb[kb & 1][i], acc, 0, 0, 0);
#pragma unroll
      for (int i = 0; i < 4; ++i) { SGB(0x8, 1); SGB(0x100, 3); }
      SB();
    }
    const float cs = 0.25f * 1.4426950408889634f;
    float n0 = 0.f, n1 = 0.f;
#pragma unroll
    for (int r = 0; r < 8; ++r) { acc[r] *= cs; acc[r + 8] *= cs; n0 = fmaf(acc[r], acc[r], n0); n1 = fmaf(acc[r + 8], acc[r + 8], n1); }
    char* qi = smem + L_QI + (tt * 4 + nt * 2) * 1024 + lane * 16;
    *(u32x4*)qi = u32x4{pack_bf(acc[0], acc[1]), pack_bf(acc[2], acc[3]), pack_bf(acc[4], acc[5]), pack_bf(acc[6], acc[7])};
    *(u32x4*)(qi + 1024) = u32x4{pack_bf(acc[8], acc[9]), pack_bf(acc[10], acc[11]), pack_bf(acc[12], acc[13]), pack_bf(acc[14], acc[15])};
    const float nm = wave_maxpos(fmaxf(add32(n0), add32(n1)));
    if (lane == 0) {
      *(float*)(CTL + C_QMAX + w * 4) = nm;
      asm volatile("s_waitcnt lgkmcnt(0)\n\tds_add_u32 %0, %1" :: "v"((unsigned)(uintptr_t)(LAS char*)(CTL + C_CNT)), "v"(1u) : "memory");
    }
  }
  STAMP(0, 5);

  f32x32 kv0, kv1, kv2;
  asm volatile(VCONST_ASM : KV_OUT : [cst] "v"((lane & 15) ? 0u : 0x3f803f80u));
  float kmax;
  {
    u32x2 f;
    SPIN {
      f = ld64(rs, WS_KVF + (unsigned)(g8 * 256 + w * 32 + l31) * 8);
      if (__all(f[1] == tag)) break;
      __builtin_amdgcn_s_sleep(1);
    }
    CBAR();
    kmax = __uint_as_float(f[0]);
  }
  STAMP(1, 0);
  {
    const unsigned kb = WS_K + (unsigned)(w * 256 + l31) * 32 + h * 16;
    const unsigned vp = WS_V + (unsigned)(w * 32 + 2 * blk + h) * VHALF + (lane & 15) * 16;
    asm volatile(LOADS_ASM : KV_IO : KV_ADDR : "memory");
  }
  kmax = wave_maxpos(kmax);
  lds_wait_ge(CTL + C_CNT, 4u);
  STAMP(1, 1);
  const f32x4 qn = *(const f32x4*)(CTL + C_QMAX);
  const float qmax = fmaxf(fmaxf(qn[0], qn[1]), fmaxf(qn[2], qn[3]));
  const float bound = __builtin_amdgcn_sqrtf(qmax * kmax) * 1.02f + 0.01f;
  int fasti = __builtin_amdgcn_readfirstlane(bound <= 48.0f ? 1 : 0);
  asm volatile("" : "+s"(fasti));
  const bool fast = fasti != 0;
  const int idx = (int)0xDDDDDDDDu;
  bf16x8 zf = {};
  asm volatile("" : "+v"(zf));
  const f32x16 z16 = {};
  const char* qbase = smem + L_QI + lane * 16;
  f32x16 o;
  float shift = bound;
  bf16x8 qf = *(const bf16x8*)(qbase + ((w + 1) & 7) * 1024);
  const unsigned lds0 = (unsigned)(uintptr_t)(LAS char*)smem;
  unsigned qa = lds0 + L_QI + lane * 16 + ((w + 2) & 7) * 1024;
  f32x16 cc = splat16(-bound);
  f32x32 sab;
  int i = 0;
  unsigned so = ((w + 1) & 7) * PREG + 6 * PSLOT, qo = ((w + 2) & 7) * 1024; float t1_; unsigned t2_;
#define RUN_OUT "=&{v[240:255]}"(o), "+{v[192:207]}"(cc), [q] "+v"(qf), [qa] "+v"(qa), [i] "+s"(i), [so] "+s"(so), [qo] "+s"(qo), [t1] "=&v"(t1_), [t2] "=&v"(t2_)
#define RUN_IN [z] "v"(zf), [idx] "v"(idx), [sh] "v"(shift), [lo] "s"(0xffffffffull), [l16] "v"(lds0 + lane * 16), [l4] "v"(lds0 + lane * 4), \
               [qb] "v"(lds0 + L_QI + lane * 16)
  STAMP(3, 0);
  asm volatile("s_cmp_eq_u32 %[fast], 0\n\ts_cbranch_scc1 .Lfb_slow0\n\t" PROLOG0_ASM PASS0_ASM POST_ASM
               "s_branch .Lfb_out%=\n.Lfb_slow0:\n\ts_waitcnt vmcnt(0)\n\t" SWAPS_ASM "\n.Lfb_out%=:"
               : "=&{v[208:239]}"(sab), RUN_OUT, KV_IO : RUN_IN, [fast] "s"(fasti), [stop] "s"(0) : "scc", "memory");

  if (w == 0) {
        int lane = tid; asm volatile("" : "+v"(lane));
        unsigned m1 = 0, m2 = 0;
        SPIN {
          bool ok = true; m1 = 0; m2 = 0;
#pragma unroll
          for (int j = 0; j < 4; ++j) {
            const u32x4 cd = ld128(rs, WS_CAND + (unsigned)(g8 * 256 + j * 64 + lane) * 16);
            ok = ok && cd[1] == tag && cd[3] == tag;
            m2 = max(max(m2, cd[2]), min(m1, cd[0])); m1 = max(m1, cd[0]);
          }
          if (__all(ok)) break;
          __builtin_amdgcn_s_sleep(1);
        }
        CBAR();
        const unsigned k1 = __builtin_amdgcn_readfirstlane(wave_maxu(m1));
        const unsigned k2 = __builtin_amdgcn_readfirstlane(wave_maxu(m1 == k1 ? m2 : m1));
        if (lane == 0) *(u32x2*)(CTL + C_K1) = u32x2{k1, k2};
        if (b < NU) {
          glds16(x + key_tok(k1) * D_, lane * 16, smem + L_XS);
          glds16(x + key_tok(k2) * D_, lane * 16, smem + L_XS + 1024);
          const unsigned go = (lane >> 3) * (F_ * 2) + (((lane & 7) ^ (((lane >> 4) & 1) << 2)) << 4);
#pragma unroll
          for (int j = 0; j < 16; ++j) glds16((j < 8 ? wg : wu) + ((u & 7) * 64 + (j & 7) * 8) * F_ + (u >> 3) * 64, go, smem + L_GU + j * 1024);
        }
  }
  if (w == 1 && b < NU) {
#pragma unroll
    for (int f = 0; f < 8; ++f) glds16(wd + (u * 8 + f) * D_, lane * 16, smem + (f < 6 ? L_DN + f * 1088 : L_DN2 + (f - 6) * 1088));
  }
  GLD128(xe, row_off, xrows + hg * 64);
  asm volatile("global_load_dwordx4 v[64:67], %1, %5\n\tglobal_load_dwordx4 v[68:71], %2, %5\n\tglobal_load_dwordx4 v[72:75], %3, %5\n\t"
               "global_load_dwordx4 v[76:79], %4, %5\n\tglobal_load_dwordx4 v[80:83], %1, %6\n\tglobal_load_dwordx4 v[84:87], %2, %6\n\t"
               "global_load_dwordx4 v[88:91], %3, %6\n\tglobal_load_dwordx4 v[92:95], %4, %6"
               : "=&{v[64:95]}"(wov) : "v"(row_off), "v"(row_off + 8192), "v"(row_off + 16384), "v"(row_off + 24576),
                 "s"(wo + (size_t)(w * 64) * D_ + hg * 64), "s"(wo + (size_t)(w * 64 + 32) * D_ + hg * 64) : "memory");

#pragma nounroll
  while (i < 8) {
    if (b < NU && ((w == 0 && i == 2) || (w == 1 && (i == 4 || i == 5)))) {
      int lane = tid & 63, u = (b & 7) * 28 + (b >> 3);
      asm volatile("" : "+v"(lane), "+s"(u));
      const int h = lane >> 5, l31 = lane & 31, blk = (lane >> 4) & 1, q4 = (lane & 15) >> 2, p4 = lane & 3;
      if (i == 2) {
        VMWAIT(9);
        {
          const u32x4 xs0 = *(const u32x4*)(smem + L_XS + h * 1024 + l31 * 16), xs1 = *(const u32x4*)(smem + L_XS + h * 1024 + 512 + l31 * 16);
          float s = 0.f;
#pragma unroll
          for (int j = 0; j < 4; ++j) { s = dot2(xs0[j], 0x3f803f80u, s); s = dot2(xs1[j], 0x3f803f80u, s); }
          const float mean = sum32(s) * (1.0f / 512.0f);
          float v = 0.f;
#pragma unroll
          for (int j = 0; j < 4; ++j) {
            const float a0 = bf_lo(xs0[j]) - mean, a1 = bf_hi(xs0[j]) - mean, a2 = bf_lo(xs1[j]) - mean, a3 = bf_hi(xs1[j]) - mean;
            v = fmaf(a0, a0, v); v = fmaf(a1, a1, v); v = fmaf(a2, a2, v); v = fmaf(a3, a3, v);
          }
          const float mb = rbf(mean), inv = __builtin_amdgcn_rcpf(ln_sd(sum32(v) * (1.0f / 512.0f)));
          u32x4 o0;
#pragma unroll
          for (int j = 0; j < 4; ++j) o0[j] = ln_pair((u & 4) ? xs1[j] : xs0[j], mb, inv);
          *(u32x4*)(smem + L_XS + h * 1024 + l31 * 16) = o0;
        }
        const char* xb = opq(smem + L_XS + (lane & 1) * 1024 + (u & 3) * 128 + h * 16);
        bf16x8 fx[4];
#pragma unroll
        for (int ks = 0; ks < 4; ++ks) fx[ks] = *(const bf16x8*)(xb + ks * 32);
#pragma unroll
        for (int nt = 0; nt < 2; ++nt) {
          const char* wb = opq(smem + L_GU + (8 * h + q4) * 128 + (((4 * nt + 2 * blk + (p4 >> 1)) ^ (((q4 >> 1) & 1) << 2)) << 4) + 8 * (p4 & 1));
#pragma unroll
          for (int mat = 0; mat < 2; ++mat) {
            f32x16 z = z16;
#pragma unroll
            for (int ks = 0; ks < 4; ++ks) z = QK(fx[ks], tr_frag(wb + mat * 8192 + ks * 2048, wb + mat * 8192 + ks * 2048 + 512), z);
            if (lane < 32) st128(rs, WS_P1 + (unsigned)u * 2048 + (mat * 64 + nt * 32 + lane) * 16, u32x4{__float_as_uint(z[0]), tag, __float_as_uint(z[1]), tag});
            SB();
          }
        }
      } else if (i == 4) {
        const unsigned po = WS_P1 + (unsigned)((u & ~7) + (lane >> 3)) * 2048 + ((u & 7) * 8 + (lane & 7)) * 16;
        u32x4 pg, pu;
        SPIN {
          pg = ld128(rs, po); pu = ld128(rs, po + 1024);
          if (__all(pg[1] == tag && pg[3] == tag && pu[1] == tag && pu[3] == tag)) break;
          __builtin_amdgcn_s_sleep(2);
        }
        DRAIN();
        float t[4] = {__uint_as_float(pg[0]), __uint_as_float(pg[2]), __uint_as_float(pu[0]), __uint_as_float(pu[2])};
#pragma unroll
        for (int j = 0; j < 4; ++j) { t[j] += dpp_f<0x128>(t[j]); t[j] = add32(add16(t[j])); }
        const unsigned hp = pack_bf(rbf(silu_f(rbf(t[0]))) * rbf(t[2]), rbf(silu_f(rbf(t[1]))) * rbf(t[3]));
        if (lane < 8) { *(u16*)(smem + L_HS + lane * 2) = (u16)hp; *(u16*)(smem + L_HS + 16 + lane * 2) = (u16)(hp >> 16); }
        u32x4 ha = *(const u32x4*)(smem + L_HS + (lane & 1) * 16);
        if (lane >= 2) ha = u32x4{0u, 0u, 0u, 0u};
        const bf16x8 A = __builtin_bit_cast(bf16x8, ha);
        const int co = (2 * blk + (p4 >> 1)) * 16 + 8 * (p4 & 1);
        const char* db0 = opq(smem + L_DN + q4 * 1088 + co);
        const char* db1 = opq(smem + (q4 < 2 ? L_DN + (4 + q4) * 1088 : L_DN2 + (q4 - 2) * 1088) + co);
#pragma unroll 2
        for (int ct = 0; ct < 16; ++ct) {
          const bf16x8 bw = tr_frag(db0 + ct * 64, db1 + ct * 64);
          const f32x16 z = QK(A, bw, z16);
          auto sw = __builtin_amdgcn_permlane32_swap(__float_as_uint(z[0]), __float_as_uint(z[1]), false, false);
          st32(rs, WS_YP + (unsigned)u * 4096 + h * 2048 + (ct * 32 + l31) * 4, sw[0]);
        }
      } else {
        DRAIN();
        if (lane < 8) st32(rs, WS_YF + lane * 1024 + u * 4, tag);
      }
    }
    STAMP(3, i);
    int stop = 8;
    if (w == 0 && b < NU && i < 2) stop = 2;
    if (w == 1 && b < NU && i < 5) stop = i < 4 ? 4 : 5;
    if (!fast) {
      stop = i + 1;
      float m = -3.0e38f;
      asm volatile(ROWMAX_ASM : [m] "+v"(m), "=&{v[208:239]}"(sab) : [q] "v"(qf), KV_IN);
      shift = max32(m);
      cc = splat16(-shift);
      asm volatile(PROLOG_ASM : "=&{v[208:239]}"(sab), "+{v[192:207]}"(cc) : [q] "v"(qf), KV_IN);
    }
    asm volatile(".Lfb_loop%=:\n\t" PASSN_ASM POST_ASM "s_cbranch_scc1 .Lfb_loop%=\n.Lfb_out%=:"
                 : "+{v[208:239]}"(sab), RUN_OUT : RUN_IN, [stop] "s"(stop), KV_IN : "scc", "memory");
  }
  {
  int tid = threadIdx.x; asm volatile("" : "+v"(tid));
  const int lane = tid & 63, rr = lane >> 3, c = lane & 7, h = lane >> 5, l31 = lane & 31, blk = (lane >> 4) & 1, q4 = (lane & 15) >> 2, p4 = lane & 3;
  if (w < 4 && b >= NU) {
    SPIN {
      const u32x4 f = ld128(rs, WS_YF + g8 * 1024 + min(lane, 55) * 16);
      if (__all(f[0] == tag && f[1] == tag && f[2] == tag && f[3] == tag)) break;
      __builtin_amdgcn_s_sleep(8);
    }
    CBAR();
    const unsigned rw = (unsigned)((b - NU) * 4 + w);
    const unsigned yo = WS_YP + (unsigned)(lane >> 1) * 4096 + rw * 32 + (lane & 1) * 16;
    f32x4 y = {};
#pragma unroll
    for (int j = 0; j < 7; ++j) y += __builtin_bit_cast(f32x4, ld128(rs, yo + j * 32 * 4096));
#pragma unroll
    for (int j = 0; j < 4; ++j) { float t = y[j]; t += dpp_f<0x4E>(t); t += dpp_f<0x124>(t); t += dpp_f<0x128>(t); y[j] = add32(add16(t)); }
    if (lane < 2) {
      st128(rs, WS_YG + rw * 64 + lane * 32, u32x4{__float_as_uint(y[0]), tag, __float_as_uint(y[1]), tag});
      st128(rs, WS_YG + rw * 64 + lane * 32 + 16, u32x4{__float_as_uint(y[2]), tag, __float_as_uint(y[3]), tag});
    }
  }
  STAMP(1, 2);
  RAW_BARRIER();
  STAMP(1, 3);
  const unsigned fr = (unsigned)((tb * 2 + (w >> 2)) * 32 + hg * 4 + (w & 3));
  {
    float a[8], pl[7], pm[7];
    auto sw = __builtin_amdgcn_permlane32_swap(__float_as_uint(o[8]), __float_as_uint(o[8]), false, false);
    float l = __uint_as_float(sw[0]);
    float mx = shift;
#pragma unroll
    for (int sidx = 0; sidx < 7; ++sidx) {
      const char* sl = smem + w * PREG + sidx * PSLOT + 2048 + l31 * 4;
      pl[sidx] = *(const float*)sl; pm[sidx] = *(const float*)(sl + 128);
      mx = fmaxf(mx, pm[sidx]);
    }
    const float f0 = __builtin_amdgcn_exp2f(shift - mx);
    l *= f0;
#pragma unroll
    for (int r = 0; r < 8; ++r) a[r] = o[r] * f0;
#pragma unroll
    for (int sidx = 0; sidx < 7; ++sidx) {
      const char* sl = smem + w * PREG + sidx * PSLOT;
      const f32x4 p0 = *(const f32x4*)(sl + lane * 16), p1 = *(const f32x4*)(sl + 1024 + lane * 16);
      const float f = __builtin_amdgcn_exp2f(pm[sidx] - mx);
#pragma unroll
      for (int r = 0; r < 4; ++r) { a[r] = fmaf(p0[r], f, a[r]); a[r + 4] = fmaf(p1[r], f, a[r + 4]); }
      l = fmaf(pl[sidx], f, l);
    }
    const float inv = __builtin_amdgcn_rcpf(l);
    const u32x4 of = {min(pack_bf(a[0] * inv, a[1] * inv), 0xfffffffeu), min(pack_bf(a[2] * inv, a[3] * inv), 0xfffffffeu),
                      min(pack_bf(a[4] * inv, a[5] * inv), 0xfffffffeu), min(pack_bf(a[6] * inv, a[7] * inv), 0xfffffffeu)};
    st128(rs, WS_O + fr * 1024 + lane * 16, of);
    *(u32x4*)(smem + L_QI + w * 1024 + lane * 16) = of;
  }
  asm volatile("s_waitcnt vmcnt(1) lgkmcnt(0)\n\tds_write_b128 %0, v[64:67]\n\tds_write_b128 %0, v[68:71] offset:1024\n\tds_write_b128 %0, v[72:75] offset:2048\n\t"
               "ds_write_b128 %0, v[76:79] offset:3072\n\tds_write_b128 %0, v[80:83] offset:4096\n\tds_write_b128 %0, v[84:87] offset:5120\n\t"
               "ds_write_b128 %0, v[88:91] offset:6144\n\tds_write_b128 %0, v[92:95] offset:7168"
               :: "v"((unsigned)(uintptr_t)(LAS char*)smem + w * PREG + rr * 128 + ((c ^ (((rr >> 1) & 1) << 2)) << 4)), "{v[64:95]}"(wov) : "memory");
  if (b == 0 && tid == 0) st32(rs, WS_EPOCH, tag);
  STAMP(1, 4);
  RAW_BARRIER();
  const int etok = tid >> 3, gtok = tb * 64 + etok;
  float gate[8];
  {
    const int tt = w >> 2, kq = w & 3;
    const unsigned fo = WS_O + (unsigned)((tb * 2 + tt) * 32 + kq * 8) * 1024 + lane * 16;
    u32x4 fb[8];
#pragma unroll
    for (int i = 0; i < 8; ++i) fb[i] = ld128(rs, fo + i * 1024);
    SB();
    bf16x8 fa[2][8];
#pragma unroll
    for (int i = 0; i < 8; ++i) {
      const int piece = 2 * kq + (i >> 2), row0 = (i & 3) * 16 + 4 * h + q4;
#pragma unroll
      for (int nt = 0; nt < 2; ++nt) {
        const char* wb = smem + piece * PREG + row0 * 128 + (((4 * nt + 2 * blk + (p4 >> 1)) ^ (((q4 >> 1) & 1) << 2)) << 4) + 8 * (p4 & 1);
        fa[nt][i] = tr_frag(wb, wb + 8 * 128);
      }
    }
    STAMP(2, 0);
    {
      const unsigned k1 = *(const unsigned*)(CTL + C_K1), k2 = *(const unsigned*)(CTL + C_K2);
      const int sel = (gtok == key_tok(k1)) ? 0 : ((gtok == key_tok(k2)) ? 1 : -1);
      const f32x2 st = *(const f32x2*)(CTL + C_STAT + etok * 8);
      const float inv = __builtin_amdgcn_rcpf(st[1]);
      float upd[8];
#pragma unroll
      for (int j = 0; j < 8; ++j) upd[j] = 0.f;
      if (__any(sel >= 0)) {
        const float rsg = rbf(sigm_f(key_val(sel > 0 ? k2 : k1)));
        const unsigned go = WS_YG + (unsigned)((sel > 0 ? 512 : 0) + hg * 64 + c * 8) * 8;
        SPIN {
          bool ok = true;
#pragma unroll
          for (int j = 0; j < 4; ++j) {
            const u32x4 gq = ld128(rs, go + j * 16);
            ok = ok && gq[1] == tag && gq[3] == tag;
            upd[2 * j] = __uint_as_float(gq[0]); upd[2 * j + 1] = __uint_as_float(gq[2]);
          }
          if (__all(ok)) break;
          __builtin_amdgcn_s_sleep(1);
        }
#pragma unroll
        for (int j = 0; j < 8; ++j) upd[j] = sel >= 0 ? rbf(rbf(upd[j]) * rsg) : 0.f;
      }
#pragma unroll
      for (int j = 0; j < 8; ++j) {
        const float xf = (j & 1) ? bf_hi(xe[j >> 1]) : bf_lo(xe[j >> 1]);
        gate[j] = rbf(silu_f(rbf(rbf(rbf(xf - st[0]) * inv) + upd[j])));
      }
    }
    SB();
    STAMP(2, 1);
    if ((hg >> 1) == kq) {
      if (hg & 1) {
#pragma unroll
        for (int i = 0; i < 4; ++i) fb[4 + i] = *(const u32x4*)(smem + L_QI + (tt * 4 + i) * 1024 + lane * 16);
      } else {
#pragma unroll
        for (int i = 0; i < 4; ++i) fb[i] = *(const u32x4*)(smem + L_QI + (tt * 4 + i) * 1024 + lane * 16);
      }
    }
    SPIN {
      unsigned bad = 0;
#pragma unroll
      for (int i = 0; i < 8; ++i) bad |= __any(max(max(fb[i][0], fb[i][1]), max(fb[i][2], fb[i][3])) == 0xffffffffu) ? 1u << i : 0u;
      if (!bad) break;
      SPIN {
        const unsigned pv = ld32(rs, fo - lane * 16 + (lane & 7) * 1024 + 1020);
        if (!__any(pv == 0xffffffffu && ((bad >> (lane & 7)) & 1u))) break;
        __builtin_amdgcn_s_sleep(1);
      }
#pragma unroll
      for (int i = 0; i < 8; ++i) if ((bad >> i) & 1u) fb[i] = ld128(rs, fo + i * 1024);
    }
    f32x16 acc0 = {}, acc1 = {};
#pragma unroll
    for (int i = 0; i < 8; ++i) {
      acc0 = QK(fa[0][i], __builtin_bit_cast(bf16x8, fb[i]), acc0);
      acc1 = QK(fa[1][i], __builtin_bit_cast(bf16x8, fb[i]), acc1);
    }
    STAMP(2, 2);
    char* pr = smem + w * PREG + 8192 + l31 * 256;
#pragma unroll
    for (int g = 0; g < 4; ++g) {
      *(f32x4*)(pr + (((2 * g + h) ^ (l31 & 7)) << 4)) = f32x4{acc0[4 * g], acc0[4 * g + 1], acc0[4 * g + 2], acc0[4 * g + 3]};
      *(f32x4*)(pr + 128 + (((2 * g + h) ^ (l31 & 7)) << 4)) = f32x4{acc1[4 * g], acc1[4 * g + 1], acc1[4 * g + 2], acc1[4 * g + 3]};
    }
  }
  RAW_BARRIER();
  {
    f32x4 s0v = {}, s1v = {};
#pragma unroll
    for (int kq = 0; kq < 4; ++kq) {
      const char* pr = smem + ((w >> 2) * 4 + kq) * PREG + 8192 + (etok & 31) * 256 + (c >> 2) * 128;
      s0v += *(const f32x4*)(pr + ((((2 * c) & 7) ^ rr) << 4)); s1v += *(const f32x4*)(pr + ((((2 * c + 1) & 7) ^ rr) << 4));
    }
    float res[8];
#pragma unroll
    for (int j = 0; j < 8; ++j) {
      const float xf = (j & 1) ? bf_hi(xe[j >> 1]) : bf_lo(xe[j >> 1]);
      const float sv = rbf(j < 4 ? s0v[j & 3] : s1v[j & 3]);
      res[j] = xf + rbf(sv * gate[j]);
    }
    *(u32x4*)(out + (size_t)gtok * D_ + hg * 64 + c * 8) =
        u32x4{pack_bf(res[0], res[1]), pack_bf(res[2], res[3]), pack_bf(res[4], res[5]), pack_bf(res[6], res[7])};
  }
  }
  asm volatile(".Lfb_end:");
  STAMP(2, 3);
}

extern "C" void kernel_launch(void* const* d_in, const int* in_sizes, int n_in, void* d_out, int out_size, void* d_ws, size_t ws_size,
                              hipStream_t stream) {
  fused_block<<<256, 512, 0, stream>>>((const u16*)d_in[0], (const u16*)d_in[1], (const u16*)d_in[2], (const u16*)d_in[3], (const u16*)d_in[4],
                                       (const u16*)d_in[6], (const u16*)d_in[7], (const u16*)d_in[8], (const u16*)d_in[9], (char*)d_ws, (u16*)d_out);
}
```

```cpp
#include <hip/hip_runtime.h>
#include <hip/hip_bf16.h>
#include <stdint.h>

typedef __attribute__((ext_vector_type(2))) __bf16 bf16x2;
typedef __attribute__((ext_vector_type(8))) __bf16 bf16x8;
typedef __attribute__((ext_vector_type(4))) short s16x4;
typedef __attribute__((ext_vector_type(8))) short s16x8;
typedef __attribute__((ext_vector_type(2))) float f32x2;
typedef __attribute__((ext_vector_type(4))) float f32x4;
typedef __attribute__((ext_vector_type(16))) float f32x16;
typedef __attribute__((ext_vector_type(32))) float f32x32;
typedef __attribute__((ext_vector_type(2))) unsigned u32x2;
typedef __attribute__((ext_vector_type(4))) unsigned u32x4;
typedef unsigned short u16;
typedef __amdgpu_buffer_rsrc_t rsrc_t;
#define LAS __attribute__((address_space(3)))
#define DI __device__ __forceinline__

constexpr int S_ = 2048, D_ = 512, F_ = 1792;
constexpr int NU = 224;
constexpr int VHALF = 256;
constexpr int SC1 = 16;
constexpr unsigned SPIN_MAX = 1u << 18;

constexpr unsigned WS_EPOCH = 0;
constexpr unsigned WS_KVF = 1024;
constexpr unsigned WS_CAND = WS_KVF + 8 * 256 * 8;
constexpr unsigned WS_YF = WS_CAND + 8 * 512 * 8;
constexpr unsigned WS_YG = WS_YF + 8 * 1024;
constexpr unsigned WS_K = WS_YG + 1024 * 8;
constexpr unsigned WS_V = WS_K + S_ * 32;
constexpr unsigned WS_YP = WS_V + 256 * VHALF;
constexpr unsigned WS_P1 = WS_YP + NU * 2 * D_ * 4;
constexpr unsigned WS_O = WS_P1 + NU * 2048;
constexpr unsigned WS_END = WS_O + S_ * D_ * 2;

constexpr int L_WI = 0;
constexpr int L_XI = 65536;
constexpr int L_KI = 131072;
constexpr int PSLOT = 2304;
constexpr int PREG = 16384;
constexpr int L_XS = 7 * PREG;
constexpr int L_GU = 131072;
constexpr int L_QI = 147456;
constexpr int L_CTL = L_QI + 8192;
constexpr int L_DN = L_CTL + 1024;
constexpr int L_DN2 = 7 * PREG + PSLOT + 128;
constexpr int L_HS = 0;
constexpr int L_TOTAL = L_DN + 6 * 1088;
constexpr int C_CNT = 0;
constexpr int C_SEL = 4;
constexpr int C_K1 = 8, C_K2 = 12;
constexpr int C_QMAX = 16;
constexpr int C_R = 32;
constexpr int C_STAT = 64;

#define STAMP(kid, i)

DI float bf_lo(unsigned u) { return __uint_as_float(u << 16); }
DI float bf_hi(unsigned u) { return __uint_as_float(u & 0xffff0000u); }
DI unsigned pack_bf(float a, float b) { f32x2 f = {a, b}; return __builtin_bit_cast(unsigned, __builtin_convertvector(f, bf16x2)); }
DI float rbf(float a) { return (float)(__bf16)a; }
DI float dot2(unsigned a, unsigned b, float c) { return __builtin_amdgcn_fdot2_f32_bf16(__builtin_bit_cast(bf16x2, a), __builtin_bit_cast(bf16x2, b), c, false); }
DI void glds16(const void* sbase, unsigned voff, void* lds_wave_base) {
  unsigned keep; const unsigned dst = (unsigned)(uintptr_t)(LAS char*)lds_wave_base;
  asm volatile("s_mov_b32 %0, m0\n\ts_mov_b32 m0, %2\n\ts_nop 0\n\tglobal_load_lds_dwordx4 %1, %3\n\ts_mov_b32 m0, %0"
               : "=&s"(keep) : "v"(voff), "s"(dst), "s"(sbase) : "memory");
}
#define GLD128(dst, voff, sbase) asm volatile("global_load_dwordx4 %0, %1, %2" : "=v"(dst) : "v"(voff), "s"(sbase) : "memory")
DI const char* opq(const char* p) { unsigned a = (unsigned)(uintptr_t)(LAS char*)p; asm volatile("" : "+v"(a)); return (const char*)(LAS char*)(uintptr_t)a; }
DI s16x4 tr_read(const char* p) { return __builtin_amdgcn_ds_read_tr16_b64_v4i16((LAS s16x4*)p); }
DI bf16x8 tr_frag(const char* p0, const char* p1) {
  s16x4 a = tr_read(p0), b = tr_read(p1);
  s16x8 v = __builtin_shufflevector(a, b, 0, 1, 2, 3, 4, 5, 6, 7);
  return __builtin_bit_cast(bf16x8, v);
}
template <int CTRL> DI float dpp_f(float v) { return __int_as_float(__builtin_amdgcn_update_dpp(0, __float_as_int(v), CTRL, 0xf, 0xf, true)); }
template <int CTRL> DI unsigned dpp_u(unsigned v) { return (unsigned)__builtin_amdgcn_update_dpp(0, (int)v, CTRL, 0xf, 0xf, true); }
DI float sum8(float v) {
  v += dpp_f<0xB1>(v); v += dpp_f<0x4E>(v); v += dpp_f<0x141>(v);
  return v;
}
DI float add16(float v) { auto r = __builtin_amdgcn_permlane16_swap(__float_as_uint(v), __float_as_uint(v), false, false); return __uint_as_float(r[0]) + __uint_as_float(r[1]); }
DI float add32(float v) { auto r = __builtin_amdgcn_permlane32_swap(__float_as_uint(v), __float_as_uint(v), false, false); return __uint_as_float(r[0]) + __uint_as_float(r[1]); }
DI float max16(float v) { auto r = __builtin_amdgcn_permlane16_swap(__float_as_uint(v), __float_as_uint(v), false, false); return fmaxf(__uint_as_float(r[0]), __uint_as_float(r[1])); }
DI float max32(float v) { auto r = __builtin_amdgcn_permlane32_swap(__float_as_uint(v), __float_as_uint(v), false, false); return fmaxf(__uint_as_float(r[0]), __uint_as_float(r[1])); }
DI float sum32(float v) { v = sum8(v); v += dpp_f<0x140>(v); return add16(v); }
DI float wave_sum(float v) { return add32(sum32(v)); }
DI float wave_maxf(float v) {
  v = fmaxf(v, dpp_f<0xB1>(v)); v = fmaxf(v, dpp_f<0x4E>(v)); v = fmaxf(v, dpp_f<0x141>(v)); v = fmaxf(v, dpp_f<0x140>(v));
  return max32(max16(v));
}
DI unsigned wave_maxu(unsigned v) {
  v = max(v, dpp_u<0xB1>(v)); v = max(v, dpp_u<0x4E>(v)); v = max(v, dpp_u<0x141>(v)); v = max(v, dpp_u<0x140>(v));
  auto r = __builtin_amdgcn_permlane16_swap(v, v, false, false); v = max(r[0], r[1]);
  auto q = __builtin_amdgcn_permlane32_swap(v, v, false, false); return max(q[0], q[1]);
}
DI float wave_maxpos(float v) { return __uint_as_float(wave_maxu(__float_as_uint(v))); }
DI float silu_f(float v) { return v * __builtin_amdgcn_rcpf(1.0f + __builtin_amdgcn_exp2f(-1.4426950408889634f * v)); }
DI float sigm_f(float v) { return __builtin_amdgcn_rcpf(1.0f + __builtin_amdgcn_exp2f(-1.4426950408889634f * v)); }
DI float ln_sd(float var) { return rbf(__builtin_amdgcn_sqrtf(rbf(rbf(var) + 1e-5f))); }
DI unsigned ln_pair(unsigned xu, float mb, float inv) {
  const unsigned d = pack_bf(bf_lo(xu) - mb, bf_hi(xu) - mb);
  return pack_bf(bf_lo(d) * inv, bf_hi(d) * inv);
}
DI unsigned silu_pair(unsigned v) { return pack_bf(silu_f(bf_lo(v)), silu_f(bf_hi(v))); }

DI void stats32(const u32x4& a, const u32x4& b2, float& mean, float& var) {
  float s = 0.f, q = 0.f;
#pragma unroll
  for (int j = 0; j < 4; ++j) { s = dot2(a[j], 0x3f803f80u, s); s = dot2(b2[j], 0x3f803f80u, s); q = dot2(a[j], a[j], q); q = dot2(b2[j], b2[j], q); }
  mean = sum32(s) * (1.0f / 512.0f);
  var = fmaf(-mean, mean, sum32(q) * (1.0f / 512.0f));
  if (__builtin_expect(__any(!(var >= mean * mean * 0.015625f)), 0)) {
    float v = 0.f;
#pragma unroll
    for (int j = 0; j < 4; ++j) {
      const float a0 = bf_lo(a[j]) - mean, a1 = bf_hi(a[j]) - mean, a2 = bf_lo(b2[j]) - mean, a3 = bf_hi(b2[j]) - mean;
      v = fmaf(a0, a0, v); v = fmaf(a1, a1, v); v = fmaf(a2, a2, v); v = fmaf(a3, a3, v);
    }
    var = sum32(v) * (1.0f / 512.0f);
  }
}

DI unsigned make_key(float r_bf, int token) {
  unsigned b = __float_as_uint(r_bf) >> 16;
  b = (b & 0x8000u) ? (~b & 0xffffu) : (b | 0x8000u);
  return (b << 16) | (unsigned)(2047 - token);
}
DI float key_val(unsigned key) {
  unsigned b = key >> 16;
  b = (b & 0x8000u) ? (b & 0x7fffu) : (~b & 0xffffu);
  return __uint_as_float(b << 16);
}
DI int key_tok(unsigned key) { return 2047 - (int)(key & 0x7ffu); }

DI u32x4 ld128(rsrc_t rs, unsigned off) { return __builtin_amdgcn_raw_buffer_load_b128(rs, (int)off, 0, SC1); }
DI u32x2 ld64(rsrc_t rs, unsigned off) { return __builtin_amdgcn_raw_buffer_load_b64(rs, (int)off, 0, SC1); }
DI unsigned ld32(rsrc_t rs, unsigned off) { return __builtin_amdgcn_raw_buffer_load_b32(rs, (int)off, 0, SC1); }
DI void st128(rsrc_t rs, unsigned off, u32x4 v) { __builtin_amdgcn_raw_buffer_store_b128(v, rs, (int)off, 0, SC1); }
DI void st64(rsrc_t rs, unsigned off, u32x2 v) { __builtin_amdgcn_raw_buffer_store_b64(v, rs, (int)off, 0, SC1); }
DI void st32(rsrc_t rs, unsigned off, unsigned v) { __builtin_amdgcn_raw_buffer_store_b32(v, rs, (int)off, 0, SC1); }
#define DRAIN() asm volatile("s_waitcnt vmcnt(0)" ::: "memory")
#define VMWAIT(n) asm volatile("s_waitcnt vmcnt(" #n ")" ::: "memory")
#define CBAR() asm volatile("" ::: "memory")
#define RAW_BARRIER() do { asm volatile("s_waitcnt lgkmcnt(0)" ::: "memory"); __builtin_amdgcn_s_barrier(); asm volatile("" ::: "memory"); } while (0)
#define SPIN _Pragma("nounroll") for (unsigned s_ = 0; s_ < SPIN_MAX; ++s_)
DI unsigned lds_ld(const char* p) { return __hip_atomic_load((LAS unsigned*)p, __ATOMIC_RELAXED, __HIP_MEMORY_SCOPE_WORKGROUP); }
DI void lds_wait_ge(const char* p, unsigned want) {
  SPIN {
    if ((unsigned)__builtin_amdgcn_readfirstlane(lds_ld(p)) >= want) break;
    __builtin_amdgcn_s_sleep(1);
  }
  CBAR();
}
#define QK(KF, QF, C) __builtin_amdgcn_mfma_f32_32x32x16_bf16((KF), (QF), (C), 0, 0, 0)
DI f32x16 splat16(float v) { f32x16 r;
#pragma unroll
  for (int i = 0; i < 16; ++i) r[i] = v;
  return r; }
#define SB() __builtin_amdgcn_sched_barrier(0)
#define SGB(mask, n) __builtin_amdgcn_sched_group_barrier(mask, n, 0)

#define VCONST_ASM \
  "v_mov_b32 v132, %[cst]\n\t" "v_mov_b32 v133, %[cst]\n\t" "v_mov_b32 v134, %[cst]\n\t" "v_mov_b32 v135, %[cst]\n\t" "v_mov_b32 v140, %[cst]\n\t" \
  "v_mov_b32 v141, %[cst]\n\t" "v_mov_b32 v142, %[cst]\n\t" "v_mov_b32 v143, %[cst]\n\t" "v_mov_b32 v148, %[cst]\n\t" "v_mov_b32 v149, %[cst]\n\t" \
  "v_mov_b32 v150, %[cst]\n\t" "v_mov_b32 v151, %[cst]\n\t" "v_mov_b32 v156, %[cst]\n\t" "v_mov_b32 v157, %[cst]\n\t" "v_mov_b32 v158, %[cst]\n\t" \
  "v_mov_b32 v159, %[cst]\n\t" "v_mov_b32 v164, %[cst]\n\t" "v_mov_b32 v165, %[cst]\n\t" "v_mov_b32 v166, %[cst]\n\t" "v_mov_b32 v167, %[cst]\n\t" \
  "v_mov_b32 v172, %[cst]\n\t" "v_mov_b32 v173, %[cst]\n\t" "v_mov_b32 v174, %[cst]\n\t" "v_mov_b32 v175, %[cst]\n\t" "v_mov_b32 v180, %[cst]\n\t" \
  "v_mov_b32 v181, %[cst]\n\t" "v_mov_b32 v182, %[cst]\n\t" "v_mov_b32 v183, %[cst]\n\t" "v_mov_b32 v188, %[cst]\n\t" "v_mov_b32 v189, %[cst]\n\t" \
  "v_mov_b32 v190, %[cst]\n\t" "v_mov_b32 v191, %[cst]\n\t" ""
#define LOADS_ASM \
  "buffer_load_dwordx4 v[96:99], %[ka0], %[rs], 0 offen offset:0 sc1\n\t" "buffer_load_dwordx4 v[128:131], %[va0], %[rs], 0 offen offset:0 sc1\n\t" \
  "buffer_load_dwordx4 v[100:103], %[ka0], %[rs], 0 offen offset:1024 sc1\n\t" "buffer_load_dwordx4 v[136:139], %[va0], %[rs], 0 offen offset:1024 sc1\n\t" \
  "buffer_load_dwordx4 v[104:107], %[ka0], %[rs], 0 offen offset:2048 sc1\n\t" "buffer_load_dwordx4 v[144:147], %[va0], %[rs], 0 offen offset:2048 sc1\n\t" \
  "buffer_load_dwordx4 v[108:111], %[ka0], %[rs], 0 offen offset:3072 sc1\n\t" "buffer_load_dwordx4 v[152:155], %[va0], %[rs], 0 offen offset:3072 sc1\n\t" \
  "buffer_load_dwordx4 v[112:115], %[ka1], %[rs], 0 offen offset:0 sc1\n\t" "buffer_load_dwordx4 v[160:163], %[va1], %[rs], 0 offen offset:0 sc1\n\t" \
  "buffer_load_dwordx4 v[116:119], %[ka1], %[rs], 0 offen offset:1024 sc1\n\t" "buffer_load_dwordx4 v[168:171], %[va1], %[rs], 0 offen offset:1024 sc1\n\t" \
  "buffer_load_dwordx4 v[120:123], %[ka1], %[rs], 0 offen offset:2048 sc1\n\t" "buffer_load_dwordx4 v[176:179], %[va1], %[rs], 0 offen offset:2048 sc1\n\t" \
  "buffer_load_dwordx4 v[124:127], %[ka1], %[rs], 0 offen offset:3072 sc1\n\t" "buffer_load_dwordx4 v[184:187], %[va1], %[rs], 0 offen offset:3072 sc1\n\t" ""
#define SWAPS_ASM \
  "v_permlane16_swap_b32 v128, v132\n\t" "v_permlane16_swap_b32 v129, v133\n\t" "v_permlane16_swap_b32 v130, v134\n\t" "v_permlane16_swap_b32 v131, v135\n\t" \
  "v_permlane16_swap_b32 v136, v140\n\t" "v_permlane16_swap_b32 v137, v141\n\t" "v_permlane16_swap_b32 v138, v142\n\t" "v_permlane16_swap_b32 v139, v143\n\t" \
  "v_permlane16_swap_b32 v144, v148\n\t" "v_permlane16_swap_b32 v145, v149\n\t" "v_permlane16_swap_b32 v146, v150\n\t" "v_permlane16_swap_b32 v147, v151\n\t" \
  "v_permlane16_swap_b32 v152, v156\n\t" "v_permlane16_swap_b32 v153, v157\n\t" "v_permlane16_swap_b32 v154, v158\n\t" "v_permlane16_swap_b32 v155, v159\n\t" \
  "v_permlane16_swap_b32 v160, v164\n\t" "v_permlane16_swap_b32 v161, v165\n\t" "v_permlane16_swap_b32 v162, v166\n\t" "v_permlane16_swap_b32 v163, v167\n\t" \
  "v_permlane16_swap_b32 v168, v172\n\t" "v_permlane16_swap_b32 v169, v173\n\t" "v_permlane16_swap_b32 v170, v174\n\t" "v_permlane16_swap_b32 v171, v175\n\t" \
  "v_permlane16_swap_b32 v176, v180\n\t" "v_permlane16_swap_b32 v177, v181\n\t" "v_permlane16_swap_b32 v178, v182\n\t" "v_permlane16_swap_b32 v179, v183\n\t" \
  "v_permlane16_swap_b32 v184, v188\n\t" "v_permlane16_swap_b32 v185, v189\n\t" "v_permlane16_swap_b32 v186, v190\n\t" "v_permlane16_swap_b32 v187, v191\n\t" ""
#define PROLOG0_ASM \
  "s_waitcnt vmcnt(15)\n\t" "s_nop 1\n\t" "v_mfma_f32_32x32x16_bf16 v[208:223], v[96:99], %[q], v[192:207]\n\t" "s_waitcnt vmcnt(13)\n\t" \
  "v_mfma_f32_32x32x16_bf16 v[224:239], v[100:103], %[q], v[192:207]\n\t" "v_permlane16_swap_b32 v128, v132\n\t" "v_permlane16_swap_b32 v129, v133\n\t" \
  "v_permlane16_swap_b32 v130, v134\n\t" "v_permlane16_swap_b32 v131, v135\n\t" "s_nop 6\n\t" "v_exp_f32 v208, v208\n\t" "v_exp_f32 v209, v209\n\t" \
  "v_exp_f32 v210, v210\n\t" "v_exp_f32 v211, v211\n\t" "v_exp_f32 v212, v212\n\t" "v_exp_f32 v213, v213\n\t" "v_exp_f32 v214, v214\n\t" "v_exp_f32 v215, v215\n\t" \
  "v_exp_f32 v216, v216\n\t" "v_exp_f32 v217, v217\n\t" "v_exp_f32 v218, v218\n\t" "v_exp_f32 v219, v219\n\t" "v_exp_f32 v220, v220\n\t" ""
#define PROLOG_ASM \
  "s_nop 1\n\t" "v_mfma_f32_32x32x16_bf16 v[208:223], v[96:99], %[q], v[192:207]\n\t" "v_mfma_f32_32x32x16_bf16 v[224:239], v[100:103], %[q], v[192:207]\n\t" \
  "s_nop 10\n\t" "v_exp_f32 v208, v208\n\t" "v_exp_f32 v209, v209\n\t" "v_exp_f32 v210, v210\n\t" "v_exp_f32 v211, v211\n\t" "v_exp_f32 v212, v212\n\t" \
  "v_exp_f32 v213, v213\n\t" "v_exp_f32 v214, v214\n\t" "v_exp_f32 v215, v215\n\t" "v_exp_f32 v216, v216\n\t" "v_exp_f32 v217, v217\n\t" "v_exp_f32 v218, v218\n\t" \
  "v_exp_f32 v219, v219\n\t" "v_exp_f32 v220, v220\n\t" ""
#define PASS0_ASM \
  "v_mfma_f32_32x32x16_bf16 v[240:255], %[z], %[z], 0\n\t" "v_exp_f32 v221, v221\n\t" "v_exp_f32 v222, v222\n\t" "v_exp_f32 v223, v223\n\t" \
  "v_smfmac_f32_32x32x32_bf16 v[240:255], v[128:131], v[208:215], %[idx]\n\t" "v_exp_f32 v224, v224\n\t" "v_exp_f32 v225, v225\n\t" \
  "v_smfmac_f32_32x32x32_bf16 v[240:255], v[132:135], v[216:223], %[idx]\n\t" "v_exp_f32 v226, v226\n\t" "v_exp_f32 v227, v227\n\t" "v_exp_f32 v228, v228\n\t" \
  "s_waitcnt vmcnt(11)\n\t" "v_mfma_f32_32x32x16_bf16 v[208:223], v[104:107], %[q], v[192:207]\n\t" "v_permlane16_swap_b32 v136, v140\n\t" \
  "v_permlane16_swap_b32 v137, v141\n\t" "v_permlane16_swap_b32 v138, v142\n\t" "v_permlane16_swap_b32 v139, v143\n\t" "v_exp_f32 v229, v229\n\t" \
  "v_exp_f32 v230, v230\n\t" "v_exp_f32 v231, v231\n\t" "v_exp_f32 v232, v232\n\t" "v_exp_f32 v233, v233\n\t" \
  "v_smfmac_f32_32x32x32_bf16 v[240:255], v[136:139], v[224:231], %[idx]\n\t" "v_exp_f32 v234, v234\n\t" "v_exp_f32 v235, v235\n\t" "v_exp_f32 v236, v236\n\t" \
  "v_exp_f32 v237, v237\n\t" "v_exp_f32 v238, v238\n\t" "v_exp_f32 v239, v239\n\t" "v_exp_f32 v208, v208\n\t" "v_exp_f32 v209, v209\n\t" \
  "v_smfmac_f32_32x32x32_bf16 v[240:255], v[140:143], v[232:239], %[idx]\n\t" "v_exp_f32 v210, v210\n\t" "v_exp_f32 v211, v211\n\t" "v_exp_f32 v212, v212\n\t" \
  "s_waitcnt vmcnt(9)\n\t" "v_mfma_f32_32x32x16_bf16 v[224:239], v[108:111], %[q], v[192:207]\n\t" "v_permlane16_swap_b32 v144, v148\n\t" \
  "v_permlane16_swap_b32 v145, v149\n\t" "v_permlane16_swap_b32 v146, v150\n\t" "v_permlane16_swap_b32 v147, v151\n\t" "v_exp_f32 v213, v213\n\t" \
  "v_exp_f32 v214, v214\n\t" "v_exp_f32 v215, v215\n\t" "v_exp_f32 v216, v216\n\t" "v_exp_f32 v217, v217\n\t" \
  "v_smfmac_f32_32x32x32_bf16 v[240:255], v[144:147], v[208:215], %[idx]\n\t" "v_exp_f32 v218, v218\n\t" "v_exp_f32 v219, v219\n\t" "v_exp_f32 v220, v220\n\t" \
  "v_exp_f32 v221, v221\n\t" "v_exp_f32 v222, v222\n\t" "v_exp_f32 v223, v223\n\t" "v_exp_f32 v224, v224\n\t" "v_exp_f32 v225, v225\n\t" \
  "v_smfmac_f32_32x32x32_bf16 v[240:255], v[148:151], v[216:223], %[idx]\n\t" "v_exp_f32 v226, v226\n\t" "v_exp_f32 v227, v227\n\t" "v_exp_f32 v228, v228\n\t" \
  "s_waitcnt vmcnt(7)\n\t" "v_mfma_f32_32x32x16_bf16 v[208:223], v[112:115], %[q], v[192:207]\n\t" "v_permlane16_swap_b32 v152, v156\n\t" \
  "v_permlane16_swap_b32 v153, v157\n\t" "v_permlane16_swap_b32 v154, v158\n\t" "v_permlane16_swap_b32 v155, v159\n\t" "v_exp_f32 v229, v229\n\t" \
  "v_exp_f32 v230, v230\n\t" "v_exp_f32 v231, v231\n\t" "v_exp_f32 v232, v232\n\t" "v_exp_f32 v233, v233\n\t" \
  "v_smfmac_f32_32x32x32_bf16 v[240:255], v[152:155], v[224:231], %[idx]\n\t" "v_exp_f32 v234, v234\n\t" "v_exp_f32 v235, v235\n\t" "v_exp_f32 v236, v236\n\t" \
  "v_exp_f32 v237, v237\n\t" "v_exp_f32 v238, v238\n\t" "v_exp_f32 v239, v239\n\t" "v_exp_f32 v208, v208\n\t" "v_exp_f32 v209, v209\n\t" \
  "v_smfmac_f32_32x32x32_bf16 v[240:255], v[156:159], v[232:239], %[idx]\n\t" "v_exp_f32 v210, v210\n\t" "v_exp_f32 v211, v211\n\t" "v_exp_f32 v212, v212\n\t" \
  "s_waitcnt vmcnt(5)\n\t" "v_mfma_f32_32x32x16_bf16 v[224:239], v[116:119], %[q], v[192:207]\n\t" "v_permlane16_swap_b32 v160, v164\n\t" \
  "v_permlane16_swap_b32 v161, v165\n\t" "v_permlane16_swap_b32 v162, v166\n\t" "v_permlane16_swap_b32 v163, v167\n\t" "v_exp_f32 v213, v213\n\t" \
  "v_exp_f32 v214, v214\n\t" "v_exp_f32 v215, v215\n\t" "v_exp_f32 v216, v216\n\t" "v_exp_f32 v217, v217\n\t" \
  "v_smfmac_f32_32x32x32_bf16 v[240:255], v[160:163], v[208:215], %[idx]\n\t" "v_exp_f32 v218, v218\n\t" "v_exp_f32 v219, v219\n\t" "v_exp_f32 v220, v220\n\t" \
  "v_exp_f32 v221, v221\n\t" "v_exp_f32 v222, v222\n\t" "v_exp_f32 v223, v223\n\t" "v_exp_f32 v224, v224\n\t" "v_exp_f32 v225, v225\n\t" \
  "v_smfmac_f32_32x32x32_bf16 v[240:255], v[164:167], v[216:223], %[idx]\n\t" "v_exp_f32 v226, v226\n\t" "v_exp_f32 v227, v227\n\t" "v_exp_f32 v228, v228\n\t" \
  "s_waitcnt vmcnt(3)\n\t" "v_mfma_f32_32x32x16_bf16 v[208:223], v[120:123], %[q], v[192:207]\n\t" "v_permlane16_swap_b32 v168, v172\n\t" \
  "v_permlane16_swap_b32 v169, v173\n\t" "v_permlane16_swap_b32 v170, v174\n\t" "v_permlane16_swap_b32 v171, v175\n\t" "v_exp_f32 v229, v229\n\t" \
  "v_exp_f32 v230, v230\n\t" "v_exp_f32 v231, v231\n\t" "v_exp_f32 v232, v232\n\t" "v_exp_f32 v233, v233\n\t" \
  "v_smfmac_f32_32x32x32_bf16 v[240:255], v[168:171], v[224:231], %[idx]\n\t" "v_exp_f32 v234, v234\n\t" "v_exp_f32 v235, v235\n\t" "v_exp_f32 v236, v236\n\t" \
  "v_exp_f32 v237, v237\n\t" "v_exp_f32 v238, v238\n\t" "v_exp_f32 v239, v239\n\t" "v_exp_f32 v208, v208\n\t" "v_exp_f32 v209, v209\n\t" \
  "v_smfmac_f32_32x32x32_bf16 v[240:255], v[172:175], v[232:239], %[idx]\n\t" "v_exp_f32 v210, v210\n\t" "v_exp_f32 v211, v211\n\t" "v_exp_f32 v212, v212\n\t" \
  "s_waitcnt vmcnt(1)\n\t" "v_mfma_f32_32x32x16_bf16 v[224:239], v[124:127], %[q], v[192:207]\n\t" "ds_read_b128 %[q], %[qa]\n\t" "v_permlane16_swap_b32 v176, v180\n\t" \
  "v_permlane16_swap_b32 v177, v181\n\t" "v_permlane16_swap_b32 v178, v182\n\t" "v_permlane16_swap_b32 v179, v183\n\t" "v_exp_f32 v213, v213\n\t" \
  "v_exp_f32 v214, v214\n\t" "v_exp_f32 v215, v215\n\t" "v_exp_f32 v216, v216\n\t" "v_exp_f32 v217, v217\n\t" \
  "v_smfmac_f32_32x32x32_bf16 v[240:255], v[176:179], v[208:215], %[idx]\n\t" "v_exp_f32 v218, v218\n\t" "v_exp_f32 v219, v219\n\t" "v_exp_f32 v220, v220\n\t" \
  "v_exp_f32 v221, v221\n\t" "v_exp_f32 v222, v222\n\t" "v_exp_f32 v223, v223\n\t" "v_exp_f32 v224, v224\n\t" "v_exp_f32 v225, v225\n\t" \
  "v_smfmac_f32_32x32x32_bf16 v[240:255], v[180:183], v[216:223], %[idx]\n\t" "v_exp_f32 v226, v226\n\t" "v_exp_f32 v227, v227\n\t" "v_exp_f32 v228, v228\n\t" \
  "s_waitcnt vmcnt(0) lgkmcnt(0)\n\t" "v_mfma_f32_32x32x16_bf16 v[208:223], v[96:99], %[q], v[192:207]\n\t" "v_permlane16_swap_b32 v184, v188\n\t" \
  "v_permlane16_swap_b32 v185, v189\n\t" "v_permlane16_swap_b32 v186, v190\n\t" "v_permlane16_swap_b32 v187, v191\n\t" "v_exp_f32 v229, v229\n\t" \
  "v_exp_f32 v230, v230\n\t" "v_exp_f32 v231, v231\n\t" "v_exp_f32 v232, v232\n\t" "v_exp_f32 v233, v233\n\t" \
  "v_smfmac_f32_32x32x32_bf16 v[240:255], v[184:187], v[224:231], %[idx]\n\t" "v_exp_f32 v234, v234\n\t" "v_exp_f32 v235, v235\n\t" "v_exp_f32 v236, v236\n\t" \
  "v_exp_f32 v237, v237\n\t" "v_exp_f32 v238, v238\n\t" "v_exp_f32 v239, v239\n\t" "v_exp_f32 v208, v208\n\t" "v_exp_f32 v209, v209\n\t" \
  "v_smfmac_f32_32x32x32_bf16 v[240:255], v[188:191], v[232:239], %[idx]\n\t" "v_exp_f32 v210, v210\n\t" "v_exp_f32 v211, v211\n\t" "v_exp_f32 v212, v212\n\t" \
  "v_mfma_f32_32x32x16_bf16 v[224:239], v[100:103], %[q], v[192:207]\n\t" "v_exp_f32 v213, v213\n\t" "v_exp_f32 v214, v214\n\t" "v_exp_f32 v215, v215\n\t" \
  "v_exp_f32 v216, v216\n\t" "v_exp_f32 v217, v217\n\t" "v_exp_f32 v218, v218\n\t" "v_exp_f32 v219, v219\n\t" "v_exp_f32 v220, v220\n\t" ""
#define PASSN_ASM \
  "v_mfma_f32_32x32x16_bf16 v[240:255], %[z], %[z], 0\n\t" "v_exp_f32 v221, v221\n\t" "v_exp_f32 v222, v222\n\t" "v_exp_f32 v223, v223\n\t" \
  "v_smfmac_f32_32x32x32_bf16 v[240:255], v[128:131], v[208:215], %[idx]\n\t" "v_exp_f32 v224, v224\n\t" "v_exp_f32 v225, v225\n\t" \
  "v_smfmac_f32_32x32x32_bf16 v[240:255], v[132:135], v[216:223], %[idx]\n\t" "v_exp_f32 v226, v226\n\t" "v_exp_f32 v227, v227\n\t" "v_exp_f32 v228, v228\n\t" \
  "v_mfma_f32_32x32x16_bf16 v[208:223], v[104:107], %[q], v[192:207]\n\t" "v_exp_f32 v229, v229\n\t" "v_exp_f32 v230, v230\n\t" "v_exp_f32 v231, v231\n\t" \
  "v_exp_f32 v232, v232\n\t" "v_exp_f32 v233, v233\n\t" "v_smfmac_f32_32x32x32_bf16 v[240:255], v[136:139], v[224:231], %[idx]\n\t" "v_exp_f32 v234, v234\n\t" \
  "v_exp_f32 v235, v235\n\t" "v_exp_f32 v236, v236\n\t" "v_exp_f32 v237, v237\n\t" "v_exp_f32 v238, v238\n\t" "v_exp_f32 v239, v239\n\t" "v_exp_f32 v208, v208\n\t" \
  "v_exp_f32 v209, v209\n\t" "v_smfmac_f32_32x32x32_bf16 v[240:255], v[140:143], v[232:239], %[idx]\n\t" "v_exp_f32 v210, v210\n\t" "v_exp_f32 v211, v211\n\t" \
  "v_exp_f32 v212, v212\n\t" "v_mfma_f32_32x32x16_bf16 v[224:239], v[108:111], %[q], v[192:207]\n\t" "v_exp_f32 v213, v213\n\t" "v_exp_f32 v214, v214\n\t" \
  "v_exp_f32 v215, v215\n\t" "v_exp_f32 v216, v216\n\t" "v_exp_f32 v217, v217\n\t" "v_smfmac_f32_32x32x32_bf16 v[240:255], v[144:147], v[208:215], %[idx]\n\t" \
  "v_exp_f32 v218, v218\n\t" "v_exp_f32 v219, v219\n\t" "v_exp_f32 v220, v220\n\t" "v_exp_f32 v221, v221\n\t" "v_exp_f32 v222, v222\n\t" "v_exp_f32 v223, v223\n\t" \
  "v_exp_f32 v224, v224\n\t" "v_exp_f32 v225, v225\n\t" "v_smfmac_f32_32x32x32_bf16 v[240:255], v[148:151], v[216:223], %[idx]\n\t" "v_exp_f32 v226, v226\n\t" \
  "v_exp_f32 v227, v227\n\t" "v_exp_f32 v228, v228\n\t" "v_mfma_f32_32x32x16_bf16 v[208:223], v[112:115], %[q], v[192:207]\n\t" "v_exp_f32 v229, v229\n\t" \
  "v_exp_f32 v230, v230\n\t" "v_exp_f32 v231, v231\n\t" "v_exp_f32 v232, v232\n\t" "v_exp_f32 v233, v233\n\t" \
  "v_smfmac_f32_32x32x32_bf16 v[240:255], v[152:155], v[224:231], %[idx]\n\t" "v_exp_f32 v234, v234\n\t" "v_exp_f32 v235, v235\n\t" "v_exp_f32 v236, v236\n\t" \
  "v_exp_f32 v237, v237\n\t" "v_exp_f32 v238, v238\n\t" "v_exp_f32 v239, v239\n\t" "v_exp_f32 v208, v208\n\t" "v_exp_f32 v209, v209\n\t" \
  "v_smfmac_f32_32x32x32_bf16 v[240:255], v[156:159], v[232:239], %[idx]\n\t" "v_exp_f32 v210, v210\n\t" "v_exp_f32 v211, v211\n\t" "v_exp_f32 v212, v212\n\t" \
  "v_mfma_f32_32x32x16_bf16 v[224:239], v[116:119], %[q], v[192:207]\n\t" "v_exp_f32 v213, v213\n\t" "v_exp_f32 v214, v214\n\t" "v_exp_f32 v215, v215\n\t" \
  "v_exp_f32 v216, v216\n\t" "v_exp_f32 v217, v217\n\t" "v_smfmac_f32_32x32x32_bf16 v[240:255], v[160:163], v[208:215], %[idx]\n\t" "v_exp_f32 v218, v218\n\t" \
  "v_exp_f32 v219, v219\n\t" "v_exp_f32 v220, v220\n\t" "v_exp_f32 v221, v221\n\t" "v_exp_f32 v222, v222\n\t" "v_exp_f32 v223, v223\n\t" "v_exp_f32 v224, v224\n\t" \
  "v_exp_f32 v225, v225\n\t" "v_smfmac_f32_32x32x32_bf16 v[240:255], v[164:167], v[216:223], %[idx]\n\t" "v_exp_f32 v226, v226\n\t" "v_exp_f32 v227, v227\n\t" \
  "v_exp_f32 v228, v228\n\t" "v_mfma_f32_32x32x16_bf16 v[208:223], v[120:123], %[q], v[192:207]\n\t" "v_exp_f32 v229, v229\n\t" "v_exp_f32 v230, v230\n\t" \
  "v_exp_f32 v231, v231\n\t" "v_exp_f32 v232, v232\n\t" "v_exp_f32 v233, v233\n\t" "v_smfmac_f32_32x32x32_bf16 v[240:255], v[168:171], v[224:231], %[idx]\n\t" \
  "v_exp_f32 v234, v234\n\t" "v_exp_f32 v235, v235\n\t" "v_exp_f32 v236, v236\n\t" "v_exp_f32 v237, v237\n\t" "v_exp_f32 v238, v238\n\t" "v_exp_f32 v239, v239\n\t" \
  "v_exp_f32 v208, v208\n\t" "v_exp_f32 v209, v209\n\t" "v_smfmac_f32_32x32x32_bf16 v[240:255], v[172:175], v[232:239], %[idx]\n\t" "v_exp_f32 v210, v210\n\t" \
  "v_exp_f32 v211, v211\n\t" "v_exp_f32 v212, v212\n\t" "v_mfma_f32_32x32x16_bf16 v[224:239], v[124:127], %[q], v[192:207]\n\t" "ds_read_b128 %[q], %[qa]\n\t" \
  "v_exp_f32 v213, v213\n\t" "v_exp_f32 v214, v214\n\t" "v_exp_f32 v215, v215\n\t" "v_exp_f32 v216, v216\n\t" "v_exp_f32 v217, v217\n\t" \
  "v_smfmac_f32_32x32x32_bf16 v[240:255], v[176:179], v[208:215], %[idx]\n\t" "v_exp_f32 v218, v218\n\t" "v_exp_f32 v219, v219\n\t" "v_exp_f32 v220, v220\n\t" \
  "v_exp_f32 v221, v221\n\t" "v_exp_f32 v222, v222\n\t" "v_exp_f32 v223, v223\n\t" "v_exp_f32 v224, v224\n\t" "v_exp_f32 v225, v225\n\t" \
  "v_smfmac_f32_32x32x32_bf16 v[240:255], v[180:183], v[216:223], %[idx]\n\t" "v_exp_f32 v226, v226\n\t" "v_exp_f32 v227, v227\n\t" "v_exp_f32 v228, v228\n\t" \
  "s_waitcnt lgkmcnt(0)\n\t" "v_mfma_f32_32x32x16_bf16 v[208:223], v[96:99], %[q], v[192:207]\n\t" "v_exp_f32 v229, v229\n\t" "v_exp_f32 v230, v230\n\t" \
  "v_exp_f32 v231, v231\n\t" "v_exp_f32 v232, v232\n\t" "v_exp_f32 v233, v233\n\t" "v_smfmac_f32_32x32x32_bf16 v[240:255], v[184:187], v[224:231], %[idx]\n\t" \
  "v_exp_f32 v234, v234\n\t" "v_exp_f32 v235, v235\n\t" "v_exp_f32 v236, v236\n\t" "v_exp_f32 v237, v237\n\t" "v_exp_f32 v238, v238\n\t" "v_exp_f32 v239, v239\n\t" \
  "v_exp_f32 v208, v208\n\t" "v_exp_f32 v209, v209\n\t" "v_smfmac_f32_32x32x32_bf16 v[240:255], v[188:191], v[232:239], %[idx]\n\t" "v_exp_f32 v210, v210\n\t" \
  "v_exp_f32 v211, v211\n\t" "v_exp_f32 v212, v212\n\t" "v_mfma_f32_32x32x16_bf16 v[224:239], v[100:103], %[q], v[192:207]\n\t" "v_exp_f32 v213, v213\n\t" \
  "v_exp_f32 v214, v214\n\t" "v_exp_f32 v215, v215\n\t" "v_exp_f32 v216, v216\n\t" "v_exp_f32 v217, v217\n\t" "v_exp_f32 v218, v218\n\t" "v_exp_f32 v219, v219\n\t" \
  "v_exp_f32 v220, v220\n\t" ""
#define ROWMAX_ASM \
  "s_nop 1\n\t" "v_mfma_f32_32x32x16_bf16 v[224:239], v[96:99], %[q], 0\n\t" "s_nop 11\n\t" "v_max3_f32 %[m], %[m], v224, v225\n\t" \
  "v_max3_f32 %[m], %[m], v226, v227\n\t" "v_max3_f32 %[m], %[m], v228, v229\n\t" "v_max3_f32 %[m], %[m], v230, v231\n\t" "v_max3_f32 %[m], %[m], v232, v233\n\t" \
  "v_max3_f32 %[m], %[m], v234, v235\n\t" "v_max3_f32 %[m], %[m], v236, v237\n\t" "v_max3_f32 %[m], %[m], v238, v239\n\t" \
  "v_mfma_f32_32x32x16_bf16 v[224:239], v[100:103], %[q], 0\n\t" "s_nop 11\n\t" "v_max3_f32 %[m], %[m], v224, v225\n\t" "v_max3_f32 %[m], %[m], v226, v227\n\t" \
  "v_max3_f32 %[m], %[m], v228, v229\n\t" "v_max3_f32 %[m], %[m], v230, v231\n\t" "v_max3_f32 %[m], %[m], v232, v233\n\t" "v_max3_f32 %[m], %[m], v234, v235\n\t" \
  "v_max3_f32 %[m], %[m], v236, v237\n\t" "v_max3_f32 %[m], %[m], v238, v239\n\t" "v_mfma_f32_32x32x16_bf16 v[224:239], v[104:107], %[q], 0\n\t" "s_nop 11\n\t" \
  "v_max3_f32 %[m], %[m], v224, v225\n\t" "v_max3_f32 %[m], %[m], v226, v227\n\t" "v_max3_f32 %[m], %[m], v228, v229\n\t" "v_max3_f32 %[m], %[m], v230, v231\n\t" \
  "v_max3_f32 %[m], %[m], v232, v233\n\t" "v_max3_f32 %[m], %[m], v234, v235\n\t" "v_max3_f32 %[m], %[m], v236, v237\n\t" "v_max3_f32 %[m], %[m], v238, v239\n\t" \
  "v_mfma_f32_32x32x16_bf16 v[224:239], v[108:111], %[q], 0\n\t" "s_nop 11\n\t" "v_max3_f32 %[m], %[m], v224, v225\n\t" "v_max3_f32 %[m], %[m], v226, v227\n\t" \
  "v_max3_f32 %[m], %[m], v228, v229\n\t" "v_max3_f32 %[m], %[m], v230, v231\n\t" "v_max3_f32 %[m], %[m], v232, v233\n\t" "v_max3_f32 %[m], %[m], v234, v235\n\t" \
  "v_max3_f32 %[m], %[m], v236, v237\n\t" "v_max3_f32 %[m], %[m], v238, v239\n\t" "v_mfma_f32_32x32x16_bf16 v[224:239], v[112:115], %[q], 0\n\t" "s_nop 11\n\t" \
  "v_max3_f32 %[m], %[m], v224, v225\n\t" "v_max3_f32 %[m], %[m], v226, v227\n\t" "v_max3_f32 %[m], %[m], v228, v229\n\t" "v_max3_f32 %[m], %[m], v230, v231\n\t" \
  "v_max3_f32 %[m], %[m], v232, v233\n\t" "v_max3_f32 %[m], %[m], v234, v235\n\t" "v_max3_f32 %[m], %[m], v236, v237\n\t" "v_max3_f32 %[m], %[m], v238, v239\n\t" \
  "v_mfma_f32_32x32x16_bf16 v[224:239], v[116:119], %[q], 0\n\t" "s_nop 11\n\t" "v_max3_f32 %[m], %[m], v224, v225\n\t" "v_max3_f32 %[m], %[m], v226, v227\n\t" \
  "v_max3_f32 %[m], %[m], v228, v229\n\t" "v_max3_f32 %[m], %[m], v230, v231\n\t" "v_max3_f32 %[m], %[m], v232, v233\n\t" "v_max3_f32 %[m], %[m], v234, v235\n\t" \
  "v_max3_f32 %[m], %[m], v236, v237\n\t" "v_max3_f32 %[m], %[m], v238, v239\n\t" "v_mfma_f32_32x32x16_bf16 v[224:239], v[120:123], %[q], 0\n\t" "s_nop 11\n\t" \
  "v_max3_f32 %[m], %[m], v224, v225\n\t" "v_max3_f32 %[m], %[m], v226, v227\n\t" "v_max3_f32 %[m], %[m], v228, v229\n\t" "v_max3_f32 %[m], %[m], v230, v231\n\t" \
  "v_max3_f32 %[m], %[m], v232, v233\n\t" "v_max3_f32 %[m], %[m], v234, v235\n\t" "v_max3_f32 %[m], %[m], v236, v237\n\t" "v_max3_f32 %[m], %[m], v238, v239\n\t" \
  "v_mfma_f32_32x32x16_bf16 v[224:239], v[124:127], %[q], 0\n\t" "s_nop 11\n\t" "v_max3_f32 %[m], %[m], v224, v225\n\t" "v_max3_f32 %[m], %[m], v226, v227\n\t" \
  "v_max3_f32 %[m], %[m], v228, v229\n\t" "v_max3_f32 %[m], %[m], v230, v231\n\t" "v_max3_f32 %[m], %[m], v232, v233\n\t" "v_max3_f32 %[m], %[m], v234, v235\n\t" \
  "v_max3_f32 %[m], %[m], v236, v237\n\t" "v_max3_f32 %[m], %[m], v238, v239\n\t" ""
#define POST_ASM \
  "s_add_u32 %[i], %[i], 1\n\t" "s_cmp_eq_u32 %[i], 8\n\t" "s_cbranch_scc1 .Lfb_out%=\n\t" "v_cndmask_b32 %[t1], %[sh], v248, %[lo]\n\t" \
  "v_add_u32 %[t2], %[so], %[l16]\n\t" "ds_write_b128 %[t2], v[240:243]\n\t" "ds_write_b128 %[t2], v[244:247] offset:1024\n\t" "v_add_u32 %[t2], %[so], %[l4]\n\t" \
  "ds_write_b32 %[t2], %[t1] offset:2048\n\t" "s_add_u32 %[so], %[so], 14080\n\t" "s_and_b32 %[so], %[so], 0x1ffff\n\t" "s_add_u32 %[qo], %[qo], 1024\n\t" \
  "s_and_b32 %[qo], %[qo], 0x1fff\n\t" "v_add_u32 %[qa], %[qo], %[qb]\n\t" "s_cmp_lt_u32 %[i], %[stop]\n\t" ""

#define KV_OUT "=&{v[96:127]}"(kv0), "=&{v[128:159]}"(kv1), "=&{v[160:191]}"(kv2)
#define KV_IO "+{v[96:127]}"(kv0), "+{v[128:159]}"(kv1), "+{v[160:191]}"(kv2)
#define KV_IN "{v[96:127]}"(kv0), "{v[128:159]}"(kv1), "{v[160:191]}"(kv2)
#define KV_ADDR [ka0] "v"(kb), [ka1] "v"(kb + 4096), [va0] "v"(vp), [va1] "v"(vp + 4096), [rs] "s"(rs)

__global__ __launch_bounds__(512, 2) void fused_block(const u16* __restrict__ x, const u16* __restrict__ wq, const u16* __restrict__ wk,
                                                      const u16* __restrict__ wv, const u16* __restrict__ wo, const u16* __restrict__ wr,
                                                      const u16* __restrict__ wg, const u16* __restrict__ wu, const u16* __restrict__ wd,
                                                      char* ws, u16* __restrict__ out) {
  __shared__ __attribute__((aligned(16))) char smem[L_TOTAL];
  unsigned pf1, pf2;
  {
    unsigned long long pc; unsigned t;
    asm volatile("s_getpc_b64 %[pc]\n.Lfb_begin:\n\t"
                 "v_and_b32 %[a], 15, %[tid]\n\tv_lshlrev_b32 %[b], 7, %[a]\n\tglobal_load_dword %[b], %[b], %[pc]\n\t"
                 "s_lshr_b32 %[t], %[bx], 3\n\ts_mul_i32 %[t], %[t], 1152\n\ts_add_u32 %[t], %[t], 2048\n\t"
                 "v_min_u32 %[a], 8, %[a]\n\tv_lshl_add_u32 %[a], %[a], 7, %[t]\n\tv_min_u32 %[a], .Lfb_end-.Lfb_begin, %[a]\n\t"
                 "global_load_dword %[a], %[a], %[pc]"
                 : [pc] "=&s"(pc), [t] "=&s"(t), [a] "=&v"(pf1), [b] "=&v"(pf2) : [tid] "v"(threadIdx.x), [bx] "s"(blockIdx.x) : "scc", "memory");
  }
  char* const XI = smem + L_XI;
  char* const WI = smem + L_WI;
  char* const KI = smem + L_KI;
  char* const CTL = smem + L_CTL;
  const int tid = threadIdx.x, lane = tid & 63;
  const int w = __builtin_amdgcn_readfirstlane(tid >> 6);
  const int b = blockIdx.x, g8 = b & 7, tb = b & 31, hg = b >> 5;
  const int rr = lane >> 3, c = lane & 7;
  const int h = lane >> 5, l31 = lane & 31;
  const int isV = hg & 1, lr0 = (hg >> 1) * 16;
  const int pid = tb * 8 + hg;
  const int u = (b & 7) * 28 + (b >> 3);
  const rsrc_t rs = __builtin_amdgcn_make_buffer_rsrc(ws, 0, (int)WS_END, 0x00020000);
  STAMP(0, 0);

  unsigned tag;
  asm volatile("s_load_dword %0, %1, 0x0" : "=s"(tag) : "s"(ws) : "memory");
  const bool kvw = (w == (hg & 6));
  const bool ldw = (w == (hg | 1));
  u32x4 xo0, xo1, wr0, wr1, xe, xv[8];
  f32x32 wov;
  unsigned pf0;
  {
    const u16* p = x + (size_t)(tb * 64 + lr0 + 2 * w) * D_;
    GLD128(xo0, h * 1024 + l31 * 16, p);
    GLD128(xo1, h * 1024 + l31 * 16 + 512, p);
  }
#pragma unroll
  for (int i = 0; i < 2; ++i) glds16((isV ? wv : wk) + (w * 2 + i) * 512, lane * 16, KI + (w * 2 + i) * 1024);
  {
    unsigned long long ta = (unsigned long long)ws + (lane == 0 ? WS_YP + b * 5376 : (lane == 1 ? WS_K : WS_KVF));
    if (lane == 3) ta = (unsigned long long)(out + (size_t)tb * 64 * D_);
    asm volatile("global_load_dword %0, %1, off" : "=v"(pf0) : "v"(ta) : "memory");
  }
  GLD128(wr0, l31 * 16, wr);
  GLD128(wr1, l31 * 16 + 512, wr);
  const unsigned row_off = rr * 1024 + c * 16;
  const unsigned wq_off = rr * 1024 + ((c ^ (((rr >> 1) & 1) << 2)) << 4);
  const u16* xrows = x + (size_t)(tb * 64 + w * 8) * D_;
  if (kvw) {
    __builtin_amdgcn_s_setprio(3);
#pragma unroll
    for (int j = 0; j < 8; ++j)
      st128(rs, WS_O + (unsigned)((tb * 2 + (j >> 2)) * 32 + hg * 4 + (j & 3)) * 1024 + lane * 16, u32x4{0xffffffffu, 0xffffffffu, 0xffffffffu, 0xffffffffu});
  } else if (ldw) {
#pragma unroll
    for (int pc = 0; pc < 8; ++pc) glds16(wq + pc * 8 * D_ + hg * 64, wq_off, WI + pc * 1024);
  } else {
#pragma unroll
    for (int i = 0; i < 8; ++i) asm volatile("global_load_dwordx4 %0, %1, %2 offset:%3" : "=v"(xv[i]) : "v"(row_off), "s"(xrows), "i"(i * 128) : "memory");
  }
  VMWAIT(13);
  asm volatile("" : "+v"(xo0), "+v"(xo1), "+v"(pf1), "+v"(pf2));
  STAMP(0, 1);
  {
    float mean, var;
    stats32(xo0, xo1, mean, var);
    const float mb = rbf(mean), sd = ln_sd(var), inv = __builtin_amdgcn_rcpf(sd);
#pragma unroll
    for (int j = 0; j < 4; ++j) { xo0[j] = ln_pair(xo0[j], mb, inv); xo1[j] = ln_pair(xo1[j], mb, inv); }
    const int lrow = lr0 + 2 * w + h;
    char* xr = XI + lrow * 1024 + ((l31 ^ (lrow & 15)) << 4);
    *(u32x4*)xr = xo0; *(u32x4*)(xr + 512) = xo1;
    if (l31 == 0) *(f32x2*)(CTL + C_STAT + lrow * 8) = f32x2{mb, sd};
    if (tid < 2) *(unsigned*)(CTL + tid * 4) = 0u;
    VMWAIT(11);
    asm volatile("s_waitcnt lgkmcnt(0)" : "+s"(tag) :: "memory");
    __builtin_amdgcn_s_barrier();
    CBAR();
  }
  tag += 1u;
  STAMP(0, 2);
  if (kvw) {
    const int g = lane >> 4, q = (lane & 15) >> 2, p = lane & 3;
    const char* xb = XI + (lr0 + (lane & 15)) * 1024;
    const char* wb = opq(KI + (8 * g + q) * 32 + p * 8);
    f32x4 acc = {}, act = {};
    bf16x8 fx[2][4], fw[2][4];
#define KV_LD(bf, k0) _Pragma("unroll") for (int i = 0; i < 4; ++i) { const int ks = (k0) + i; \
      fx[bf][i] = *(const bf16x8*)(xb + ((((4 * i + g) ^ (lane & 15)) << 4) + (ks >> 2) * 256)); \
      fw[bf][i] = tr_frag(wb + ks * 1024, wb + ks * 1024 + 128); }
    KV_LD(0, 0)
#pragma unroll
    for (int kb = 0; kb < 4; ++kb) {
      if (kb < 3) { KV_LD((kb + 1) & 1, 4 * kb + 4) }
      SB();
#pragma unroll
      for (int i = 0; i < 4; ++i) {
        acc = __builtin_amdgcn_mfma_f32_16x16x32_bf16(fw[kb & 1][i], fx[kb & 1][i], acc, 0, 0, 0);
        act = __builtin_amdgcn_mfma_f32_16x16x32_bf16(fx[kb & 1][i], fw[kb & 1][i], act, 0, 0, 0);
      }
      SB();
    }
    if (isV) acc = act;
    const u32x2 pk = {pack_bf(acc[0], acc[1]), pack_bf(acc[2], acc[3])};
    float n = 0.f;
    if (!isV) {
      st64(rs, WS_K + (unsigned)(tb * 64 + lr0 + (lane & 15)) * 32 + (((g & 1) << 1) | (g >> 1)) * 8, pk);
      n = wave_maxpos(add32(add16(acc[0] * acc[0] + acc[1] * acc[1] + acc[2] * acc[2] + acc[3] * acc[3])));
    } else {
      st64(rs, WS_V + (unsigned)(pid - 1 + (g >> 1)) * VHALF + (lane & 15) * 16 + (g & 1) * 8, pk);
    }
    DRAIN();
    if (lane < 8) st64(rs, WS_KVF + (unsigned)(lane * 256 + pid) * 8, u32x2{__float_as_uint(n), tag});
    __builtin_amdgcn_s_setprio(0);
    STAMP(0, 6);
  } else if (ldw) {
#pragma unroll 4
    for (int pc = 8; pc < 28; ++pc) glds16(wq + pc * 8 * D_ + hg * 64, wq_off, WI + pc * 1024);
  } else {
    const int w6 = w - (w > hg ? 2 : 0);
#pragma unroll
    for (int i = 0; i < 6; ++i) { const int pc = 28 + i * 6 + w6; glds16(wq + pc * 8 * D_ + hg * 64, wq_off, WI + pc * 1024); }
    asm volatile("s_waitcnt vmcnt(6)" : "+v"(xv[0]), "+v"(xv[1]), "+v"(xv[2]), "+v"(xv[3]), "+v"(xv[4]), "+v"(xv[5]), "+v"(xv[6]), "+v"(xv[7]) :: "memory");
    float s = 0.f, q = 0.f;
#pragma unroll
    for (int i = 0; i < 8; ++i)
#pragma unroll
      for (int j = 0; j < 4; ++j) { s = dot2(xv[i][j], 0x3f803f80u, s); q = dot2(xv[i][j], xv[i][j], q); }
    const float mean = sum8(s) * (1.0f / 512.0f);
    float var = fmaf(-mean, mean, sum8(q) * (1.0f / 512.0f));
    if (__builtin_expect(__any(!(var >= mean * mean * 0.015625f)), 0)) {
      float v = 0.f;
#pragma unroll
      for (int i = 0; i < 8; ++i)
#pragma unroll
        for (int j = 0; j < 4; ++j) { const float a = bf_lo(xv[i][j]) - mean, bb = bf_hi(xv[i][j]) - mean; v = fmaf(a, a, v); v = fmaf(bb, bb, v); }
      var = sum8(v) * (1.0f / 512.0f);
    }
    const float st_m = rbf(mean), st_s = ln_sd(var);
    const float inv = __builtin_amdgcn_rcpf(st_s), nb = -st_m * inv;
    const int lrow = w * 8 + rr;
#pragma unroll
    for (int i = 0; i < 8; ++i) {
      u32x4 o;
#pragma unroll
      for (int j = 0; j < 4; ++j) o[j] = pack_bf(fmaf(bf_lo(xv[i][j]), inv, nb), fmaf(bf_hi(xv[i][j]), inv, nb));
      *(u32x4*)(XI + lrow * 1024 + (((i * 8 + c) ^ (lrow & 15)) << 4)) = o;
    }
    if (c == 0) *(f32x2*)(CTL + C_STAT + lrow * 8) = f32x2{st_m, st_s};
    STAMP(0, 3);
  }
  VMWAIT(28);
  asm volatile("" : "+v"(pf0), "+v"(wr0), "+v"(wr1));
  if ((w >> 2) == isV) {
    float r = 0.f;
#pragma unroll
    for (int j = 0; j < 4; ++j) { r = dot2(xo0[j], wr0[j], r); r = dot2(xo1[j], wr1[j], r); }
    const unsigned rkey = make_key(rbf(sum32(r)), pid * 8 + 2 * (w & 3) + h);
    if (l31 == 0) *(unsigned*)(CTL + C_R + (2 * (w & 3) + h) * 4) = rkey;
  }
  DRAIN();
  RAW_BARRIER();
  STAMP(0, 4);
  if (w < 4) __builtin_amdgcn_s_setprio(2); else __builtin_amdgcn_s_setprio(1);
  if (w == 4) {
    const unsigned key = lane < 8 ? *(const unsigned*)(CTL + C_R + lane * 4) : 0u;
    const unsigned k1 = wave_maxu(key), k2 = wave_maxu(key == k1 ? 0u : key);
    if (lane < 8) st128(rs, WS_CAND + (unsigned)(lane * 256 + pid) * 16, u32x4{k1, tag, k2, tag});
  }
  const int blk = (lane >> 4) & 1, q4 = (lane & 15) >> 2, p4 = lane & 3;
  if (w < 4) {
    const int nt = w & 1, tt = w >> 1;
    const int tok = tt * 32 + l31;
    const char* xb = XI + tok * 1024;
    const char* wb = WI + (8 * h + q4) * 128 + (((4 * nt + 2 * blk + (p4 >> 1)) ^ (((q4 >> 1) & 1) << 2)) << 4) + 8 * (p4 & 1);
    f32x16 acc = {};
    bf16x8 fa[2][4], fb[2][4];
#define Q_LD(bf, k0) _Pragma("unroll") for (int i = 0; i < 4; ++i) { const int ks = (k0) + i; \
      fa[bf][i] = tr_frag(wb + ks * 2048, wb + ks * 2048 + 512); \
      fb[bf][i] = *(const bf16x8*)(xb + ((((2 * (ks & 7) + h) ^ (tok & 15)) << 4) + (ks >> 3) * 256)); }
    Q_LD(0, 0)
    SB();
#pragma unroll
    for (int kb = 0; kb < 8; ++kb) {
      if (kb < 7) { Q_LD((kb + 1) & 1, 4 * kb + 4) }
#pragma unroll
      for (int i = 0; i < 4; ++i) acc = __builtin_amdgcn_mfma_f32_32x32x16_bf16(fa[kb & 1][i], fb[kb & 1][i], acc, 0, 0, 0);
#pragma unroll
      for (int i = 0; i < 4; ++i) { SGB(0x8, 1); SGB(0x100, 3); }
      SB();
    }
    const float cs = 0.25f * 1.4426950408889634f;
    float n0 = 0.f, n1 = 0.f;
#pragma unroll
    for (int r = 0; r < 8; ++r) { acc[r] *= cs; acc[r + 8] *= cs; n0 = fmaf(acc[r], acc[r], n0); n1 = fmaf(acc[r + 8], acc[r + 8], n1); }
    char* qi = smem + L_QI + (tt * 4 + nt * 2) * 1024 + lane * 16;
    *(u32x4*)qi = u32x4{pack_bf(acc[0], acc[1]), pack_bf(acc[2], acc[3]), pack_bf(acc[4], acc[5]), pack_bf(acc[6], acc[7])};
    *(u32x4*)(qi + 1024) = u32x4{pack_bf(acc[8], acc[9]), pack_bf(acc[10], acc[11]), pack_bf(acc[12], acc[13]), pack_bf(acc[14], acc[15])};
    const float nm = wave_maxpos(fmaxf(add32(n0), add32(n1)));
    if (lane == 0) {
      *(float*)(CTL + C_QMAX + w * 4) = nm;
      asm volatile("s_waitcnt lgkmcnt(0)\n\tds_add_u32 %0, %1" :: "v"((unsigned)(uintptr_t)(LAS char*)(CTL + C_CNT)), "v"(1u) : "memory");
    }
  }
  STAMP(0, 5);

  xe = *(const u32x4*)((const char*)(xrows + hg * 64) + row_off);
  f32x32 kv0, kv1, kv2;
  asm volatile(VCONST_ASM : KV_OUT : [cst] "v"((lane & 15) ? 0u : 0x3f803f80u));
  float kmax;
  {
    u32x2 f;
    SPIN {
      f = ld64(rs, WS_KVF + (unsigned)(g8 * 256 + w * 32 + l31) * 8);
      if (__all(f[1] == tag)) break;
      __builtin_amdgcn_s_sleep(1);
    }
    CBAR();
    kmax = __uint_as_float(f[0]);
  }
  STAMP(1, 0);
  {
    const unsigned kb = WS_K + (unsigned)(w * 256 + l31) * 32 + h * 16;
    const unsigned vp = WS_V + (unsigned)(w * 32 + 2 * blk + h) * VHALF + (lane & 15) * 16;
    asm volatile(LOADS_ASM : KV_IO : KV_ADDR : "memory");
  }
  kmax = wave_maxpos(kmax);
  u32x4 gatep;
  {
    const f32x2 st = *(const f32x2*)(CTL + C_STAT + (w * 8 + rr) * 8);
    const float inv = __builtin_amdgcn_rcpf(st[1]);
#pragma unroll
    for (int j = 0; j < 4; ++j) gatep[j] = silu_pair(ln_pair(xe[j], st[0], inv));
    asm volatile("" : "+v"(gatep));
  }
  lds_wait_ge(CTL + C_CNT, 4u);
  STAMP(1, 1);
  const f32x4 qn = *(const f32x4*)(CTL + C_QMAX);
  const float qmax = fmaxf(fmaxf(qn[0], qn[1]), fmaxf(qn[2], qn[3]));
  const float bound = __builtin_amdgcn_sqrtf(qmax * kmax) * 1.02f + 0.01f;
  int fasti = __builtin_amdgcn_readfirstlane(bound <= 48.0f ? 1 : 0);
  asm volatile("" : "+s"(fasti));
  const bool fast = fasti != 0;
  const int idx = (int)0xDDDDDDDDu;
  bf16x8 zf = {};
  asm volatile("" : "+v"(zf));
  const f32x16 z16 = {};
  const char* qbase = smem + L_QI + lane * 16;
  f32x16 o;
  float shift = bound;
  bf16x8 qf = *(const bf16x8*)(qbase + ((w + 1) & 7) * 1024);
  const unsigned lds0 = (unsigned)(uintptr_t)(LAS char*)smem;
  unsigned qa = lds0 + L_QI + lane * 16 + ((w + 2) & 7) * 1024;
  f32x16 cc = splat16(-bound);
  f32x32 sab;
  int i = 0;
  unsigned so = ((w + 1) & 7) * PREG + 6 * PSLOT, qo = ((w + 2) & 7) * 1024; float t1_; unsigned t2_;
#define RUN_OUT "=&{v[240:255]}"(o), "+{v[192:207]}"(cc), [q] "+v"(qf), [qa] "+v"(qa), [i] "+s"(i), [so] "+s"(so), [qo] "+s"(qo), [t1] "=&v"(t1_), [t2] "=&v"(t2_)
#define RUN_IN [z] "v"(zf), [idx] "v"(idx), [sh] "v"(shift), [lo] "s"(0xffffffffull), [l16] "v"(lds0 + lane * 16), [l4] "v"(lds0 + lane * 4), \
               [qb] "v"(lds0 + L_QI + lane * 16)
  STAMP(3, 0);
  asm volatile("s_cmp_eq_u32 %[fast], 0\n\ts_cbranch_scc1 .Lfb_slow0\n\t" PROLOG0_ASM PASS0_ASM POST_ASM
               "s_branch .Lfb_out%=\n.Lfb_slow0:\n\ts_waitcnt vmcnt(0)\n\t" SWAPS_ASM "\n.Lfb_out%=:"
               : "=&{v[208:239]}"(sab), RUN_OUT, KV_IO : RUN_IN, [fast] "s"(fasti), [stop] "s"(0) : "scc", "memory");

  if (w == 2) {
        int lane = tid & 63; asm volatile("" : "+v"(lane));
        unsigned m1 = 0, m2 = 0;
        SPIN {
          bool ok = true; m1 = 0; m2 = 0;
#pragma unroll
          for (int j = 0; j < 4; ++j) {
            const u32x4 cd = ld128(rs, WS_CAND + (unsigned)(g8 * 256 + j * 64 + lane) * 16);
            ok = ok && cd[1] == tag && cd[3] == tag;
            m2 = max(max(m2, cd[2]), min(m1, cd[0])); m1 = max(m1, cd[0]);
          }
          if (__all(ok)) break;
          __builtin_amdgcn_s_sleep(1);
        }
        CBAR();
        const unsigned k1 = __builtin_amdgcn_readfirstlane(wave_maxu(m1));
        const unsigned k2 = __builtin_amdgcn_readfirstlane(wave_maxu(m1 == k1 ? m2 : m1));
        if (lane == 0) *(u32x2*)(CTL + C_K1) = u32x2{k1, k2};
        if (b < NU) {
          const int h = lane >> 5, l31 = lane & 31;
          const u16* xr = x + key_tok(h ? k2 : k1) * D_ + l31 * 8;
        {
          const u32x4 xs0 = *(const u32x4*)xr, xs1 = *(const u32x4*)(xr + 256);
          float mean, var;
          stats32(xs0, xs1, mean, var);
          const float mb = rbf(mean), inv = __builtin_amdgcn_rcpf(ln_sd(var));
          u32x4 o0;
#pragma unroll
          for (int j = 0; j < 4; ++j) o0[j] = ln_pair((u & 4) ? xs1[j] : xs0[j], mb, inv);
          *(u32x4*)(smem + L_XS + h * 1024 + l31 * 16) = o0;
          asm volatile("s_waitcnt lgkmcnt(0)" ::: "memory");
          if (lane == 0) *(volatile LAS unsigned*)(CTL + C_SEL) = 1u;
        }
        }
  }
  if (w == 0 && b < NU) {
    int ln = tid; asm volatile("" : "+v"(ln));
    const unsigned go = (ln >> 3) * (F_ * 2) + (((ln & 7) ^ (((ln >> 4) & 1) << 2)) << 4);
#pragma unroll
    for (int j = 0; j < 16; ++j) glds16((j < 8 ? wg : wu) + ((u & 7) * 64 + (j & 7) * 8) * F_ + (u >> 3) * 64, go, smem + L_GU + j * 1024);
  }
  if (w == 1 && b < NU) {
#pragma unroll
    for (int f = 0; f < 8; ++f) glds16(wd + (u * 8 + f) * D_, lane * 16, smem + (f < 6 ? L_DN + f * 1088 : L_DN2 + (f - 6) * 1088));
  }
  asm volatile("global_load_dwordx4 v[64:67], %1, %5\n\tglobal_load_dwordx4 v[68:71], %2, %5\n\tglobal_load_dwordx4 v[72:75], %3, %5\n\t"
               "global_load_dwordx4 v[76:79], %4, %5\n\tglobal_load_dwordx4 v[80:83], %1, %6\n\tglobal_load_dwordx4 v[84:87], %2, %6\n\t"
               "global_load_dwordx4 v[88:91], %3, %6\n\tglobal_load_dwordx4 v[92:95], %4, %6"
               : "=&{v[64:95]}"(wov) : "v"(row_off), "v"(row_off + 8192), "v"(row_off + 16384), "v"(row_off + 24576),
                 "s"(wo + (size_t)(w * 64) * D_ + hg * 64), "s"(wo + (size_t)(w * 64 + 32) * D_ + hg * 64) : "memory");

#pragma nounroll
  while (i < 8) {
    if (b < NU && ((w == 0 && i == 2) || (w == 1 && i == 4))) {
      int lane = tid & 63, u = (b & 7) * 28 + (b >> 3);
      asm volatile("" : "+v"(lane), "+s"(u));
      const int h = lane >> 5, l31 = lane & 31, blk = (lane >> 4) & 1, q4 = (lane & 15) >> 2, p4 = lane & 3;
      if (i == 2) {
        VMWAIT(8);
        lds_wait_ge(CTL + C_SEL, 1u);
        const char* xb = opq(smem + L_XS + (lane & 1) * 1024 + (u & 3) * 128 + h * 16);
        bf16x8 fx[4];
#pragma unroll
        for (int ks = 0; ks < 4; ++ks) fx[ks] = *(const bf16x8*)(xb + ks * 32);
#pragma unroll
        for (int nt = 0; nt < 2; ++nt) {
          const char* wb = opq(smem + L_GU + (8 * h + q4) * 128 + (((4 * nt + 2 * blk + (p4 >> 1)) ^ (((q4 >> 1) & 1) << 2)) << 4) + 8 * (p4 & 1));
#pragma unroll
          for (int mat = 0; mat < 2; ++mat) {
            f32x16 z = z16;
#pragma unroll
            for (int ks = 0; ks < 4; ++ks) z = QK(fx[ks], tr_frag(wb + mat * 8192 + ks * 2048, wb + mat * 8192 + ks * 2048 + 512), z);
            if (lane < 32) st128(rs, WS_P1 + (unsigned)u * 2048 + (mat * 64 + nt * 32 + lane) * 16, u32x4{__float_as_uint(z[0]), tag, __float_as_uint(z[1]), tag});
            SB();
          }
        }
      } else {
        const unsigned po = WS_P1 + (unsigned)((u & ~7) + (lane >> 3)) * 2048 + ((u & 7) * 8 + (lane & 7)) * 16;
        u32x4 pg, pu;
        SPIN {
          pg = ld128(rs, po); pu = ld128(rs, po + 1024);
          if (__all(pg[1] == tag && pg[3] == tag && pu[1] == tag && pu[3] == tag)) break;
          __builtin_amdgcn_s_sleep(2);
        }
        DRAIN();
        float t[4] = {__uint_as_float(pg[0]), __uint_as_float(pg[2]), __uint_as_float(pu[0]), __uint_as_float(pu[2])};
#pragma unroll
        for (int j = 0; j < 4; ++j) { t[j] += dpp_f<0x128>(t[j]); t[j] = add32(add16(t[j])); }
        const unsigned hp = pack_bf(rbf(silu_f(rbf(t[0]))) * rbf(t[2]), rbf(silu_f(rbf(t[1]))) * rbf(t[3]));
        if (lane < 8) { *(u16*)(smem + L_HS + lane * 2) = (u16)hp; *(u16*)(smem + L_HS + 16 + lane * 2) = (u16)(hp >> 16); }
        u32x4 ha = *(const u32x4*)(smem + L_HS + (lane & 1) * 16);
        if (lane >= 2) ha = u32x4{0u, 0u, 0u, 0u};
        const bf16x8 A = __builtin_bit_cast(bf16x8, ha);
        const int co = (2 * blk + (p4 >> 1)) * 16 + 8 * (p4 & 1);
        const char* db0 = opq(smem + L_DN + q4 * 1088 + co);
        const char* db1 = opq(smem + (q4 < 2 ? L_DN + (4 + q4) * 1088 : L_DN2 + (q4 - 2) * 1088) + co);
#pragma unroll 2
        for (int ct = 0; ct < 16; ++ct) {
          const bf16x8 bw = tr_frag(db0 + ct * 64, db1 + ct * 64);
          const f32x16 z = QK(A, bw, z16);
          auto sw = __builtin_amdgcn_permlane32_swap(__float_as_uint(z[0]), __float_as_uint(z[1]), false, false);
          st32(rs, WS_YP + (unsigned)(h * 64 + ct * 4 + (l31 >> 3)) * (NU * 32) + u * 32 + (l31 & 7) * 4, sw[0]);
        }
        DRAIN();
        if (lane < 8) st32(rs, WS_YF + lane * 1024 + u * 4, tag);
      }
    }
  if (w < 4 && i == 7 && b < NU) __builtin_amdgcn_s_setprio(0);
    STAMP(3, i);
    int stop = 8;
    if (b < NU) {
      if (w < 4 && i < 7) stop = 7;
      if (w == 0 && i < 2) stop = 2;
      if (w == 1 && i < 4) stop = 4;
    }
    if (!fast) {
      stop = i + 1;
      float m = -3.0e38f;
      asm volatile(ROWMAX_ASM : [m] "+v"(m), "=&{v[208:239]}"(sab) : [q] "v"(qf), KV_IN);
      shift = max32(m);
      cc = splat16(-shift);
      asm volatile(PROLOG_ASM : "=&{v[208:239]}"(sab), "+{v[192:207]}"(cc) : [q] "v"(qf), KV_IN);
    }
    asm volatile(".Lfb_loop%=:\n\t" PASSN_ASM POST_ASM "s_cbranch_scc1 .Lfb_loop%=\n.Lfb_out%=:"
                 : "+{v[208:239]}"(sab), RUN_OUT : RUN_IN, [stop] "s"(stop), KV_IN : "scc", "memory");
  }
  {
  int tid = threadIdx.x; asm volatile("" : "+v"(tid));
  const int lane = tid & 63, rr = lane >> 3, c = lane & 7, h = lane >> 5, l31 = lane & 31, blk = (lane >> 4) & 1, q4 = (lane & 15) >> 2, p4 = lane & 3;
  if (w < 4 && b >= NU) {
    SPIN {
      const u32x4 f = ld128(rs, WS_YF + g8 * 1024 + min(lane, 55) * 16);
      if (__all(f[0] == tag && f[1] == tag && f[2] == tag && f[3] == tag)) break;
      __builtin_amdgcn_s_sleep(8);
    }
    CBAR();
    const unsigned rw = (unsigned)((b - NU) * 4 + w);
    const unsigned yo = WS_YP + rw * (NU * 32) + lane * 16;
    f32x4 y = {};
#pragma unroll
    for (int j = 0; j < 7; ++j) y += __builtin_bit_cast(f32x4, ld128(rs, yo + j * 1024));
#pragma unroll
    for (int j = 0; j < 4; ++j) { float t = y[j]; t += dpp_f<0x4E>(t); t += dpp_f<0x124>(t); t += dpp_f<0x128>(t); y[j] = add32(add16(t)); }
    if (lane < 2) {
      st128(rs, WS_YG + rw * 64 + lane * 32, u32x4{__float_as_uint(y[0]), tag, __float_as_uint(y[1]), tag});
      st128(rs, WS_YG + rw * 64 + lane * 32 + 16, u32x4{__float_as_uint(y[2]), tag, __float_as_uint(y[3]), tag});
    }
  }
  __builtin_amdgcn_s_setprio(0);
  DRAIN();
  const u32x4 xe = *(const u32x4*)(x + (size_t)(tb * 64 + (tid >> 3)) * D_ + hg * 64 + c * 8);
  STAMP(1, 2);
  RAW_BARRIER();
  STAMP(1, 3);
  const unsigned fr = (unsigned)((tb * 2 + (w >> 2)) * 32 + hg * 4 + (w & 3));
  {
    float a[8], pl[7], pm[7];
    auto sw = __builtin_amdgcn_permlane32_swap(__float_as_uint(o[8]), __float_as_uint(o[8]), false, false);
    float l = __uint_as_float(sw[0]);
    float mx = shift;
#pragma unroll
    for (int sidx = 0; sidx < 7; ++sidx) {
      const char* sl = smem + w * PREG + sidx * PSLOT + 2048 + l31 * 4;
      pl[sidx] = *(const float*)sl; pm[sidx] = *(const float*)(sl + 128);
      mx = fmaxf(mx, pm[sidx]);
    }
    const float f0 = __builtin_amdgcn_exp2f(shift - mx);
    l *= f0;
#pragma unroll
    for (int r = 0; r < 8; ++r) a[r] = o[r] * f0;
#pragma unroll
    for (int sidx = 0; sidx < 7; ++sidx) {
      const char* sl = smem + w * PREG + sidx * PSLOT;
      const f32x4 p0 = *(const f32x4*)(sl + lane * 16), p1 = *(const f32x4*)(sl + 1024 + lane * 16);
      const float f = __builtin_amdgcn_exp2f(pm[sidx] - mx);
#pragma unroll
      for (int r = 0; r < 4; ++r) { a[r] = fmaf(p0[r], f, a[r]); a[r + 4] = fmaf(p1[r], f, a[r + 4]); }
      l = fmaf(pl[sidx], f, l);
    }
    const float inv = __builtin_amdgcn_rcpf(l);
    const u32x4 of = {min(pack_bf(a[0] * inv, a[1] * inv), 0xfffffffeu), min(pack_bf(a[2] * inv, a[3] * inv), 0xfffffffeu),
                      min(pack_bf(a[4] * inv, a[5] * inv), 0xfffffffeu), min(pack_bf(a[6] * inv, a[7] * inv), 0xfffffffeu)};
    st128(rs, WS_O + fr * 1024 + lane * 16, of);
    *(u32x4*)(smem + L_QI + w * 1024 + lane * 16) = of;
  }
  asm volatile("s_waitcnt lgkmcnt(0)\n\tds_write_b128 %0, v[64:67]\n\tds_write_b128 %0, v[68:71] offset:1024\n\tds_write_b128 %0, v[72:75] offset:2048\n\t"
               "ds_write_b128 %0, v[76:79] offset:3072\n\tds_write_b128 %0, v[80:83] offset:4096\n\tds_write_b128 %0, v[84:87] offset:5120\n\t"
               "ds_write_b128 %0, v[88:91] offset:6144\n\tds_write_b128 %0, v[92:95] offset:7168"
               :: "v"((unsigned)(uintptr_t)(LAS char*)smem + w * PREG + rr * 128 + ((c ^ (((rr >> 1) & 1) << 2)) << 4)), "{v[64:95]}"(wov) : "memory");
  if (b == 0 && tid == 0) st32(rs, WS_EPOCH, tag);
  STAMP(1, 4);
  RAW_BARRIER();
  const int etok = tid >> 3, gtok = tb * 64 + etok;
  {
    const int tt = w >> 2, kq = w & 3;
    bf16x8 fa[2][8];
#pragma unroll
    for (int nt = 0; nt < 2; ++nt) {
      const char* wb = opq(smem + 2 * kq * PREG + (4 * h + q4) * 128 + (((4 * nt + 2 * blk + (p4 >> 1)) ^ (((q4 >> 1) & 1) << 2)) << 4) + 8 * (p4 & 1));
#pragma unroll
      for (int i = 0; i < 8; ++i) fa[nt][i] = tr_frag(wb + (i >> 2) * PREG + (i & 3) * 2048, wb + (i >> 2) * PREG + (i & 3) * 2048 + 1024);
    }
    SB();
    const unsigned fo = WS_O + (unsigned)((tb * 2 + tt) * 32 + kq * 8) * 1024 + lane * 16;
    u32x4 fb[8];
    if (hg != 2 * kq) {
#pragma unroll
      for (int i = 0; i < 4; ++i) fb[i] = ld128(rs, fo + i * 1024);
    } else {
#pragma unroll
      for (int i = 0; i < 4; ++i) fb[i] = *(const u32x4*)(smem + L_QI + (tt * 4 + i) * 1024 + lane * 16);
    }
    if (hg != 2 * kq + 1) {
#pragma unroll
      for (int i = 4; i < 8; ++i) fb[i] = ld128(rs, fo + i * 1024);
    } else {
#pragma unroll
      for (int i = 0; i < 4; ++i) fb[4 + i] = *(const u32x4*)(smem + L_QI + (tt * 4 + i) * 1024 + lane * 16);
    }
    SB();
    STAMP(2, 0);
    {
      const unsigned k1 = *(const unsigned*)(CTL + C_K1), k2 = *(const unsigned*)(CTL + C_K2);
      const int sel = (gtok == key_tok(k1)) ? 0 : ((gtok == key_tok(k2)) ? 1 : -1);
      if (__builtin_expect(__any(sel >= 0), 0)) {
        const f32x2 st = *(const f32x2*)(CTL + C_STAT + etok * 8);
        const float inv = __builtin_amdgcn_rcpf(st[1]);
        const float rsg = rbf(sigm_f(key_val(sel > 0 ? k2 : k1)));
        const unsigned go = WS_YG + (unsigned)((sel > 0 ? 512 : 0) + hg * 64 + c * 8) * 8;
        u32x4 gq[4];
        SPIN {
          bool ok = true;
#pragma unroll
          for (int j = 0; j < 4; ++j) { gq[j] = ld128(rs, go + j * 16); ok = ok && gq[j][1] == tag && gq[j][3] == tag; }
          if (__all(ok)) break;
          __builtin_amdgcn_s_sleep(1);
        }
#pragma unroll
        for (int j = 0; j < 4; ++j) {
          const unsigned xn = ln_pair(xe[j], st[0], inv);
          const float u0 = rbf(rbf(__uint_as_float(gq[j][0])) * rsg), u1 = rbf(rbf(__uint_as_float(gq[j][2])) * rsg);
          if (sel >= 0) gatep[j] = silu_pair(pack_bf(bf_lo(xn) + u0, bf_hi(xn) + u1));
        }
      }
    }
    SB();
    STAMP(2, 1);
    f32x16 acc0 = {}, acc1 = {};
#define FB_MAX(i) max(max(fb[i][0], fb[i][1]), max(fb[i][2], fb[i][3]))
#pragma unroll
    for (int hf = 0; hf < 2; ++hf) {
      if (__builtin_expect(__any(max(max(FB_MAX(4 * hf), FB_MAX(4 * hf + 1)), max(FB_MAX(4 * hf + 2), FB_MAX(4 * hf + 3))) == 0xffffffffu), 0)) {
        SPIN {
          unsigned bad = 0;
#pragma unroll
          for (int i = 4 * hf; i < 8; ++i) bad |= __any(FB_MAX(i) == 0xffffffffu) ? 1u << i : 0u;
          const unsigned need = bad & (15u << (4 * hf));
          if (!need) break;
          SPIN {
            const unsigned pv = ld32(rs, fo - lane * 16 + (lane & 7) * 1024 + 1020);
            if (!__any(pv == 0xffffffffu && ((need >> (lane & 7)) & 1u))) break;
            __builtin_amdgcn_s_sleep(1);
          }
#pragma unroll
          for (int i = 4 * hf; i < 8; ++i) if ((bad >> i) & 1u) fb[i] = ld128(rs, fo + i * 1024);
        }
      }
#pragma unroll
      for (int i = 4 * hf; i < 4 * hf + 4; ++i) {
        acc0 = QK(fa[0][i], __builtin_bit_cast(bf16x8, fb[i]), acc0);
        acc1 = QK(fa[1][i], __builtin_bit_cast(bf16x8, fb[i]), acc1);
      }
      SB();
    }
    STAMP(2, 2);
    char* pr = smem + w * PREG + 8192 + l31 * 256;
#pragma unroll
    for (int g = 0; g < 4; ++g) {
      *(f32x4*)(pr + (((2 * g + h) ^ (l31 & 7)) << 4)) = f32x4{acc0[4 * g], acc0[4 * g + 1], acc0[4 * g + 2], acc0[4 * g + 3]};
      *(f32x4*)(pr + 128 + (((2 * g + h) ^ (l31 & 7)) << 4)) = f32x4{acc1[4 * g], acc1[4 * g + 1], acc1[4 * g + 2], acc1[4 * g + 3]};
    }
  }
  RAW_BARRIER();
  {
    f32x4 s0v = {}, s1v = {};
#pragma unroll
    for (int kq = 0; kq < 4; ++kq) {
      const char* pr = smem + ((w >> 2) * 4 + kq) * PREG + 8192 + (etok & 31) * 256 + (c >> 2) * 128;
      s0v += *(const f32x4*)(pr + ((((2 * c) & 7) ^ rr) << 4)); s1v += *(const f32x4*)(pr + ((((2 * c + 1) & 7) ^ rr) << 4));
    }
    u32x4 ov;
#pragma unroll
    for (int j = 0; j < 4; ++j) {
      const unsigned sv = j < 2 ? pack_bf(s0v[2 * j], s0v[2 * j + 1]) : pack_bf(s1v[2 * j - 4], s1v[2 * j - 3]);
      const unsigned pr2 = pack_bf(bf_lo(sv) * bf_lo(gatep[j]), bf_hi(sv) * bf_hi(gatep[j]));
      ov[j] = pack_bf(bf_lo(xe[j]) + bf_lo(pr2), bf_hi(xe[j]) + bf_hi(pr2));
    }
    *(u32x4*)(out + (size_t)gtok * D_ + hg * 64 + c * 8) = ov;
  }
  }
  asm volatile(".Lfb_end:");
  STAMP(2, 3);
}

extern "C" void kernel_launch(void* const* d_in, const int* in_sizes, int n_in, void* d_out, int out_size, void* d_ws, size_t ws_size,
                              hipStream_t stream) {
  fused_block<<<256, 512, 0, stream>>>((const u16*)d_in[0], (const u16*)d_in[1], (const u16*)d_in[2], (const u16*)d_in[3], (const u16*)d_in[4],
                                       (const u16*)d_in[6], (const u16*)d_in[7], (const u16*)d_in[8], (const u16*)d_in[9], (char*)d_ws, (u16*)d_out);
}
```

```cpp
#include <hip/hip_runtime.h>
#include <hip/hip_bf16.h>
#include <stdint.h>

typedef __attribute__((ext_vector_type(2))) __bf16 bf16x2;
typedef __attribute__((ext_vector_type(8))) __bf16 bf16x8;
typedef __attribute__((ext_vector_type(4))) short s16x4;
typedef __attribute__((ext_vector_type(8))) short s16x8;
typedef __attribute__((ext_vector_type(2))) float f32x2;
typedef __attribute__((ext_vector_type(4))) float f32x4;
typedef __attribute__((ext_vector_type(16))) float f32x16;
typedef __attribute__((ext_vector_type(32))) float f32x32;
typedef __attribute__((ext_vector_type(2))) unsigned u32x2;
typedef __attribute__((ext_vector_type(4))) unsigned u32x4;
typedef unsigned short u16;
typedef __amdgpu_buffer_rsrc_t rsrc_t;
#define LAS __attribute__((address_space(3)))
#define DI __device__ __forceinline__

constexpr int S_ = 2048, D_ = 512, F_ = 1792;
constexpr int NU = 224;
constexpr int VHALF = 256;
constexpr int SC1 = 16;
constexpr unsigned SPIN_MAX = 1u << 18;

constexpr unsigned WS_EPOCH = 0;
constexpr unsigned WS_KVF = 1024;
constexpr unsigned WS_CAND = WS_KVF + 8 * 256 * 8;
constexpr unsigned WS_YF = WS_CAND + 8 * 512 * 8;
constexpr unsigned WS_YG = WS_YF + 8 * 1024;
constexpr unsigned WS_K = WS_YG + 1024 * 8;
constexpr unsigned WS_V = WS_K + S_ * 32;
constexpr unsigned WS_YP = WS_V + 256 * VHALF;
constexpr unsigned WS_P1 = WS_YP + NU * 2 * D_ * 4;
constexpr unsigned WS_O = WS_P1 + NU * 2048;
constexpr unsigned WS_END = WS_O + S_ * D_ * 2;

constexpr int L_WI = 0;
constexpr int L_XI = 65536;
constexpr int L_KI = 131072;
constexpr int PSLOT = 2304;
constexpr int PREG = 16384;
constexpr int L_XS = 7 * PREG;
constexpr int L_GU = 131072;
constexpr int L_QI = 147456;
constexpr int L_CTL = L_QI + 8192;
constexpr int L_DN = L_CTL + 1024;
constexpr int L_DN2 = 7 * PREG + PSLOT + 128;
constexpr int L_HS = 0;
constexpr int L_TOTAL = L_DN + 6 * 1088;
constexpr int C_CNT = 0;
constexpr int C_SEL = 4;
constexpr int C_K1 = 8, C_K2 = 12;
constexpr int C_QMAX = 16;
constexpr int C_R = 32;
constexpr int C_STAT = 64;

#define STAMP(kid, i)

DI float bf_lo(unsigned u) { return __uint_as_float(u << 16); }
DI float bf_hi(unsigned u) { return __uint_as_float(u & 0xffff0000u); }
DI unsigned pack_bf(float a, float b) { f32x2 f = {a, b}; return __builtin_bit_cast(unsigned, __builtin_convertvector(f, bf16x2)); }
DI float rbf(float a) { return (float)(__bf16)a; }
DI float dot2(unsigned a, unsigned b, float c) { return __builtin_amdgcn_fdot2_f32_bf16(__builtin_bit_cast(bf16x2, a), __builtin_bit_cast(bf16x2, b), c, false); }
DI void glds16(const void* sbase, unsigned voff, void* lds_wave_base) {
  unsigned keep; const unsigned dst = (unsigned)(uintptr_t)(LAS char*)lds_wave_base;
  asm volatile("s_mov_b32 %0, m0\n\ts_mov_b32 m0, %2\n\ts_nop 0\n\tglobal_load_lds_dwordx4 %1, %3\n\ts_mov_b32 m0, %0"
               : "=&s"(keep) : "v"(voff), "s"(dst), "s"(sbase) : "memory");
}
#define GLD128(dst, voff, sbase) asm volatile("global_load_dwordx4 %0, %1, %2" : "=v"(dst) : "v"(voff), "s"(sbase) : "memory")
DI const char* opq(const char* p) { unsigned a = (unsigned)(uintptr_t)(LAS char*)p; asm volatile("" : "+v"(a)); return (const char*)(LAS char*)(uintptr_t)a; }
DI s16x4 tr_read(const char* p) { return __builtin_amdgcn_ds_read_tr16_b64_v4i16((LAS s16x4*)p); }
DI bf16x8 tr_frag(const char* p0, const char* p1) {
  s16x4 a = tr_read(p0), b = tr_read(p1);
  s16x8 v = __builtin_shufflevector(a, b, 0, 1, 2, 3, 4, 5, 6, 7);
  return __builtin_bit_cast(bf16x8, v);
}
template <int CTRL> DI float dpp_f(float v) { return __int_as_float(__builtin_amdgcn_update_dpp(0, __float_as_int(v), CTRL, 0xf, 0xf, true)); }
template <int CTRL> DI unsigned dpp_u(unsigned v) { return (unsigned)__builtin_amdgcn_update_dpp(0, (int)v, CTRL, 0xf, 0xf, true); }
DI float sum8(float v) {
  v += dpp_f<0xB1>(v); v += dpp_f<0x4E>(v); v += dpp_f<0x141>(v);
  return v;
}
DI float add16(float v) { auto r = __builtin_amdgcn_permlane16_swap(__float_as_uint(v), __float_as_uint(v), false, false); return __uint_as_float(r[0]) + __uint_as_float(r[1]); }
DI float add32(float v) { auto r = __builtin_amdgcn_permlane32_swap(__float_as_uint(v), __float_as_uint(v), false, false); return __uint_as_float(r[0]) + __uint_as_float(r[1]); }
DI float max16(float v) { auto r = __builtin_amdgcn_permlane16_swap(__float_as_uint(v), __float_as_uint(v), false, false); return fmaxf(__uint_as_float(r[0]), __uint_as_float(r[1])); }
DI float max32(float v) { auto r = __builtin_amdgcn_permlane32_swap(__float_as_uint(v), __float_as_uint(v), false, false); return fmaxf(__uint_as_float(r[0]), __uint_as_float(r[1])); }
DI float sum32(float v) { v = sum8(v); v += dpp_f<0x140>(v); return add16(v); }
DI float wave_sum(float v) { return add32(sum32(v)); }
DI float wave_maxf(float v) {
  v = fmaxf(v, dpp_f<0xB1>(v)); v = fmaxf(v, dpp_f<0x4E>(v)); v = fmaxf(v, dpp_f<0x141>(v)); v = fmaxf(v, dpp_f<0x140>(v));
  return max32(max16(v));
}
DI unsigned wave_maxu(unsigned v) {
  v = max(v, dpp_u<0xB1>(v)); v = max(v, dpp_u<0x4E>(v)); v = max(v, dpp_u<0x141>(v)); v = max(v, dpp_u<0x140>(v));
  auto r = __builtin_amdgcn_permlane16_swap(v, v, false, false); v = max(r[0], r[1]);
  auto q = __builtin_amdgcn_permlane32_swap(v, v, false, false); return max(q[0], q[1]);
}
DI float wave_maxpos(float v) { return __uint_as_float(wave_maxu(__float_as_uint(v))); }
DI float silu_f(float v) { return v * __builtin_amdgcn_rcpf(1.0f + __builtin_amdgcn_exp2f(-1.4426950408889634f * v)); }
DI float sigm_f(float v) { return __builtin_amdgcn_rcpf(1.0f + __builtin_amdgcn_exp2f(-1.4426950408889634f * v)); }
DI float ln_sd(float var) { return rbf(__builtin_amdgcn_sqrtf(rbf(rbf(var) + 1e-5f))); }
DI unsigned ln_pair(unsigned xu, float mb, float inv) {
  const unsigned d = pack_bf(bf_lo(xu) - mb, bf_hi(xu) - mb);
  return pack_bf(bf_lo(d) * inv, bf_hi(d) * inv);
}
DI unsigned silu_pair(unsigned v) { return pack_bf(silu_f(bf_lo(v)), silu_f(bf_hi(v))); }

DI void stats32(const u32x4& a, const u32x4& b2, float& mean, float& var) {
  float s = 0.f, q = 0.f;
#pragma unroll
  for (int j = 0; j < 4; ++j) { s = dot2(a[j], 0x3f803f80u, s); s = dot2(b2[j], 0x3f803f80u, s); q = dot2(a[j], a[j], q); q = dot2(b2[j], b2[j], q); }
  mean = sum32(s) * (1.0f / 512.0f);
  var = fmaf(-mean, mean, sum32(q) * (1.0f / 512.0f));
  if (__builtin_expect(__any(!(var >= mean * mean * 0.015625f)), 0)) {
    float v = 0.f;
#pragma unroll
    for (int j = 0; j < 4; ++j) {
      const float a0 = bf_lo(a[j]) - mean, a1 = bf_hi(a[j]) - mean, a2 = bf_lo(b2[j]) - mean, a3 = bf_hi(b2[j]) - mean;
      v = fmaf(a0, a0, v); v = fmaf(a1, a1, v); v = fmaf(a2, a2, v); v = fmaf(a3, a3, v);
    }
    var = sum32(v) * (1.0f / 512.0f);
  }
}

DI unsigned make_key(float r_bf, int token) {
  unsigned b = __float_as_uint(r_bf) >> 16;
  b = (b & 0x8000u) ? (~b & 0xffffu) : (b | 0x8000u);
  return (b << 16) | (unsigned)(2047 - token);
}
DI float key_val(unsigned key) {
  unsigned b = key >> 16;
  b = (b & 0x8000u) ? (b & 0x7fffu) : (~b & 0xffffu);
  return __uint_as_float(b << 16);
}
DI int key_tok(unsigned key) { return 2047 - (int)(key & 0x7ffu); }

DI u32x4 ld128(rsrc_t rs, unsigned off) { return __builtin_amdgcn_raw_buffer_load_b128(rs, (int)off, 0, SC1); }
DI u32x2 ld64(rsrc_t rs, unsigned off) { return __builtin_amdgcn_raw_buffer_load_b64(rs, (int)off, 0, SC1); }
DI unsigned ld32(rsrc_t rs, unsigned off) { return __builtin_amdgcn_raw_buffer_load_b32(rs, (int)off, 0, SC1); }
DI void st128(rsrc_t rs, unsigned off, u32x4 v) { __builtin_amdgcn_raw_buffer_store_b128(v, rs, (int)off, 0, SC1); }
DI void st64(rsrc_t rs, unsigned off, u32x2 v) { __builtin_amdgcn_raw_buffer_store_b64(v, rs, (int)off, 0, SC1); }
DI void st32(rsrc_t rs, unsigned off, unsigned v) { __builtin_amdgcn_raw_buffer_store_b32(v, rs, (int)off, 0, SC1); }
#define DRAIN() asm volatile("s_waitcnt vmcnt(0)" ::: "memory")
#define VMWAIT(n) asm volatile("s_waitcnt vmcnt(" #n ")" ::: "memory")
#define CBAR() asm volatile("" ::: "memory")
#define RAW_BARRIER() do { asm volatile("s_waitcnt lgkmcnt(0)" ::: "memory"); __builtin_amdgcn_s_barrier(); asm volatile("" ::: "memory"); } while (0)
#define SPIN _Pragma("nounroll") for (unsigned s_ = 0; s_ < SPIN_MAX; ++s_)
DI unsigned lds_ld(const char* p) { return __hip_atomic_load((LAS unsigned*)p, __ATOMIC_RELAXED, __HIP_MEMORY_SCOPE_WORKGROUP); }
DI void lds_wait_ge(const char* p, unsigned want) {
  SPIN {
    if ((unsigned)__builtin_amdgcn_readfirstlane(lds_ld(p)) >= want) break;
    __builtin_amdgcn_s_sleep(1);
  }
  CBAR();
}
#define QK(KF, QF, C) __builtin_amdgcn_mfma_f32_32x32x16_bf16((KF), (QF), (C), 0, 0, 0)
DI f32x16 splat16(float v) { f32x16 r;
#pragma unroll
  for (int i = 0; i < 16; ++i) r[i] = v;
  return r; }
#define SB() __builtin_amdgcn_sched_barrier(0)
#define SGB(mask, n) __builtin_amdgcn_sched_group_barrier(mask, n, 0)

#define VCONST_ASM \
  "v_mov_b32 v132, %[cst]\n\t" "v_mov_b32 v133, %[cst]\n\t" "v_mov_b32 v134, %[cst]\n\t" "v_mov_b32 v135, %[cst]\n\t" "v_mov_b32 v140, %[cst]\n\t" \
  "v_mov_b32 v141, %[cst]\n\t" "v_mov_b32 v142, %[cst]\n\t" "v_mov_b32 v143, %[cst]\n\t" "v_mov_b32 v148, %[cst]\n\t" "v_mov_b32 v149, %[cst]\n\t" \
  "v_mov_b32 v150, %[cst]\n\t" "v_mov_b32 v151, %[cst]\n\t" "v_mov_b32 v156, %[cst]\n\t" "v_mov_b32 v157, %[cst]\n\t" "v_mov_b32 v158, %[cst]\n\t" \
  "v_mov_b32 v159, %[cst]\n\t" "v_mov_b32 v164, %[cst]\n\t" "v_mov_b32 v165, %[cst]\n\t" "v_mov_b32 v166, %[cst]\n\t" "v_mov_b32 v167, %[cst]\n\t" \
  "v_mov_b32 v172, %[cst]\n\t" "v_mov_b32 v173, %[cst]\n\t" "v_mov_b32 v174, %[cst]\n\t" "v_mov_b32 v175, %[cst]\n\t" "v_mov_b32 v180, %[cst]\n\t" \
  "v_mov_b32 v181, %[cst]\n\t" "v_mov_b32 v182, %[cst]\n\t" "v_mov_b32 v183, %[cst]\n\t" "v_mov_b32 v188, %[cst]\n\t" "v_mov_b32 v189, %[cst]\n\t" \
  "v_mov_b32 v190, %[cst]\n\t" "v_mov_b32 v191, %[cst]\n\t" ""
#define LOADS_ASM \
  "buffer_load_dwordx4 v[96:99], %[ka0], %[rs], 0 offen offset:0 sc1\n\t" "buffer_load_dwordx4 v[128:131], %[va0], %[rs], 0 offen offset:0 sc1\n\t" \
  "buffer_load_dwordx4 v[100:103], %[ka0], %[rs], 0 offen offset:1024 sc1\n\t" "buffer_load_dwordx4 v[136:139], %[va0], %[rs], 0 offen offset:1024 sc1\n\t" \
  "buffer_load_dwordx4 v[104:107], %[ka0], %[rs], 0 offen offset:2048 sc1\n\t" "buffer_load_dwordx4 v[144:147], %[va0], %[rs], 0 offen offset:2048 sc1\n\t" \
  "buffer_load_dwordx4 v[108:111], %[ka0], %[rs], 0 offen offset:3072 sc1\n\t" "buffer_load_dwordx4 v[152:155], %[va0], %[rs], 0 offen offset:3072 sc1\n\t" \
  "buffer_load_dwordx4 v[112:115], %[ka1], %[rs], 0 offen offset:0 sc1\n\t" "buffer_load_dwordx4 v[160:163], %[va1], %[rs], 0 offen offset:0 sc1\n\t" \
  "buffer_load_dwordx4 v[116:119], %[ka1], %[rs], 0 offen offset:1024 sc1\n\t" "buffer_load_dwordx4 v[168:171], %[va1], %[rs], 0 offen offset:1024 sc1\n\t" \
  "buffer_load_dwordx4 v[120:123], %[ka1], %[rs], 0 offen offset:2048 sc1\n\t" "buffer_load_dwordx4 v[176:179], %[va1], %[rs], 0 offen offset:2048 sc1\n\t" \
  "buffer_load_dwordx4 v[124:127], %[ka1], %[rs], 0 offen offset:3072 sc1\n\t" "buffer_load_dwordx4 v[184:187], %[va1], %[rs], 0 offen offset:3072 sc1\n\t" ""
#define SWAPS_ASM \
  "v_permlane16_swap_b32 v128, v132\n\t" "v_permlane16_swap_b32 v129, v133\n\t" "v_permlane16_swap_b32 v130, v134\n\t" "v_permlane16_swap_b32 v131, v135\n\t" \
  "v_permlane16_swap_b32 v136, v140\n\t" "v_permlane16_swap_b32 v137, v141\n\t" "v_permlane16_swap_b32 v138, v142\n\t" "v_permlane16_swap_b32 v139, v143\n\t" \
  "v_permlane16_swap_b32 v144, v148\n\t" "v_permlane16_swap_b32 v145, v149\n\t" "v_permlane16_swap_b32 v146, v150\n\t" "v_permlane16_swap_b32 v147, v151\n\t" \
  "v_permlane16_swap_b32 v152, v156\n\t" "v_permlane16_swap_b32 v153, v157\n\t" "v_permlane16_swap_b32 v154, v158\n\t" "v_permlane16_swap_b32 v155, v159\n\t" \
  "v_permlane16_swap_b32 v160, v164\n\t" "v_permlane16_swap_b32 v161, v165\n\t" "v_permlane16_swap_b32 v162, v166\n\t" "v_permlane16_swap_b32 v163, v167\n\t" \
  "v_permlane16_swap_b32 v168, v172\n\t" "v_permlane16_swap_b32 v169, v173\n\t" "v_permlane16_swap_b32 v170, v174\n\t" "v_permlane16_swap_b32 v171, v175\n\t" \
  "v_permlane16_swap_b32 v176, v180\n\t" "v_permlane16_swap_b32 v177, v181\n\t" "v_permlane16_swap_b32 v178, v182\n\t" "v_permlane16_swap_b32 v179, v183\n\t" \
  "v_permlane16_swap_b32 v184, v188\n\t" "v_permlane16_swap_b32 v185, v189\n\t" "v_permlane16_swap_b32 v186, v190\n\t" "v_permlane16_swap_b32 v187, v191\n\t" ""
#define PROLOG0_ASM \
  "s_waitcnt vmcnt(15)\n\t" "s_nop 1\n\t" "v_mfma_f32_32x32x16_bf16 v[208:223], v[96:99], %[q], v[192:207]\n\t" "s_waitcnt vmcnt(13)\n\t" \
  "v_mfma_f32_32x32x16_bf16 v[224:239], v[100:103], %[q], v[192:207]\n\t" "v_permlane16_swap_b32 v128, v132\n\t" "v_permlane16_swap_b32 v129, v133\n\t" \
  "v_permlane16_swap_b32 v130, v134\n\t" "v_permlane16_swap_b32 v131, v135\n\t" "s_nop 6\n\t" "v_exp_f32 v208, v208\n\t" "v_exp_f32 v209, v209\n\t" \
  "v_exp_f32 v210, v210\n\t" "v_exp_f32 v211, v211\n\t" "v_exp_f32 v212, v212\n\t" "v_exp_f32 v213, v213\n\t" "v_exp_f32 v214, v214\n\t" "v_exp_f32 v215, v215\n\t" \
  "v_exp_f32 v216, v216\n\t" "v_exp_f32 v217, v217\n\t" "v_exp_f32 v218, v218\n\t" "v_exp_f32 v219, v219\n\t" "v_exp_f32 v220, v220\n\t" ""
#define PROLOG_ASM \
  "s_nop 1\n\t" "v_mfma_f32_32x32x16_bf16 v[208:223], v[96:99], %[q], v[192:207]\n\t" "v_mfma_f32_32x32x16_bf16 v[224:239], v[100:103], %[q], v[192:207]\n\t" \
  "s_nop 10\n\t" "v_exp_f32 v208, v208\n\t" "v_exp_f32 v209, v209\n\t" "v_exp_f32 v210, v210\n\t" "v_exp_f32 v211, v211\n\t" "v_exp_f32 v212, v212\n\t" \
  "v_exp_f32 v213, v213\n\t" "v_exp_f32 v214, v214\n\t" "v_exp_f32 v215, v215\n\t" "v_exp_f32 v216, v216\n\t" "v_exp_f32 v217, v217\n\t" "v_exp_f32 v218, v218\n\t" \
  "v_exp_f32 v219, v219\n\t" "v_exp_f32 v220, v220\n\t" ""
#define PASS0_ASM \
  "v_mfma_f32_32x32x16_bf16 v[240:255], %[z], %[z], 0\n\t" "v_exp_f32 v221, v221\n\t" "v_exp_f32 v222, v222\n\t" "v_exp_f32 v223, v223\n\t" \
  "v_smfmac_f32_32x32x32_bf16 v[240:255], v[128:131], v[208:215], %[idx]\n\t" "v_exp_f32 v224, v224\n\t" "v_exp_f32 v225, v225\n\t" \
  "v_smfmac_f32_32x32x32_bf16 v[240:255], v[132:135], v[216:223], %[idx]\n\t" "v_exp_f32 v226, v226\n\t" "v_exp_f32 v227, v227\n\t" "v_exp_f32 v228, v228\n\t" \
  "s_waitcnt vmcnt(11)\n\t" "v_mfma_f32_32x32x16_bf16 v[208:223], v[104:107], %[q], v[192:207]\n\t" "v_permlane16_swap_b32 v136, v140\n\t" \
  "v_permlane16_swap_b32 v137, v141\n\t" "v_permlane16_swap_b32 v138, v142\n\t" "v_permlane16_swap_b32 v139, v143\n\t" "v_exp_f32 v229, v229\n\t" \
  "v_exp_f32 v230, v230\n\t" "v_exp_f32 v231, v231\n\t" "v_exp_f32 v232, v232\n\t" "v_exp_f32 v233, v233\n\t" \
  "v_smfmac_f32_32x32x32_bf16 v[240:255], v[136:139], v[224:231], %[idx]\n\t" "v_exp_f32 v234, v234\n\t" "v_exp_f32 v235, v235\n\t" "v_exp_f32 v236, v236\n\t" \
  "v_exp_f32 v237, v237\n\t" "v_exp_f32 v238, v238\n\t" "v_exp_f32 v239, v239\n\t" "v_exp_f32 v208, v208\n\t" "v_exp_f32 v209, v209\n\t" \
  "v_smfmac_f32_32x32x32_bf16 v[240:255], v[140:143], v[232:239], %[idx]\n\t" "v_exp_f32 v210, v210\n\t" "v_exp_f32 v211, v211\n\t" "v_exp_f32 v212, v212\n\t" \
  "s_waitcnt vmcnt(9)\n\t" "v_mfma_f32_32x32x16_bf16 v[224:239], v[108:111], %[q], v[192:207]\n\t" "v_permlane16_swap_b32 v144, v148\n\t" \
  "v_permlane16_swap_b32 v145, v149\n\t" "v_permlane16_swap_b32 v146, v150\n\t" "v_permlane16_swap_b32 v147, v151\n\t" "v_exp_f32 v213, v213\n\t" \
  "v_exp_f32 v214, v214\n\t" "v_exp_f32 v215, v215\n\t" "v_exp_f32 v216, v216\n\t" "v_exp_f32 v217, v217\n\t" \
  "v_smfmac_f32_32x32x32_bf16 v[240:255], v[144:147], v[208:215], %[idx]\n\t" "v_exp_f32 v218, v218\n\t" "v_exp_f32 v219, v219\n\t" "v_exp_f32 v220, v220\n\t" \
  "v_exp_f32 v221, v221\n\t" "v_exp_f32 v222, v222\n\t" "v_exp_f32 v223, v223\n\t" "v_exp_f32 v224, v224\n\t" "v_exp_f32 v225, v225\n\t" \
  "v_smfmac_f32_32x32x32_bf16 v[240:255], v[148:151], v[216:223], %[idx]\n\t" "v_exp_f32 v226, v226\n\t" "v_exp_f32 v227, v227\n\t" "v_exp_f32 v228, v228\n\t" \
  "s_waitcnt vmcnt(7)\n\t" "v_mfma_f32_32x32x16_bf16 v[208:223], v[112:115], %[q], v[192:207]\n\t" "v_permlane16_swap_b32 v152, v156\n\t" \
  "v_permlane16_swap_b32 v153, v157\n\t" "v_permlane16_swap_b32 v154, v158\n\t" "v_permlane16_swap_b32 v155, v159\n\t" "v_exp_f32 v229, v229\n\t" \
  "v_exp_f32 v230, v230\n\t" "v_exp_f32 v231, v231\n\t" "v_exp_f32 v232, v232\n\t" "v_exp_f32 v233, v233\n\t" \
  "v_smfmac_f32_32x32x32_bf16 v[240:255], v[152:155], v[224:231], %[idx]\n\t" "v_exp_f32 v234, v234\n\t" "v_exp_f32 v235, v235\n\t" "v_exp_f32 v236, v236\n\t" \
  "v_exp_f32 v237, v237\n\t" "v_exp_f32 v238, v238\n\t" "v_exp_f32 v239, v239\n\t" "v_exp_f32 v208, v208\n\t" "v_exp_f32 v209, v209\n\t" \
  "v_smfmac_f32_32x32x32_bf16 v[240:255], v[156:159], v[232:239], %[idx]\n\t" "v_exp_f32 v210, v210\n\t" "v_exp_f32 v211, v211\n\t" "v_exp_f32 v212, v212\n\t" \
  "s_waitcnt vmcnt(5)\n\t" "v_mfma_f32_32x32x16_bf16 v[224:239], v[116:119], %[q], v[192:207]\n\t" "v_permlane16_swap_b32 v160, v164\n\t" \
  "v_permlane16_swap_b32 v161, v165\n\t" "v_permlane16_swap_b32 v162, v166\n\t" "v_permlane16_swap_b32 v163, v167\n\t" "v_exp_f32 v213, v213\n\t" \
  "v_exp_f32 v214, v214\n\t" "v_exp_f32 v215, v215\n\t" "v_exp_f32 v216, v216\n\t" "v_exp_f32 v217, v217\n\t" \
  "v_smfmac_f32_32x32x32_bf16 v[240:255], v[160:163], v[208:215], %[idx]\n\t" "v_exp_f32 v218, v218\n\t" "v_exp_f32 v219, v219\n\t" "v_exp_f32 v220, v220\n\t" \
  "v_exp_f32 v221, v221\n\t" "v_exp_f32 v222, v222\n\t" "v_exp_f32 v223, v223\n\t" "v_exp_f32 v224, v224\n\t" "v_exp_f32 v225, v225\n\t" \
  "v_smfmac_f32_32x32x32_bf16 v[240:255], v[164:167], v[216:223], %[idx]\n\t" "v_exp_f32 v226, v226\n\t" "v_exp_f32 v227, v227\n\t" "v_exp_f32 v228, v228\n\t" \
  "s_waitcnt vmcnt(3)\n\t" "v_mfma_f32_32x32x16_bf16 v[208:223], v[120:123], %[q], v[192:207]\n\t" "v_permlane16_swap_b32 v168, v172\n\t" \
  "v_permlane16_swap_b32 v169, v173\n\t" "v_permlane16_swap_b32 v170, v174\n\t" "v_permlane16_swap_b32 v171, v175\n\t" "v_exp_f32 v229, v229\n\t" \
  "v_exp_f32 v230, v230\n\t" "v_exp_f32 v231, v231\n\t" "v_exp_f32 v232, v232\n\t" "v_exp_f32 v233, v233\n\t" \
  "v_smfmac_f32_32x32x32_bf16 v[240:255], v[168:171], v[224:231], %[idx]\n\t" "v_exp_f32 v234, v234\n\t" "v_exp_f32 v235, v235\n\t" "v_exp_f32 v236, v236\n\t" \
  "v_exp_f32 v237, v237\n\t" "v_exp_f32 v238, v238\n\t" "v_exp_f32 v239, v239\n\t" "v_exp_f32 v208, v208\n\t" "v_exp_f32 v209, v209\n\t" \
  "v_smfmac_f32_32x32x32_bf16 v[240:255], v[172:175], v[232:239], %[idx]\n\t" "v_exp_f32 v210, v210\n\t" "v_exp_f32 v211, v211\n\t" "v_exp_f32 v212, v212\n\t" \
  "s_waitcnt vmcnt(1)\n\t" "v_mfma_f32_32x32x16_bf16 v[224:239], v[124:127], %[q], v[192:207]\n\t" "ds_read_b128 %[q], %[qa]\n\t" "v_permlane16_swap_b32 v176, v180\n\t" \
  "v_permlane16_swap_b32 v177, v181\n\t" "v_permlane16_swap_b32 v178, v182\n\t" "v_permlane16_swap_b32 v179, v183\n\t" "v_exp_f32 v213, v213\n\t" \
  "v_exp_f32 v214, v214\n\t" "v_exp_f32 v215, v215\n\t" "v_exp_f32 v216, v216\n\t" "v_exp_f32 v217, v217\n\t" \
  "v_smfmac_f32_32x32x32_bf16 v[240:255], v[176:179], v[208:215], %[idx]\n\t" "v_exp_f32 v218, v218\n\t" "v_exp_f32 v219, v219\n\t" "v_exp_f32 v220, v220\n\t" \
  "v_exp_f32 v221, v221\n\t" "v_exp_f32 v222, v222\n\t" "v_exp_f32 v223, v223\n\t" "v_exp_f32 v224, v224\n\t" "v_exp_f32 v225, v225\n\t" \
  "v_smfmac_f32_32x32x32_bf16 v[240:255], v[180:183], v[216:223], %[idx]\n\t" "v_exp_f32 v226, v226\n\t" "v_exp_f32 v227, v227\n\t" "v_exp_f32 v228, v228\n\t" \
  "s_waitcnt vmcnt(0) lgkmcnt(0)\n\t" "v_mfma_f32_32x32x16_bf16 v[208:223], v[96:99], %[q], v[192:207]\n\t" "v_permlane16_swap_b32 v184, v188\n\t" \
  "v_permlane16_swap_b32 v185, v189\n\t" "v_permlane16_swap_b32 v186, v190\n\t" "v_permlane16_swap_b32 v187, v191\n\t" "v_exp_f32 v229, v229\n\t" \
  "v_exp_f32 v230, v230\n\t" "v_exp_f32 v231, v231\n\t" "v_exp_f32 v232, v232\n\t" "v_exp_f32 v233, v233\n\t" \
  "v_smfmac_f32_32x32x32_bf16 v[240:255], v[184:187], v[224:231], %[idx]\n\t" "v_exp_f32 v234, v234\n\t" "v_exp_f32 v235, v235\n\t" "v_exp_f32 v236, v236\n\t" \
  "v_exp_f32 v237, v237\n\t" "v_exp_f32 v238, v238\n\t" "v_exp_f32 v239, v239\n\t" "v_exp_f32 v208, v208\n\t" "v_exp_f32 v209, v209\n\t" \
  "v_smfmac_f32_32x32x32_bf16 v[240:255], v[188:191], v[232:239], %[idx]\n\t" "v_exp_f32 v210, v210\n\t" "v_exp_f32 v211, v211\n\t" "v_exp_f32 v212, v212\n\t" \
  "v_mfma_f32_32x32x16_bf16 v[224:239], v[100:103], %[q], v[192:207]\n\t" "v_exp_f32 v213, v213\n\t" "v_exp_f32 v214, v214\n\t" "v_exp_f32 v215, v215\n\t" \
  "v_exp_f32 v216, v216\n\t" "v_exp_f32 v217, v217\n\t" "v_exp_f32 v218, v218\n\t" "v_exp_f32 v219, v219\n\t" "v_exp_f32 v220, v220\n\t" ""
#define PASSN_ASM \
  "v_mfma_f32_32x32x16_bf16 v[240:255], %[z], %[z], 0\n\t" "v_exp_f32 v221, v221\n\t" "v_exp_f32 v222, v222\n\t" "v_exp_f32 v223, v223\n\t" \
  "v_smfmac_f32_32x32x32_bf16 v[240:255], v[128:131], v[208:215], %[idx]\n\t" "v_exp_f32 v224, v224\n\t" "v_exp_f32 v225, v225\n\t" \
  "v_smfmac_f32_32x32x32_bf16 v[240:255], v[132:135], v[216:223], %[idx]\n\t" "v_exp_f32 v226, v226\n\t" "v_exp_f32 v227, v227\n\t" "v_exp_f32 v228, v228\n\t" \
  "v_mfma_f32_32x32x16_bf16 v[208:223], v[104:107], %[q], v[192:207]\n\t" "v_exp_f32 v229, v229\n\t" "v_exp_f32 v230, v230\n\t" "v_exp_f32 v231, v231\n\t" \
  "v_exp_f32 v232, v232\n\t" "v_exp_f32 v233, v233\n\t" "v_smfmac_f32_32x32x32_bf16 v[240:255], v[136:139], v[224:231], %[idx]\n\t" "v_exp_f32 v234, v234\n\t" \
  "v_exp_f32 v235, v235\n\t" "v_exp_f32 v236, v236\n\t" "v_exp_f32 v237, v237\n\t" "v_exp_f32 v238, v238\n\t" "v_exp_f32 v239, v239\n\t" "v_exp_f32 v208, v208\n\t" \
  "v_exp_f32 v209, v209\n\t" "v_smfmac_f32_32x32x32_bf16 v[240:255], v[140:143], v[232:239], %[idx]\n\t" "v_exp_f32 v210, v210\n\t" "v_exp_f32 v211, v211\n\t" \
  "v_exp_f32 v212, v212\n\t" "v_mfma_f32_32x32x16_bf16 v[224:239], v[108:111], %[q], v[192:207]\n\t" "v_exp_f32 v213, v213\n\t" "v_exp_f32 v214, v214\n\t" \
  "v_exp_f32 v215, v215\n\t" "v_exp_f32 v216, v216\n\t" "v_exp_f32 v217, v217\n\t" "v_smfmac_f32_32x32x32_bf16 v[240:255], v[144:147], v[208:215], %[idx]\n\t" \
  "v_exp_f32 v218, v218\n\t" "v_exp_f32 v219, v219\n\t" "v_exp_f32 v220, v220\n\t" "v_exp_f32 v221, v221\n\t" "v_exp_f32 v222, v222\n\t" "v_exp_f32 v223, v223\n\t" \
  "v_exp_f32 v224, v224\n\t" "v_exp_f32 v225, v225\n\t" "v_smfmac_f32_32x32x32_bf16 v[240:255], v[148:151], v[216:223], %[idx]\n\t" "v_exp_f32 v226, v226\n\t" \
  "v_exp_f32 v227, v227\n\t" "v_exp_f32 v228, v228\n\t" "v_mfma_f32_32x32x16_bf16 v[208:223], v[112:115], %[q], v[192:207]\n\t" "v_exp_f32 v229, v229\n\t" \
  "v_exp_f32 v230, v230\n\t" "v_exp_f32 v231, v231\n\t" "v_exp_f32 v232, v232\n\t" "v_exp_f32 v233, v233\n\t" \
  "v_smfmac_f32_32x32x32_bf16 v[240:255], v[152:155], v[224:231], %[idx]\n\t" "v_exp_f32 v234, v234\n\t" "v_exp_f32 v235, v235\n\t" "v_exp_f32 v236, v236\n\t" \
  "v_exp_f32 v237, v237\n\t" "v_exp_f32 v238, v238\n\t" "v_exp_f32 v239, v239\n\t" "v_exp_f32 v208, v208\n\t" "v_exp_f32 v209, v209\n\t" \
  "v_smfmac_f32_32x32x32_bf16 v[240:255], v[156:159], v[232:239], %[idx]\n\t" "v_exp_f32 v210, v210\n\t" "v_exp_f32 v211, v211\n\t" "v_exp_f32 v212, v212\n\t" \
  "v_mfma_f32_32x32x16_bf16 v[224:239], v[116:119], %[q], v[192:207]\n\t" "v_exp_f32 v213, v213\n\t" "v_exp_f32 v214, v214\n\t" "v_exp_f32 v215, v215\n\t" \
  "v_exp_f32 v216, v216\n\t" "v_exp_f32 v217, v217\n\t" "v_smfmac_f32_32x32x32_bf16 v[240:255], v[160:163], v[208:215], %[idx]\n\t" "v_exp_f32 v218, v218\n\t" \
  "v_exp_f32 v219, v219\n\t" "v_exp_f32 v220, v220\n\t" "v_exp_f32 v221, v221\n\t" "v_exp_f32 v222, v222\n\t" "v_exp_f32 v223, v223\n\t" "v_exp_f32 v224, v224\n\t" \
  "v_exp_f32 v225, v225\n\t" "v_smfmac_f32_32x32x32_bf16 v[240:255], v[164:167], v[216:223], %[idx]\n\t" "v_exp_f32 v226, v226\n\t" "v_exp_f32 v227, v227\n\t" \
  "v_exp_f32 v228, v228\n\t" "v_mfma_f32_32x32x16_bf16 v[208:223], v[120:123], %[q], v[192:207]\n\t" "v_exp_f32 v229, v229\n\t" "v_exp_f32 v230, v230\n\t" \
  "v_exp_f32 v231, v231\n\t" "v_exp_f32 v232, v232\n\t" "v_exp_f32 v233, v233\n\t" "v_smfmac_f32_32x32x32_bf16 v[240:255], v[168:171], v[224:231], %[idx]\n\t" \
  "v_exp_f32 v234, v234\n\t" "v_exp_f32 v235, v235\n\t" "v_exp_f32 v236, v236\n\t" "v_exp_f32 v237, v237\n\t" "v_exp_f32 v238, v238\n\t" "v_exp_f32 v239, v239\n\t" \
  "v_exp_f32 v208, v208\n\t" "v_exp_f32 v209, v209\n\t" "v_smfmac_f32_32x32x32_bf16 v[240:255], v[172:175], v[232:239], %[idx]\n\t" "v_exp_f32 v210, v210\n\t" \
  "v_exp_f32 v211, v211\n\t" "v_exp_f32 v212, v212\n\t" "v_mfma_f32_32x32x16_bf16 v[224:239], v[124:127], %[q], v[192:207]\n\t" "ds_read_b128 %[q], %[qa]\n\t" \
  "v_exp_f32 v213, v213\n\t" "v_exp_f32 v214, v214\n\t" "v_exp_f32 v215, v215\n\t" "v_exp_f32 v216, v216\n\t" "v_exp_f32 v217, v217\n\t" \
  "v_smfmac_f32_32x32x32_bf16 v[240:255], v[176:179], v[208:215], %[idx]\n\t" "v_exp_f32 v218, v218\n\t" "v_exp_f32 v219, v219\n\t" "v_exp_f32 v220, v220\n\t" \
  "v_exp_f32 v221, v221\n\t" "v_exp_f32 v222, v222\n\t" "v_exp_f32 v223, v223\n\t" "v_exp_f32 v224, v224\n\t" "v_exp_f32 v225, v225\n\t" \
  "v_smfmac_f32_32x32x32_bf16 v[240:255], v[180:183], v[216:223], %[idx]\n\t" "v_exp_f32 v226, v226\n\t" "v_exp_f32 v227, v227\n\t" "v_exp_f32 v228, v228\n\t" \
  "s_waitcnt lgkmcnt(0)\n\t" "v_mfma_f32_32x32x16_bf16 v[208:223], v[96:99], %[q], v[192:207]\n\t" "v_exp_f32 v229, v229\n\t" "v_exp_f32 v230, v230\n\t" \
  "v_exp_f32 v231, v231\n\t" "v_exp_f32 v232, v232\n\t" "v_exp_f32 v233, v233\n\t" "v_smfmac_f32_32x32x32_bf16 v[240:255], v[184:187], v[224:231], %[idx]\n\t" \
  "v_exp_f32 v234, v234\n\t" "v_exp_f32 v235, v235\n\t" "v_exp_f32 v236, v236\n\t" "v_exp_f32 v237, v237\n\t" "v_exp_f32 v238, v238\n\t" "v_exp_f32 v239, v239\n\t" \
  "v_exp_f32 v208, v208\n\t" "v_exp_f32 v209, v209\n\t" "v_smfmac_f32_32x32x32_bf16 v[240:255], v[188:191], v[232:239], %[idx]\n\t" "v_exp_f32 v210, v210\n\t" \
  "v_exp_f32 v211, v211\n\t" "v_exp_f32 v212, v212\n\t" "v_mfma_f32_32x32x16_bf16 v[224:239], v[100:103], %[q], v[192:207]\n\t" "v_exp_f32 v213, v213\n\t" \
  "v_exp_f32 v214, v214\n\t" "v_exp_f32 v215, v215\n\t" "v_exp_f32 v216, v216\n\t" "v_exp_f32 v217, v217\n\t" "v_exp_f32 v218, v218\n\t" "v_exp_f32 v219, v219\n\t" \
  "v_exp_f32 v220, v220\n\t" ""
#define ROWMAX_ASM \
  "s_nop 1\n\t" "v_mfma_f32_32x32x16_bf16 v[224:239], v[96:99], %[q], 0\n\t" "s_nop 11\n\t" "v_max3_f32 %[m], %[m], v224, v225\n\t" \
  "v_max3_f32 %[m], %[m], v226, v227\n\t" "v_max3_f32 %[m], %[m], v228, v229\n\t" "v_max3_f32 %[m], %[m], v230, v231\n\t" "v_max3_f32 %[m], %[m], v232, v233\n\t" \
  "v_max3_f32 %[m], %[m], v234, v235\n\t" "v_max3_f32 %[m], %[m], v236, v237\n\t" "v_max3_f32 %[m], %[m], v238, v239\n\t" \
  "v_mfma_f32_32x32x16_bf16 v[224:239], v[100:103], %[q], 0\n\t" "s_nop 11\n\t" "v_max3_f32 %[m], %[m], v224, v225\n\t" "v_max3_f32 %[m], %[m], v226, v227\n\t" \
  "v_max3_f32 %[m], %[m], v228, v229\n\t" "v_max3_f32 %[m], %[m], v230, v231\n\t" "v_max3_f32 %[m], %[m], v232, v233\n\t" "v_max3_f32 %[m], %[m], v234, v235\n\t" \
  "v_max3_f32 %[m], %[m], v236, v237\n\t" "v_max3_f32 %[m], %[m], v238, v239\n\t" "v_mfma_f32_32x32x16_bf16 v[224:239], v[104:107], %[q], 0\n\t" "s_nop 11\n\t" \
  "v_max3_f32 %[m], %[m], v224, v225\n\t" "v_max3_f32 %[m], %[m], v226, v227\n\t" "v_max3_f32 %[m], %[m], v228, v229\n\t" "v_max3_f32 %[m], %[m], v230, v231\n\t" \
  "v_max3_f32 %[m], %[m], v232, v233\n\t" "v_max3_f32 %[m], %[m], v234, v235\n\t" "v_max3_f32 %[m], %[m], v236, v237\n\t" "v_max3_f32 %[m], %[m], v238, v239\n\t" \
  "v_mfma_f32_32x32x16_bf16 v[224:239], v[108:111], %[q], 0\n\t" "s_nop 11\n\t" "v_max3_f32 %[m], %[m], v224, v225\n\t" "v_max3_f32 %[m], %[m], v226, v227\n\t" \
  "v_max3_f32 %[m], %[m], v228, v229\n\t" "v_max3_f32 %[m], %[m], v230, v231\n\t" "v_max3_f32 %[m], %[m], v232, v233\n\t" "v_max3_f32 %[m], %[m], v234, v235\n\t" \
  "v_max3_f32 %[m], %[m], v236, v237\n\t" "v_max3_f32 %[m], %[m], v238, v239\n\t" "v_mfma_f32_32x32x16_bf16 v[224:239], v[112:115], %[q], 0\n\t" "s_nop 11\n\t" \
  "v_max3_f32 %[m], %[m], v224, v225\n\t" "v_max3_f32 %[m], %[m], v226, v227\n\t" "v_max3_f32 %[m], %[m], v228, v229\n\t" "v_max3_f32 %[m], %[m], v230, v231\n\t" \
  "v_max3_f32 %[m], %[m], v232, v233\n\t" "v_max3_f32 %[m], %[m], v234, v235\n\t" "v_max3_f32 %[m], %[m], v236, v237\n\t" "v_max3_f32 %[m], %[m], v238, v239\n\t" \
  "v_mfma_f32_32x32x16_bf16 v[224:239], v[116:119], %[q], 0\n\t" "s_nop 11\n\t" "v_max3_f32 %[m], %[m], v224, v225\n\t" "v_max3_f32 %[m], %[m], v226, v227\n\t" \
  "v_max3_f32 %[m], %[m], v228, v229\n\t" "v_max3_f32 %[m], %[m], v230, v231\n\t" "v_max3_f32 %[m], %[m], v232, v233\n\t" "v_max3_f32 %[m], %[m], v234, v235\n\t" \
  "v_max3_f32 %[m], %[m], v236, v237\n\t" "v_max3_f32 %[m], %[m], v238, v239\n\t" "v_mfma_f32_32x32x16_bf16 v[224:239], v[120:123], %[q], 0\n\t" "s_nop 11\n\t" \
  "v_max3_f32 %[m], %[m], v224, v225\n\t" "v_max3_f32 %[m], %[m], v226, v227\n\t" "v_max3_f32 %[m], %[m], v228, v229\n\t" "v_max3_f32 %[m], %[m], v230, v231\n\t" \
  "v_max3_f32 %[m], %[m], v232, v233\n\t" "v_max3_f32 %[m], %[m], v234, v235\n\t" "v_max3_f32 %[m], %[m], v236, v237\n\t" "v_max3_f32 %[m], %[m], v238, v239\n\t" \
  "v_mfma_f32_32x32x16_bf16 v[224:239], v[124:127], %[q], 0\n\t" "s_nop 11\n\t" "v_max3_f32 %[m], %[m], v224, v225\n\t" "v_max3_f32 %[m], %[m], v226, v227\n\t" \
  "v_max3_f32 %[m], %[m], v228, v229\n\t" "v_max3_f32 %[m], %[m], v230, v231\n\t" "v_max3_f32 %[m], %[m], v232, v233\n\t" "v_max3_f32 %[m], %[m], v234, v235\n\t" \
  "v_max3_f32 %[m], %[m], v236, v237\n\t" "v_max3_f32 %[m], %[m], v238, v239\n\t" ""
#define POST_ASM \
  "s_add_u32 %[i], %[i], 1\n\t" "s_cmp_eq_u32 %[i], 8\n\t" "s_cbranch_scc1 .Lfb_out%=\n\t" "v_cndmask_b32 %[t1], %[sh], v248, %[lo]\n\t" \
  "v_add_u32 %[t2], %[so], %[l16]\n\t" "ds_write_b128 %[t2], v[240:243]\n\t" "ds_write_b128 %[t2], v[244:247] offset:1024\n\t" "v_add_u32 %[t2], %[so], %[l4]\n\t" \
  "ds_write_b32 %[t2], %[t1] offset:2048\n\t" "s_add_u32 %[so], %[so], 14080\n\t" "s_and_b32 %[so], %[so], 0x1ffff\n\t" "s_add_u32 %[qo], %[qo], 1024\n\t" \
  "s_and_b32 %[qo], %[qo], 0x1fff\n\t" "v_add_u32 %[qa], %[qo], %[qb]\n\t" ""
#define WOLOAD_ASM \
  "s_cmp_lg_u32 %[i], %[iw]\n\t" "s_cbranch_scc1 .Lfb_skip%=\n\t" "s_mov_b64 %[wb2], %[wb]\n\t" "global_load_dwordx4 v[64:67], %[ro], %[wb2]\n\t" \
  "v_add_u32 %[ro], 0x2000, %[ro]\n\t" "global_load_dwordx4 v[68:71], %[ro], %[wb2]\n\t" "v_add_u32 %[ro], 0x2000, %[ro]\n\t" \
  "global_load_dwordx4 v[72:75], %[ro], %[wb2]\n\t" "v_add_u32 %[ro], 0x2000, %[ro]\n\t" "global_load_dwordx4 v[76:79], %[ro], %[wb2]\n\t" \
  "v_add_u32 %[ro], 0x2000, %[ro]\n\t" "global_load_dwordx4 v[80:83], %[ro], %[wb2]\n\t" "v_add_u32 %[ro], 0x2000, %[ro]\n\t" \
  "global_load_dwordx4 v[84:87], %[ro], %[wb2]\n\t" "v_add_u32 %[ro], 0x2000, %[ro]\n\t" "global_load_dwordx4 v[88:91], %[ro], %[wb2]\n\t" \
  "v_add_u32 %[ro], 0x2000, %[ro]\n\t" "global_load_dwordx4 v[92:95], %[ro], %[wb2]\n\t" ""

#define KV_OUT "=&{v[96:127]}"(kv0), "=&{v[128:159]}"(kv1), "=&{v[160:191]}"(kv2)
#define KV_IO "+{v[96:127]}"(kv0), "+{v[128:159]}"(kv1), "+{v[160:191]}"(kv2)
#define KV_IN "{v[96:127]}"(kv0), "{v[128:159]}"(kv1), "{v[160:191]}"(kv2)
#define KV_ADDR [ka0] "v"(kb), [ka1] "v"(kb + 4096), [va0] "v"(vp), [va1] "v"(vp + 4096), [rs] "s"(rs)

__global__ __launch_bounds__(512, 2) void fused_block(const u16* __restrict__ x, const u16* __restrict__ wq, const u16* __restrict__ wk,
                                                      const u16* __restrict__ wv, const u16* __restrict__ wo, const u16* __restrict__ wr,
                                                      const u16* __restrict__ wg, const u16* __restrict__ wu, const u16* __restrict__ wd,
                                                      char* ws, u16* __restrict__ out) {
  __shared__ __attribute__((aligned(16))) char smem[L_TOTAL];
  unsigned pf1, pf2;
  {
    unsigned long long pc; unsigned t;
    asm volatile("s_getpc_b64 %[pc]\n.Lfb_begin:\n\tv_readfirstlane_b32 %[t], %[tid]\n\ts_cmp_lg_u32 %[t], 0\n\ts_cbranch_scc1 .Lfb_nopf\n\t"
                 "v_and_b32 %[a], 15, %[tid]\n\tv_lshlrev_b32 %[b], 7, %[a]\n\tglobal_load_dword %[b], %[b], %[pc]\n\t"
                 "s_lshr_b32 %[t], %[bx], 3\n\ts_mul_i32 %[t], %[t], 1152\n\ts_add_u32 %[t], %[t], 2048\n\t"
                 "v_min_u32 %[a], 8, %[a]\n\tv_lshl_add_u32 %[a], %[a], 7, %[t]\n\tv_min_u32 %[a], .Lfb_end-.Lfb_begin, %[a]\n\t"
                 "global_load_dword %[a], %[a], %[pc]\n.Lfb_nopf:"
                 : [pc] "=&s"(pc), [t] "=&s"(t), [a] "=&v"(pf1), [b] "=&v"(pf2) : [tid] "v"(threadIdx.x), [bx] "s"(blockIdx.x) : "scc", "memory");
  }
  char* const XI = smem + L_XI;
  char* const WI = smem + L_WI;
  char* const KI = smem + L_KI;
  char* const CTL = smem + L_CTL;
  const int tid = threadIdx.x, lane = tid & 63;
  const int w = __builtin_amdgcn_readfirstlane(tid >> 6);
  const int b = blockIdx.x, g8 = b & 7, tb = b & 31, hg = b >> 5;
  const int rr = lane >> 3, c = lane & 7;
  const int h = lane >> 5, l31 = lane & 31;
  const int isV = hg & 1, lr0 = (hg >> 1) * 16;
  const int pid = tb * 8 + hg;
  const int u = (b & 7) * 28 + (b >> 3);
  const rsrc_t rs = __builtin_amdgcn_make_buffer_rsrc(ws, 0, (int)WS_END, 0x00020000);
  STAMP(0, 0);

  unsigned tag;
  asm volatile("s_load_dword %0, %1, 0x0" : "=s"(tag) : "s"(ws) : "memory");
  const bool kvw = (w == (hg & 6));
  const bool ldw = (w == (hg | 1));
  u32x4 xo0, xo1, wr0, wr1, xe, xv[8];
  f32x32 wov;
  unsigned pf0 = 0;
  {
    const u16* p = x + (size_t)(tb * 64 + lr0 + 2 * w) * D_;
    GLD128(xo0, h * 1024 + l31 * 16, p);
    GLD128(xo1, h * 1024 + l31 * 16 + 512, p);
  }
#pragma unroll
  for (int i = 0; i < 2; ++i) glds16((isV ? wv : wk) + (w * 2 + i) * 512, lane * 16, KI + (w * 2 + i) * 1024);
  const unsigned row_off = rr * 1024 + c * 16;
  const unsigned wq_off = rr * 1024 + ((c ^ (((rr >> 1) & 1) << 2)) << 4);
  const u16* xrows = x + (size_t)(tb * 64 + w * 8) * D_;
  if (kvw) {
    __builtin_amdgcn_s_setprio(3);
  {
    unsigned long long ta = (unsigned long long)ws + (lane == 0 ? WS_YP + b * 5376 : (lane == 1 ? WS_K : WS_KVF));
    if (lane == 3) ta = (unsigned long long)(out + (size_t)tb * 64 * D_);
    asm volatile("global_load_dword %0, %1, off" : "=v"(pf0) : "v"(ta) : "memory");
  }
#pragma unroll
    for (int j = 0; j < 8; ++j)
      st128(rs, WS_O + (unsigned)((tb * 2 + (j >> 2)) * 32 + hg * 4 + (j & 3)) * 1024 + lane * 16, u32x4{0xffffffffu, 0xffffffffu, 0xffffffffu, 0xffffffffu});
  } else if (ldw) {
#pragma unroll
    for (int pc = 0; pc < 8; ++pc) glds16(wq + pc * 8 * D_ + hg * 64, wq_off, WI + pc * 1024);
  } else {
#pragma unroll
    for (int i = 0; i < 8; ++i) asm volatile("global_load_dwordx4 %0, %1, %2 offset:%3" : "=v"(xv[i]) : "v"(row_off), "s"(xrows), "i"(i * 128) : "memory");
  }
  VMWAIT(10);
  asm volatile("" : "+v"(xo0), "+v"(xo1), "+v"(pf1), "+v"(pf2));
  STAMP(0, 1);
  {
    float mean, var;
    stats32(xo0, xo1, mean, var);
    asm volatile("" : "+v"(xo0), "+v"(xo1));
    const float mb = rbf(mean), sd = ln_sd(var), inv = __builtin_amdgcn_rcpf(sd);
#pragma unroll
    for (int j = 0; j < 4; ++j) { xo0[j] = ln_pair(xo0[j], mb, inv); xo1[j] = ln_pair(xo1[j], mb, inv); }
    const int lrow = lr0 + 2 * w + h;
    char* xr = XI + lrow * 1024 + ((l31 ^ (lrow & 15)) << 4);
    *(u32x4*)xr = xo0; *(u32x4*)(xr + 512) = xo1;
    if (l31 == 0) *(f32x2*)(CTL + C_STAT + lrow * 8) = f32x2{mb, sd};
    if (tid < 2) *(unsigned*)(CTL + tid * 4) = 0u;
    if (kvw) VMWAIT(9); else VMWAIT(8);
    asm volatile("s_waitcnt lgkmcnt(0)" : "+s"(tag) :: "memory");
    __builtin_amdgcn_s_barrier();
    CBAR();
  }
  tag += 1u;
  STAMP(0, 2);
  if ((w >> 2) == isV) { GLD128(wr0, l31 * 16, wr); GLD128(wr1, l31 * 16 + 512, wr); }
  if (kvw) {
    const int g = lane >> 4, q = (lane & 15) >> 2, p = lane & 3;
    const char* xb = XI + (lr0 + (lane & 15)) * 1024;
    const char* wb = opq(KI + (8 * g + q) * 32 + p * 8);
    f32x4 acc = {}, act = {};
    bf16x8 fx[2][4], fw[2][4];
#define KV_LD(bf, k0) _Pragma("unroll") for (int i = 0; i < 4; ++i) { const int ks = (k0) + i; \
      fx[bf][i] = *(const bf16x8*)(xb + ((((4 * i + g) ^ (lane & 15)) << 4) + (ks >> 2) * 256)); \
      fw[bf][i] = tr_frag(wb + ks * 1024, wb + ks * 1024 + 128); }
    KV_LD(0, 0)
#pragma unroll
    for (int kb = 0; kb < 4; ++kb) {
      if (kb < 3) { KV_LD((kb + 1) & 1, 4 * kb + 4) }
      SB();
#pragma unroll
      for (int i = 0; i < 4; ++i) {
        acc = __builtin_amdgcn_mfma_f32_16x16x32_bf16(fw[kb & 1][i], fx[kb & 1][i], acc, 0, 0, 0);
        act = __builtin_amdgcn_mfma_f32_16x16x32_bf16(fx[kb & 1][i], fw[kb & 1][i], act, 0, 0, 0);
      }
      SB();
    }
    if (isV) acc = act;
    const u32x2 pk = {pack_bf(acc[0], acc[1]), pack_bf(acc[2], acc[3])};
    float n = 0.f;
    if (!isV) {
      st64(rs, WS_K + (unsigned)(tb * 64 + lr0 + (lane & 15)) * 32 + (((g & 1) << 1) | (g >> 1)) * 8, pk);
      n = wave_maxpos(add32(add16(acc[0] * acc[0] + acc[1] * acc[1] + acc[2] * acc[2] + acc[3] * acc[3])));
    } else {
      st64(rs, WS_V + (unsigned)(pid - 1 + (g >> 1)) * VHALF + (lane & 15) * 16 + (g & 1) * 8, pk);
    }
    DRAIN();
    if (lane < 8) st64(rs, WS_KVF + (unsigned)(lane * 256 + pid) * 8, u32x2{__float_as_uint(n), tag});
    __builtin_amdgcn_s_setprio(0);
    STAMP(0, 6);
  } else if (ldw) {
#pragma unroll 4
    for (int pc = 8; pc < 28; ++pc) glds16(wq + pc * 8 * D_ + hg * 64, wq_off, WI + pc * 1024);
  } else {
    const int w6 = w - (w > hg ? 2 : 0);
#pragma unroll
    for (int i = 0; i < 6; ++i) { const int pc = 28 + i * 6 + w6; glds16(wq + pc * 8 * D_ + hg * 64, wq_off, WI + pc * 1024); }
    asm volatile("s_waitcnt vmcnt(6)" : "+v"(xv[0]), "+v"(xv[1]), "+v"(xv[2]), "+v"(xv[3]), "+v"(xv[4]), "+v"(xv[5]), "+v"(xv[6]), "+v"(xv[7]) :: "memory");
    float s = 0.f, q = 0.f;
    unsigned ones = 0x3f803f80u; asm volatile("" : "+v"(ones));
#pragma unroll
    for (int i = 0; i < 8; ++i)
#pragma unroll
      for (int j = 0; j < 4; ++j) { s = dot2(xv[i][j], ones, s); q = dot2(xv[i][j], xv[i][j], q); }
    const float mean = sum8(s) * (1.0f / 512.0f);
    float var = fmaf(-mean, mean, sum8(q) * (1.0f / 512.0f));
    if (__builtin_expect(__any(!(var >= mean * mean * 0.015625f)), 0)) {
      float v = 0.f;
#pragma unroll
      for (int i = 0; i < 8; ++i)
#pragma unroll
        for (int j = 0; j < 4; ++j) { const float a = bf_lo(xv[i][j]) - mean, bb = bf_hi(xv[i][j]) - mean; v = fmaf(a, a, v); v = fmaf(bb, bb, v); }
      var = sum8(v) * (1.0f / 512.0f);
    }
    asm volatile("" : "+v"(xv[0]), "+v"(xv[1]), "+v"(xv[2]), "+v"(xv[3]), "+v"(xv[4]), "+v"(xv[5]), "+v"(xv[6]), "+v"(xv[7]));
    const float st_m = rbf(mean), st_s = ln_sd(var);
    const float inv = __builtin_amdgcn_rcpf(st_s), nb = -st_m * inv;
    const int lrow = w * 8 + rr;
#pragma unroll
    for (int i = 0; i < 8; ++i) {
      u32x4 o;
#pragma unroll
      for (int j = 0; j < 4; ++j) o[j] = pack_bf(fmaf(bf_lo(xv[i][j]), inv, nb), fmaf(bf_hi(xv[i][j]), inv, nb));
      *(u32x4*)(XI + lrow * 1024 + (((i * 8 + c) ^ (lrow & 15)) << 4)) = o;
    }
    if (c == 0) *(f32x2*)(CTL + C_STAT + lrow * 8) = f32x2{st_m, st_s};
    STAMP(0, 3);
  }
  VMWAIT(20);
  asm volatile("" : "+v"(pf0), "+v"(wr0), "+v"(wr1));
  if ((w >> 2) == isV) {
    float r = 0.f;
#pragma unroll
    for (int j = 0; j < 4; ++j) { r = dot2(xo0[j], wr0[j], r); r = dot2(xo1[j], wr1[j], r); }
    const unsigned rkey = make_key(rbf(sum32(r)), pid * 8 + 2 * (w & 3) + h);
    if (l31 == 0) *(unsigned*)(CTL + C_R + (2 * (w & 3) + h) * 4) = rkey;
  }
  if (!kvw) DRAIN();
  RAW_BARRIER();
  STAMP(0, 4);
  if (w < 4) __builtin_amdgcn_s_setprio(2); else __builtin_amdgcn_s_setprio(1);
  if (w == 4) {
    const unsigned key = lane < 8 ? *(const unsigned*)(CTL + C_R + lane * 4) : 0u;
    const unsigned k1 = wave_maxu(key), k2 = wave_maxu(key == k1 ? 0u : key);
    if (lane < 8) st128(rs, WS_CAND + (unsigned)(lane * 256 + pid) * 16, u32x4{k1, tag, k2, tag});
  }
  const int blk = (lane >> 4) & 1, q4 = (lane & 15) >> 2, p4 = lane & 3;
  if (w < 4) {
    const int nt = w & 1, tt = w >> 1;
    const int tok = tt * 32 + l31;
    const char* xb = XI + tok * 1024;
    const char* wb = WI + (8 * h + q4) * 128 + (((4 * nt + 2 * blk + (p4 >> 1)) ^ (((q4 >> 1) & 1) << 2)) << 4) + 8 * (p4 & 1);
    f32x16 acc = {};
    bf16x8 fa[2][4], fb[2][4];
#define Q_LD(bf, k0) _Pragma("unroll") for (int i = 0; i < 4; ++i) { const int ks = (k0) + i; \
      fa[bf][i] = tr_frag(wb + ks * 2048, wb + ks * 2048 + 512); \
      fb[bf][i] = *(const bf16x8*)(xb + ((((2 * (ks & 7) + h) ^ (tok & 15)) << 4) + (ks >> 3) * 256)); }
    Q_LD(0, 0)
    SB();
#pragma unroll
    for (int kb = 0; kb < 8; ++kb) {
      if (kb < 7) { Q_LD((kb + 1) & 1, 4 * kb + 4) }
#pragma unroll
      for (int i = 0; i < 4; ++i) acc = __builtin_amdgcn_mfma_f32_32x32x16_bf16(fa[kb & 1][i], fb[kb & 1][i], acc, 0, 0, 0);
#pragma unroll
      for (int i = 0; i < 4; ++i) { SGB(0x8, 1); SGB(0x100, 3); }
      SB();
    }
    const float cs = 0.25f * 1.4426950408889634f;
    float n0 = 0.f, n1 = 0.f;
#pragma unroll
    for (int r = 0; r < 8; ++r) { acc[r] *= cs; acc[r + 8] *= cs; n0 = fmaf(acc[r], acc[r], n0); n1 = fmaf(acc[r + 8], acc[r + 8], n1); }
    char* qi = smem + L_QI + (tt * 4 + nt * 2) * 1024 + lane * 16;
    *(u32x4*)qi = u32x4{pack_bf(acc[0], acc[1]), pack_bf(acc[2], acc[3]), pack_bf(acc[4], acc[5]), pack_bf(acc[6], acc[7])};
    *(u32x4*)(qi + 1024) = u32x4{pack_bf(acc[8], acc[9]), pack_bf(acc[10], acc[11]), pack_bf(acc[12], acc[13]), pack_bf(acc[14], acc[15])};
    const float nm = wave_maxpos(fmaxf(add32(n0), add32(n1)));
    if (lane == 0) {
      *(float*)(CTL + C_QMAX + w * 4) = nm;
      asm volatile("s_waitcnt lgkmcnt(0)\n\tds_add_u32 %0, %1" :: "v"((unsigned)(uintptr_t)(LAS char*)(CTL + C_CNT)), "v"(1u) : "memory");
    }
  }
  STAMP(0, 5);

  xe = *(const u32x4*)((const char*)(xrows + hg * 64) + row_off);
  f32x32 kv0, kv1, kv2;
  asm volatile(VCONST_ASM : KV_OUT : [cst] "v"((lane & 15) ? 0u : 0x3f803f80u));
  float kmax;
  {
    u32x2 f;
    SPIN {
      f = ld64(rs, WS_KVF + (unsigned)(g8 * 256 + w * 32 + l31) * 8);
      if (__all(f[1] == tag)) break;
      __builtin_amdgcn_s_sleep(1);
    }
    CBAR();
    kmax = __uint_as_float(f[0]);
  }
  STAMP(1, 0);
  {
    const unsigned kb = WS_K + (unsigned)(w * 256 + l31) * 32 + h * 16;
    const unsigned vp = WS_V + (unsigned)(w * 32 + 2 * blk + h) * VHALF + (lane & 15) * 16;
    asm volatile(LOADS_ASM : KV_IO : KV_ADDR : "memory");
  }
  kmax = wave_maxpos(kmax);
  u32x4 gatep;
  {
    const f32x2 st = *(const f32x2*)(CTL + C_STAT + (w * 8 + rr) * 8);
    const float inv = __builtin_amdgcn_rcpf(st[1]);
#pragma unroll
    for (int j = 0; j < 4; ++j) gatep[j] = silu_pair(ln_pair(xe[j], st[0], inv));
    asm volatile("" : "+v"(gatep));
  }
  lds_wait_ge(CTL + C_CNT, 4u);
  STAMP(1, 1);
  const f32x4 qn = *(const f32x4*)(CTL + C_QMAX);
  const float qmax = fmaxf(fmaxf(qn[0], qn[1]), fmaxf(qn[2], qn[3]));
  const float bound = __builtin_amdgcn_sqrtf(qmax * kmax) * 1.02f + 0.01f;
  int fasti = __builtin_amdgcn_readfirstlane(bound <= 48.0f ? 1 : 0);
  asm volatile("" : "+s"(fasti));
  const bool fast = fasti != 0;
  const int idx = (int)0xDDDDDDDDu;
  bf16x8 zf = {};
  asm volatile("" : "+v"(zf));
  const f32x16 z16 = {};
  const char* qbase = smem + L_QI + lane * 16;
  f32x16 o;
  float shift = bound;
  bf16x8 qf = *(const bf16x8*)(qbase + ((w + 1) & 7) * 1024);
  const unsigned lds0 = (unsigned)(uintptr_t)(LAS char*)smem;
  unsigned qa = lds0 + L_QI + lane * 16 + ((w + 2) & 7) * 1024;
  f32x16 cc = splat16(-bound);
  f32x32 sab;
  int i = 0;
  unsigned so = ((w + 1) & 7) * PREG + 6 * PSLOT, qo = ((w + 2) & 7) * 1024; float t1_; unsigned t2_;
#define RUN_OUT "=&{v[240:255]}"(o), "+{v[192:207]}"(cc), [q] "+v"(qf), [qa] "+v"(qa), [i] "+s"(i), [so] "+s"(so), [qo] "+s"(qo), [t1] "=&v"(t1_), [t2] "=&v"(t2_)
#define RUN_IN [z] "v"(zf), [idx] "v"(idx), [sh] "v"(shift), [lo] "s"(0xffffffffull), [l16] "v"(lds0 + lane * 16), [l4] "v"(lds0 + lane * 4), \
               [qb] "v"(lds0 + L_QI + lane * 16)
  STAMP(3, 0);
  asm volatile("s_cmp_eq_u32 %[fast], 0\n\ts_cbranch_scc1 .Lfb_slow0\n\t" PROLOG0_ASM PASS0_ASM POST_ASM
               "s_branch .Lfb_out%=\n.Lfb_slow0:\n\ts_waitcnt vmcnt(0)\n\t" SWAPS_ASM "\n.Lfb_out%=:"
               : "=&{v[208:239]}"(sab), RUN_OUT, KV_IO : RUN_IN, [fast] "s"(fasti) : "scc", "memory");

  if (w == 2) {
        int lane = tid & 63; asm volatile("" : "+v"(lane));
        unsigned m1 = 0, m2 = 0;
        SPIN {
          bool ok = true; m1 = 0; m2 = 0;
#pragma unroll
          for (int j = 0; j < 4; ++j) {
            const u32x4 cd = ld128(rs, WS_CAND + (unsigned)(g8 * 256 + j * 64 + lane) * 16);
            ok = ok && cd[1] == tag && cd[3] == tag;
            m2 = max(max(m2, cd[2]), min(m1, cd[0])); m1 = max(m1, cd[0]);
          }
          if (__all(ok)) break;
          __builtin_amdgcn_s_sleep(1);
        }
        CBAR();
        const unsigned k1 = __builtin_amdgcn_readfirstlane(wave_maxu(m1));
        const unsigned k2 = __builtin_amdgcn_readfirstlane(wave_maxu(m1 == k1 ? m2 : m1));
        if (lane == 0) *(u32x2*)(CTL + C_K1) = u32x2{k1, k2};
        if (b < NU) {
          const int h = lane >> 5, l31 = lane & 31;
          const u16* xr = x + key_tok(h ? k2 : k1) * D_ + l31 * 8;
        {
          const u32x4 xs0 = *(const u32x4*)xr, xs1 = *(const u32x4*)(xr + 256);
          float mean, var;
          stats32(xs0, xs1, mean, var);
          const float mb = rbf(mean), inv = __builtin_amdgcn_rcpf(ln_sd(var));
          u32x4 o0;
#pragma unroll
          for (int j = 0; j < 4; ++j) o0[j] = ln_pair((u & 4) ? xs1[j] : xs0[j], mb, inv);
          *(u32x4*)(smem + L_XS + h * 1024 + l31 * 16) = o0;
          asm volatile("s_waitcnt lgkmcnt(0)" ::: "memory");
          if (lane == 0) *(volatile LAS unsigned*)(CTL + C_SEL) = 1u;
        }
        }
  }
  if (w == 0 && b < NU) {
    int ln = tid; asm volatile("" : "+v"(ln));
    const unsigned go = (ln >> 3) * (F_ * 2) + (((ln & 7) ^ (((ln >> 4) & 1) << 2)) << 4);
#pragma unroll
    for (int j = 0; j < 16; ++j) glds16((j < 8 ? wg : wu) + ((u & 7) * 64 + (j & 7) * 8) * F_ + (u >> 3) * 64, go, smem + L_GU + j * 1024);
  }
  if (w == 1 && b < NU) {
#pragma unroll
    for (int f = 0; f < 8; ++f) glds16(wd + (u * 8 + f) * D_, lane * 16, smem + (f < 6 ? L_DN + f * 1088 : L_DN2 + (f - 6) * 1088));
  }
  asm volatile("" : "=&{v[64:95]}"(wov));
  const int iw = (0x33222453 >> (4 * w)) & 15;
  unsigned ro = row_off;
  unsigned long long wb2_;

#pragma nounroll
  while (i < 8) {
    if (b < NU && ((w == 0 && i == 2) || (w == 1 && i == 4))) {
      int lane = tid & 63, u = (b & 7) * 28 + (b >> 3);
      asm volatile("" : "+v"(lane), "+s"(u));
      const int h = lane >> 5, l31 = lane & 31, blk = (lane >> 4) & 1, q4 = (lane & 15) >> 2, p4 = lane & 3;
      if (i == 2) {
        DRAIN();
        lds_wait_ge(CTL + C_SEL, 1u);
        const char* xb = opq(smem + L_XS + (lane & 1) * 1024 + (u & 3) * 128 + h * 16);
        bf16x8 fx[4];
#pragma unroll
        for (int ks = 0; ks < 4; ++ks) fx[ks] = *(const bf16x8*)(xb + ks * 32);
#pragma unroll
        for (int nt = 0; nt < 2; ++nt) {
          const char* wb = opq(smem + L_GU + (8 * h + q4) * 128 + (((4 * nt + 2 * blk + (p4 >> 1)) ^ (((q4 >> 1) & 1) << 2)) << 4) + 8 * (p4 & 1));
#pragma unroll
          for (int mat = 0; mat < 2; ++mat) {
            f32x16 z = z16;
#pragma unroll
            for (int ks = 0; ks < 4; ++ks) z = QK(fx[ks], tr_frag(wb + mat * 8192 + ks * 2048, wb + mat * 8192 + ks * 2048 + 512), z);
            if (lane < 32) st128(rs, WS_P1 + (unsigned)u * 2048 + (mat * 64 + nt * 32 + lane) * 16, u32x4{__float_as_uint(z[0]), tag, __float_as_uint(z[1]), tag});
            SB();
          }
        }
      } else {
        const unsigned po = WS_P1 + (unsigned)((u & ~7) + (lane >> 3)) * 2048 + ((u & 7) * 8 + (lane & 7)) * 16;
        u32x4 pg, pu;
        SPIN {
          pg = ld128(rs, po); pu = ld128(rs, po + 1024);
          if (__all(pg[1] == tag && pg[3] == tag && pu[1] == tag && pu[3] == tag)) break;
          __builtin_amdgcn_s_sleep(2);
        }
        DRAIN();
        float t[4] = {__uint_as_float(pg[0]), __uint_as_float(pg[2]), __uint_as_float(pu[0]), __uint_as_float(pu[2])};
#pragma unroll
        for (int j = 0; j < 4; ++j) { t[j] += dpp_f<0x128>(t[j]); t[j] = add32(add16(t[j])); }
        const unsigned hp = pack_bf(rbf(silu_f(rbf(t[0]))) * rbf(t[2]), rbf(silu_f(rbf(t[1]))) * rbf(t[3]));
        if (lane < 8) { *(u16*)(smem + L_HS + lane * 2) = (u16)hp; *(u16*)(smem + L_HS + 16 + lane * 2) = (u16)(hp >> 16); }
        u32x4 ha = *(const u32x4*)(smem + L_HS + (lane & 1) * 16);
        if (lane >= 2) ha = u32x4{0u, 0u, 0u, 0u};
        const bf16x8 A = __builtin_bit_cast(bf16x8, ha);
        const int co = (2 * blk + (p4 >> 1)) * 16 + 8 * (p4 & 1);
        const char* db0 = opq(smem + L_DN + q4 * 1088 + co);
        const char* db1 = opq(smem + (q4 < 2 ? L_DN + (4 + q4) * 1088 : L_DN2 + (q4 - 2) * 1088) + co);
#pragma unroll 2
        for (int ct = 0; ct < 16; ++ct) {
          const bf16x8 bw = tr_frag(db0 + ct * 64, db1 + ct * 64);
          const f32x16 z = QK(A, bw, z16);
          auto sw = __builtin_amdgcn_permlane32_swap(__float_as_uint(z[0]), __float_as_uint(z[1]), false, false);
          st32(rs, WS_YP + (unsigned)(h * 64 + ct * 4 + (l31 >> 3)) * (NU * 32) + u * 32 + (l31 & 7) * 4, sw[0]);
        }
        DRAIN();
        if (lane < 8) st32(rs, WS_YF + lane * 1024 + u * 4, tag);
      }
    }
  if (w < 4 && i == 7 && b < NU) __builtin_amdgcn_s_setprio(0);
    STAMP(3, i);
    int stop = 8;
    if (b < NU) {
      if (w < 4 && i < 7) stop = 7;
      if (w == 0 && i < 2) stop = 2;
      if (w == 1 && i < 4) stop = 4;
    }
    if (!fast) {
      stop = i + 1;
      float m = -3.0e38f;
      asm volatile(ROWMAX_ASM : [m] "+v"(m), "=&{v[208:239]}"(sab) : [q] "v"(qf), KV_IN);
      shift = max32(m);
      cc = splat16(-shift);
      asm volatile(PROLOG_ASM : "=&{v[208:239]}"(sab), "+{v[192:207]}"(cc) : [q] "v"(qf), KV_IN);
    }
    asm volatile(".Lfb_loop%=:\n\t" PASSN_ASM POST_ASM WOLOAD_ASM "\n.Lfb_skip%=:\n\ts_cmp_lt_u32 %[i], %[stop]\n\ts_cbranch_scc1 .Lfb_loop%=\n.Lfb_out%=:"
                 : "+{v[208:239]}"(sab), RUN_OUT, "+{v[64:95]}"(wov), [ro] "+v"(ro), [wb2] "=&s"(wb2_)
                 : RUN_IN, [stop] "s"(stop), [iw] "s"(iw), [wb] "s"(wo + (size_t)(w * 64) * D_ + hg * 64), KV_IN : "scc", "memory");
  }
  {
  int tid = threadIdx.x; asm volatile("" : "+v"(tid));
  const int lane = tid & 63, rr = lane >> 3, c = lane & 7, h = lane >> 5, l31 = lane & 31, blk = (lane >> 4) & 1, q4 = (lane & 15) >> 2, p4 = lane & 3;
  if (w < 4 && b >= NU) {
    SPIN {
      const u32x4 f = ld128(rs, WS_YF + g8 * 1024 + min(lane, 55) * 16);
      if (__all(f[0] == tag && f[1] == tag && f[2] == tag && f[3] == tag)) break;
      __builtin_amdgcn_s_sleep(8);
    }
    CBAR();
    const unsigned rw = (unsigned)((b - NU) * 4 + w);
    const unsigned yo = WS_YP + rw * (NU * 32) + lane * 16;
    f32x4 y = {};
#pragma unroll
    for (int j = 0; j < 7; ++j) y += __builtin_bit_cast(f32x4, ld128(rs, yo + j * 1024));
#pragma unroll
    for (int j = 0; j < 4; ++j) { float t = y[j]; t += dpp_f<0x4E>(t); t += dpp_f<0x124>(t); t += dpp_f<0x128>(t); y[j] = add32(add16(t)); }
    if (lane < 2) {
      st128(rs, WS_YG + rw * 64 + lane * 32, u32x4{__float_as_uint(y[0]), tag, __float_as_uint(y[1]), tag});
      st128(rs, WS_YG + rw * 64 + lane * 32 + 16, u32x4{__float_as_uint(y[2]), tag, __float_as_uint(y[3]), tag});
    }
  }
  __builtin_amdgcn_s_setprio(0);
  DRAIN();
  const u32x4 xe = *(const u32x4*)(x + (size_t)(tb * 64 + (tid >> 3)) * D_ + hg * 64 + c * 8);
  STAMP(1, 2);
  RAW_BARRIER();
  STAMP(1, 3);
  const unsigned fr = (unsigned)((tb * 2 + (w >> 2)) * 32 + hg * 4 + (w & 3));
  {
    float a[8], pl[7], pm[7];
    auto sw = __builtin_amdgcn_permlane32_swap(__float_as_uint(o[8]), __float_as_uint(o[8]), false, false);
    float l = __uint_as_float(sw[0]);
    float mx = shift;
#pragma unroll
    for (int sidx = 0; sidx < 7; ++sidx) {
      const char* sl = smem + w * PREG + sidx * PSLOT + 2048 + l31 * 4;
      pl[sidx] = *(const float*)sl; pm[sidx] = *(const float*)(sl + 128);
      mx = fmaxf(mx, pm[sidx]);
    }
    const float f0 = __builtin_amdgcn_exp2f(shift - mx);
    l *= f0;
#pragma unroll
    for (int r = 0; r < 8; ++r) a[r] = o[r] * f0;
#pragma unroll
    for (int sidx = 0; sidx < 7; ++sidx) {
      const char* sl = smem + w * PREG + sidx * PSLOT;
      const f32x4 p0 = *(const f32x4*)(sl + lane * 16), p1 = *(const f32x4*)(sl + 1024 + lane * 16);
      const float f = __builtin_amdgcn_exp2f(pm[sidx] - mx);
#pragma unroll
      for (int r = 0; r < 4; ++r) { a[r] = fmaf(p0[r], f, a[r]); a[r + 4] = fmaf(p1[r], f, a[r + 4]); }
      l = fmaf(pl[sidx], f, l);
    }
    const float inv = __builtin_amdgcn_rcpf(l);
    const u32x4 of = {min(pack_bf(a[0] * inv, a[1] * inv), 0xfffffffeu), min(pack_bf(a[2] * inv, a[3] * inv), 0xfffffffeu),
                      min(pack_bf(a[4] * inv, a[5] * inv), 0xfffffffeu), min(pack_bf(a[6] * inv, a[7] * inv), 0xfffffffeu)};
    st128(rs, WS_O + fr * 1024 + lane * 16, of);
    *(u32x4*)(smem + L_QI + w * 1024 + lane * 16) = of;
  }
  asm volatile("s_waitcnt lgkmcnt(0)\n\tds_write_b128 %0, v[64:67]\n\tds_write_b128 %0, v[68:71] offset:1024\n\tds_write_b128 %0, v[72:75] offset:2048\n\t"
               "ds_write_b128 %0, v[76:79] offset:3072\n\tds_write_b128 %0, v[80:83] offset:4096\n\tds_write_b128 %0, v[84:87] offset:5120\n\t"
               "ds_write_b128 %0, v[88:91] offset:6144\n\tds_write_b128 %0, v[92:95] offset:7168"
               :: "v"((unsigned)(uintptr_t)(LAS char*)smem + w * PREG + rr * 128 + ((c ^ (((rr >> 1) & 1) << 2)) << 4)), "{v[64:95]}"(wov) : "memory");
  if (b == 0 && tid == 0) st32(rs, WS_EPOCH, tag);
  STAMP(1, 4);
  RAW_BARRIER();
  const int etok = tid >> 3, gtok = tb * 64 + etok;
  {
    const int tt = w >> 2, kq = w & 3;
    bf16x8 fa[2][8];
#pragma unroll
    for (int nt = 0; nt < 2; ++nt) {
      const char* wb = opq(smem + 2 * kq * PREG + (4 * h + q4) * 128 + (((4 * nt + 2 * blk + (p4 >> 1)) ^ (((q4 >> 1) & 1) << 2)) << 4) + 8 * (p4 & 1));
#pragma unroll
      for (int i = 0; i < 8; ++i) fa[nt][i] = tr_frag(wb + (i >> 2) * PREG + (i & 3) * 2048, wb + (i >> 2) * PREG + (i & 3) * 2048 + 1024);
    }
    SB();
    const unsigned fo = WS_O + (unsigned)((tb * 2 + tt) * 32 + kq * 8) * 1024 + lane * 16;
    u32x4 fb[8];
    if (hg != 2 * kq) {
#pragma unroll
      for (int i = 0; i < 4; ++i) fb[i] = ld128(rs, fo + i * 1024);
    } else {
#pragma unroll
      for (int i = 0; i < 4; ++i) fb[i] = *(const u32x4*)(smem + L_QI + (tt * 4 + i) * 1024 + lane * 16);
    }
    if (hg != 2 * kq + 1) {
#pragma unroll
      for (int i = 4; i < 8; ++i) fb[i] = ld128(rs, fo + i * 1024);
    } else {
#pragma unroll
      for (int i = 0; i < 4; ++i) fb[4 + i] = *(const u32x4*)(smem + L_QI + (tt * 4 + i) * 1024 + lane * 16);
    }
    SB();
    STAMP(2, 0);
    {
      const unsigned k1 = *(const unsigned*)(CTL + C_K1), k2 = *(const unsigned*)(CTL + C_K2);
      const int sel = (gtok == key_tok(k1)) ? 0 : ((gtok == key_tok(k2)) ? 1 : -1);
      if (__builtin_expect(__any(sel >= 0), 0)) {
        const f32x2 st = *(const f32x2*)(CTL + C_STAT + etok * 8);
        const float inv = __builtin_amdgcn_rcpf(st[1]);
        const float rsg = rbf(sigm_f(key_val(sel > 0 ? k2 : k1)));
        const unsigned go = WS_YG + (unsigned)((sel > 0 ? 512 : 0) + hg * 64 + c * 8) * 8;
        u32x4 gq[4];
        SPIN {
          bool ok = true;
#pragma unroll
          for (int j = 0; j < 4; ++j) { gq[j] = ld128(rs, go + j * 16); ok = ok && gq[j][1] == tag && gq[j][3] == tag; }
          if (__all(ok)) break;
          __builtin_amdgcn_s_sleep(1);
        }
#pragma unroll
        for (int j = 0; j < 4; ++j) {
          const unsigned xn = ln_pair(xe[j], st[0], inv);
          const float u0 = rbf(rbf(__uint_as_float(gq[j][0])) * rsg), u1 = rbf(rbf(__uint_as_float(gq[j][2])) * rsg);
          if (sel >= 0) gatep[j] = silu_pair(pack_bf(bf_lo(xn) + u0, bf_hi(xn) + u1));
        }
      }
    }
    SB();
    STAMP(2, 1);
    f32x16 acc0 = {}, acc1 = {};
#define FB_MAX(i) max(max(fb[i][0], fb[i][1]), max(fb[i][2], fb[i][3]))
#pragma unroll
    for (int hf = 0; hf < 2; ++hf) {
      if (__builtin_expect(__any(max(max(FB_MAX(4 * hf), FB_MAX(4 * hf + 1)), max(FB_MAX(4 * hf + 2), FB_MAX(4 * hf + 3))) == 0xffffffffu), 0)) {
        SPIN {
          unsigned bad = 0;
#pragma unroll
          for (int i = 4 * hf; i < 8; ++i) bad |= __any(FB_MAX(i) == 0xffffffffu) ? 1u << i : 0u;
          const unsigned need = bad & (15u << (4 * hf));
          if (!need) break;
          SPIN {
            const unsigned pv = ld32(rs, fo - lane * 16 + (lane & 7) * 1024 + 1020);
            if (!__any(pv == 0xffffffffu && ((need >> (lane & 7)) & 1u))) break;
            __builtin_amdgcn_s_sleep(1);
          }
#pragma unroll
          for (int i = 4 * hf; i < 8; ++i) if ((bad >> i) & 1u) fb[i] = ld128(rs, fo + i * 1024);
        }
      }
#pragma unroll
      for (int i = 4 * hf; i < 4 * hf + 4; ++i) {
        acc0 = QK(fa[0][i], __builtin_bit_cast(bf16x8, fb[i]), acc0);
        acc1 = QK(fa[1][i], __builtin_bit_cast(bf16x8, fb[i]), acc1);
      }
      SB();
    }
    STAMP(2, 2);
    char* pr = smem + w * PREG + 8192 + l31 * 256;
#pragma unroll
    for (int g = 0; g < 4; ++g) {
      *(f32x4*)(pr + (((2 * g + h) ^ (l31 & 7)) << 4)) = f32x4{acc0[4 * g], acc0[4 * g + 1], acc0[4 * g + 2], acc0[4 * g + 3]};
      *(f32x4*)(pr + 128 + (((2 * g + h) ^ (l31 & 7)) << 4)) = f32x4{acc1[4 * g], acc1[4 * g + 1], acc1[4 * g + 2], acc1[4 * g + 3]};
    }
  }
  RAW_BARRIER();
  {
    f32x4 s0v = {}, s1v = {};
#pragma unroll
    for (int kq = 0; kq < 4; ++kq) {
      const char* pr = smem + ((w >> 2) * 4 + kq) * PREG + 8192 + (etok & 31) * 256 + (c >> 2) * 128;
      s0v += *(const f32x4*)(pr + ((((2 * c) & 7) ^ rr) << 4)); s1v += *(const f32x4*)(pr + ((((2 * c + 1) & 7) ^ rr) << 4));
    }
    u32x4 ov;
#pragma unroll
    for (int j = 0; j < 4; ++j) {
      const unsigned sv = j < 2 ? pack_bf(s0v[2 * j], s0v[2 * j + 1]) : pack_bf(s1v[2 * j - 4], s1v[2 * j - 3]);
      const unsigned pr2 = pack_bf(bf_lo(sv) * bf_lo(gatep[j]), bf_hi(sv) * bf_hi(gatep[j]));
      ov[j] = pack_bf(bf_lo(xe[j]) + bf_lo(pr2), bf_hi(xe[j]) + bf_hi(pr2));
    }
    *(u32x4*)(out + (size_t)gtok * D_ + hg * 64 + c * 8) = ov;
  }
  }
  asm volatile(".Lfb_end:");
  STAMP(2, 3);
}

extern "C" void kernel_launch(void* const* d_in, const int* in_sizes, int n_in, void* d_out, int out_size, void* d_ws, size_t ws_size,
                              hipStream_t stream) {
  fused_block<<<256, 512, 0, stream>>>((const u16*)d_in[0], (const u16*)d_in[1], (const u16*)d_in[2], (const u16*)d_in[3], (const u16*)d_in[4],
                                       (const u16*)d_in[6], (const u16*)d_in[7], (const u16*)d_in[8], (const u16*)d_in[9], (char*)d_ws, (u16*)d_out);
}
```

```cpp
#include <hip/hip_runtime.h>
#include <hip/hip_bf16.h>
#include <stdint.h>

typedef __attribute__((ext_vector_type(2))) __bf16 bf16x2;
typedef __attribute__((ext_vector_type(8))) __bf16 bf16x8;
typedef __attribute__((ext_vector_type(4))) short s16x4;
typedef __attribute__((ext_vector_type(8))) short s16x8;
typedef __attribute__((ext_vector_type(2))) float f32x2;
typedef __attribute__((ext_vector_type(4))) float f32x4;
typedef __attribute__((ext_vector_type(16))) float f32x16;
typedef __attribute__((ext_vector_type(32))) float f32x32;
typedef __attribute__((ext_vector_type(2))) unsigned u32x2;
typedef __attribute__((ext_vector_type(4))) unsigned u32x4;
typedef unsigned short u16;
typedef __amdgpu_buffer_rsrc_t rsrc_t;
#define LAS __attribute__((address_space(3)))
#define DI __device__ __forceinline__

constexpr int S_ = 2048, D_ = 512, F_ = 1792;
constexpr int NU = 224;
constexpr int VHALF = 256;
constexpr int SC1 = 16;
constexpr unsigned SPIN_MAX = 1u << 18;

constexpr unsigned WS_EPOCH = 0;
constexpr unsigned WS_KVF = 1024;
constexpr unsigned WS_CAND = WS_KVF + 8 * 256 * 8;
constexpr unsigned WS_YF = WS_CAND + 8 * 512 * 8;
constexpr unsigned WS_YG = WS_YF + 8 * 1024;
constexpr unsigned WS_K = WS_YG + 1024 * 8;
constexpr unsigned WS_V = WS_K + S_ * 32;
constexpr unsigned WS_YP = WS_V + 256 * VHALF;
constexpr unsigned WS_P1 = WS_YP + NU * 2 * D_ * 4;
constexpr unsigned WS_O = WS_P1 + NU * 2048;
constexpr unsigned WS_END = WS_O + S_ * D_ * 2;

constexpr int L_WI = 0;
constexpr int L_XI = 65536;
constexpr int L_KI = 131072;
constexpr int PSLOT = 2304;
constexpr int PREG = 16384;
constexpr int L_XS = 7 * PREG;
constexpr int L_GU = 131072;
constexpr int L_QI = 147456;
constexpr int L_CTL = L_QI + 8192;
constexpr int L_DN = L_CTL + 1024;
constexpr int L_DN2 = 7 * PREG + PSLOT + 128;
constexpr int L_HS = 0;
constexpr int L_TOTAL = L_DN + 6 * 1088;
constexpr int C_CNT = 0;
constexpr int C_SEL = 4;
constexpr int C_K1 = 8, C_K2 = 12;
constexpr int C_QMAX = 16;
constexpr int C_R = 32;
constexpr int C_STAT = 64;

#define STAMP(kid, i)

DI float bf_lo(unsigned u) { return __uint_as_float(u << 16); }
DI float bf_hi(unsigned u) { return __uint_as_float(u & 0xffff0000u); }
DI unsigned pack_bf(float a, float b) { f32x2 f = {a, b}; return __builtin_bit_cast(unsigned, __builtin_convertvector(f, bf16x2)); }
DI float rbf(float a) { return (float)(__bf16)a; }
DI float dot2(unsigned a, unsigned b, float c) { return __builtin_amdgcn_fdot2_f32_bf16(__builtin_bit_cast(bf16x2, a), __builtin_bit_cast(bf16x2, b), c, false); }
DI void glds16(const void* sbase, unsigned voff, void* lds_wave_base) {
  unsigned keep; const unsigned dst = (unsigned)(uintptr_t)(LAS char*)lds_wave_base;
  asm volatile("s_mov_b32 %0, m0\n\ts_mov_b32 m0, %2\n\ts_nop 0\n\tglobal_load_lds_dwordx4 %1, %3\n\ts_mov_b32 m0, %0"
               : "=&s"(keep) : "v"(voff), "s"(dst), "s"(sbase) : "memory");
}
#define GLD128(dst, voff, sbase) asm volatile("global_load_dwordx4 %0, %1, %2" : "=v"(dst) : "v"(voff), "s"(sbase) : "memory")
DI const char* opq(const char* p) { unsigned a = (unsigned)(uintptr_t)(LAS char*)p; asm volatile("" : "+v"(a)); return (const char*)(LAS char*)(uintptr_t)a; }
DI s16x4 tr_read(const char* p) { return __builtin_amdgcn_ds_read_tr16_b64_v4i16((LAS s16x4*)p); }
DI bf16x8 tr_frag(const char* p0, const char* p1) {
  s16x4 a = tr_read(p0), b = tr_read(p1);
  s16x8 v = __builtin_shufflevector(a, b, 0, 1, 2, 3, 4, 5, 6, 7);
  return __builtin_bit_cast(bf16x8, v);
}
template <int CTRL> DI float dpp_f(float v) { return __int_as_float(__builtin_amdgcn_update_dpp(0, __float_as_int(v), CTRL, 0xf, 0xf, true)); }
template <int CTRL> DI unsigned dpp_u(unsigned v) { return (unsigned)__builtin_amdgcn_update_dpp(0, (int)v, CTRL, 0xf, 0xf, true); }
DI float sum8(float v) {
  v += dpp_f<0xB1>(v); v += dpp_f<0x4E>(v); v += dpp_f<0x141>(v);
  return v;
}
DI float add16(float v) { auto r = __builtin_amdgcn_permlane16_swap(__float_as_uint(v), __float_as_uint(v), false, false); return __uint_as_float(r[0]) + __uint_as_float(r[1]); }
DI float add32(float v) { auto r = __builtin_amdgcn_permlane32_swap(__float_as_uint(v), __float_as_uint(v), false, false); return __uint_as_float(r[0]) + __uint_as_float(r[1]); }
DI float max16(float v) { auto r = __builtin_amdgcn_permlane16_swap(__float_as_uint(v), __float_as_uint(v), false, false); return fmaxf(__uint_as_float(r[0]), __uint_as_float(r[1])); }
DI float max32(float v) { auto r = __builtin_amdgcn_permlane32_swap(__float_as_uint(v), __float_as_uint(v), false, false); return fmaxf(__uint_as_float(r[0]), __uint_as_float(r[1])); }
DI float sum32(float v) { v = sum8(v); v += dpp_f<0x140>(v); return add16(v); }
DI float wave_sum(float v) { return add32(sum32(v)); }
DI float wave_maxf(float v) {
  v = fmaxf(v, dpp_f<0xB1>(v)); v = fmaxf(v, dpp_f<0x4E>(v)); v = fmaxf(v, dpp_f<0x141>(v)); v = fmaxf(v, dpp_f<0x140>(v));
  return max32(max16(v));
}
DI unsigned wave_maxu(unsigned v) {
  v = max(v, dpp_u<0xB1>(v)); v = max(v, dpp_u<0x4E>(v)); v = max(v, dpp_u<0x141>(v)); v = max(v, dpp_u<0x140>(v));
  auto r = __builtin_amdgcn_permlane16_swap(v, v, false, false); v = max(r[0], r[1]);
  auto q = __builtin_amdgcn_permlane32_swap(v, v, false, false); return max(q[0], q[1]);
}
DI float wave_maxpos(float v) { return __uint_as_float(wave_maxu(__float_as_uint(v))); }
DI float silu_f(float v) { return v * __builtin_amdgcn_rcpf(1.0f + __builtin_amdgcn_exp2f(-1.4426950408889634f * v)); }
DI float sigm_f(float v) { return __builtin_amdgcn_rcpf(1.0f + __builtin_amdgcn_exp2f(-1.4426950408889634f * v)); }
DI float ln_sd(float var) { return rbf(__builtin_amdgcn_sqrtf(rbf(rbf(var) + 1e-5f))); }
DI unsigned ln_pair(unsigned xu, float mb, float inv) {
  const unsigned d = pack_bf(bf_lo(xu) - mb, bf_hi(xu) - mb);
  return pack_bf(bf_lo(d) * inv, bf_hi(d) * inv);
}
DI unsigned silu_pair(unsigned v) { return pack_bf(silu_f(bf_lo(v)), silu_f(bf_hi(v))); }

DI void stats32(const u32x4& a, const u32x4& b2, float& mean, float& var) {
  float s = 0.f, q = 0.f;
#pragma unroll
  for (int j = 0; j < 4; ++j) { s = dot2(a[j], 0x3f803f80u, s); s = dot2(b2[j], 0x3f803f80u, s); q = dot2(a[j], a[j], q); q = dot2(b2[j], b2[j], q); }
  mean = sum32(s) * (1.0f / 512.0f);
  var = fmaf(-mean, mean, sum32(q) * (1.0f / 512.0f));
  if (__builtin_expect(__any(!(var >= mean * mean * 0.015625f)), 0)) {
    float v = 0.f;
#pragma unroll
    for (int j = 0; j < 4; ++j) {
      const float a0 = bf_lo(a[j]) - mean, a1 = bf_hi(a[j]) - mean, a2 = bf_lo(b2[j]) - mean, a3 = bf_hi(b2[j]) - mean;
      v = fmaf(a0, a0, v); v = fmaf(a1, a1, v); v = fmaf(a2, a2, v); v = fmaf(a3, a3, v);
    }
    var = sum32(v) * (1.0f / 512.0f);
  }
}

DI unsigned make_key(float r_bf, int token) {
  unsigned b = __float_as_uint(r_bf) >> 16;
  b = (b & 0x8000u) ? (~b & 0xffffu) : (b | 0x8000u);
  return (b << 16) | (unsigned)(2047 - token);
}
DI float key_val(unsigned key) {
  unsigned b = key >> 16;
  b = (b & 0x8000u) ? (b & 0x7fffu) : (~b & 0xffffu);
  return __uint_as_float(b << 16);
}
DI int key_tok(unsigned key) { return 2047 - (int)(key & 0x7ffu); }

DI u32x4 ld128(rsrc_t rs, unsigned off) { return __builtin_amdgcn_raw_buffer_load_b128(rs, (int)off, 0, SC1); }
DI u32x2 ld64(rsrc_t rs, unsigned off) { return __builtin_amdgcn_raw_buffer_load_b64(rs, (int)off, 0, SC1); }
DI unsigned ld32(rsrc_t rs, unsigned off) { return __builtin_amdgcn_raw_buffer_load_b32(rs, (int)off, 0, SC1); }
DI void st128(rsrc_t rs, unsigned off, u32x4 v) { __builtin_amdgcn_raw_buffer_store_b128(v, rs, (int)off, 0, SC1); }
DI void st64(rsrc_t rs, unsigned off, u32x2 v) { __builtin_amdgcn_raw_buffer_store_b64(v, rs, (int)off, 0, SC1); }
DI void st32(rsrc_t rs, unsigned off, unsigned v) { __builtin_amdgcn_raw_buffer_store_b32(v, rs, (int)off, 0, SC1); }
#define DRAIN() asm volatile("s_waitcnt vmcnt(0)" ::: "memory")
#define VMWAIT(n) asm volatile("s_waitcnt vmcnt(" #n ")" ::: "memory")
#define CBAR() asm volatile("" ::: "memory")
#define RAW_BARRIER() do { asm volatile("s_waitcnt lgkmcnt(0)" ::: "memory"); __builtin_amdgcn_s_barrier(); asm volatile("" ::: "memory"); } while (0)
#define SPIN _Pragma("nounroll") for (unsigned s_ = 0; s_ < SPIN_MAX; ++s_)
DI unsigned lds_ld(const char* p) { return __hip_atomic_load((LAS unsigned*)p, __ATOMIC_RELAXED, __HIP_MEMORY_SCOPE_WORKGROUP); }
DI void lds_wait_ge(const char* p, unsigned want) {
  SPIN {
    if ((unsigned)__builtin_amdgcn_readfirstlane(lds_ld(p)) >= want) break;
    __builtin_amdgcn_s_sleep(1);
  }
  CBAR();
}
#define QK(KF, QF, C) __builtin_amdgcn_mfma_f32_32x32x16_bf16((KF), (QF), (C), 0, 0, 0)
DI f32x16 splat16(float v) { f32x16 r;
#pragma unroll
  for (int i = 0; i < 16; ++i) r[i] = v;
  return r; }
#define SB() __builtin_amdgcn_sched_barrier(0)
#define SGB(mask, n) __builtin_amdgcn_sched_group_barrier(mask, n, 0)

#define VCONST_ASM \
  "v_mov_b32 v132, %[cst]\n\t" "v_mov_b32 v133, %[cst]\n\t" "v_mov_b32 v134, %[cst]\n\t" "v_mov_b32 v135, %[cst]\n\t" "v_mov_b32 v140, %[cst]\n\t" \
  "v_mov_b32 v141, %[cst]\n\t" "v_mov_b32 v142, %[cst]\n\t" "v_mov_b32 v143, %[cst]\n\t" "v_mov_b32 v148, %[cst]\n\t" "v_mov_b32 v149, %[cst]\n\t" \
  "v_mov_b32 v150, %[cst]\n\t" "v_mov_b32 v151, %[cst]\n\t" "v_mov_b32 v156, %[cst]\n\t" "v_mov_b32 v157, %[cst]\n\t" "v_mov_b32 v158, %[cst]\n\t" \
  "v_mov_b32 v159, %[cst]\n\t" "v_mov_b32 v164, %[cst]\n\t" "v_mov_b32 v165, %[cst]\n\t" "v_mov_b32 v166, %[cst]\n\t" "v_mov_b32 v167, %[cst]\n\t" \
  "v_mov_b32 v172, %[cst]\n\t" "v_mov_b32 v173, %[cst]\n\t" "v_mov_b32 v174, %[cst]\n\t" "v_mov_b32 v175, %[cst]\n\t" "v_mov_b32 v180, %[cst]\n\t" \
  "v_mov_b32 v181, %[cst]\n\t" "v_mov_b32 v182, %[cst]\n\t" "v_mov_b32 v183, %[cst]\n\t" "v_mov_b32 v188, %[cst]\n\t" "v_mov_b32 v189, %[cst]\n\t" \
  "v_mov_b32 v190, %[cst]\n\t" "v_mov_b32 v191, %[cst]\n\t" ""
#define LOADS_ASM \
  "buffer_load_dwordx4 v[96:99], %[ka0], %[rs], 0 offen offset:0 sc1\n\t" "buffer_load_dwordx4 v[128:131], %[va0], %[rs], 0 offen offset:0 sc1\n\t" \
  "buffer_load_dwordx4 v[100:103], %[ka0], %[rs], 0 offen offset:1024 sc1\n\t" "buffer_load_dwordx4 v[136:139], %[va0], %[rs], 0 offen offset:1024 sc1\n\t" \
  "buffer_load_dwordx4 v[104:107], %[ka0], %[rs], 0 offen offset:2048 sc1\n\t" "buffer_load_dwordx4 v[144:147], %[va0], %[rs], 0 offen offset:2048 sc1\n\t" \
  "buffer_load_dwordx4 v[108:111], %[ka0], %[rs], 0 offen offset:3072 sc1\n\t" "buffer_load_dwordx4 v[152:155], %[va0], %[rs], 0 offen offset:3072 sc1\n\t" \
  "buffer_load_dwordx4 v[112:115], %[ka1], %[rs], 0 offen offset:0 sc1\n\t" "buffer_load_dwordx4 v[160:163], %[va1], %[rs], 0 offen offset:0 sc1\n\t" \
  "buffer_load_dwordx4 v[116:119], %[ka1], %[rs], 0 offen offset:1024 sc1\n\t" "buffer_load_dwordx4 v[168:171], %[va1], %[rs], 0 offen offset:1024 sc1\n\t" \
  "buffer_load_dwordx4 v[120:123], %[ka1], %[rs], 0 offen offset:2048 sc1\n\t" "buffer_load_dwordx4 v[176:179], %[va1], %[rs], 0 offen offset:2048 sc1\n\t" \
  "buffer_load_dwordx4 v[124:127], %[ka1], %[rs], 0 offen offset:3072 sc1\n\t" "buffer_load_dwordx4 v[184:187], %[va1], %[rs], 0 offen offset:3072 sc1\n\t" ""
#define SWAPS_ASM \
  "v_permlane16_swap_b32 v128, v132\n\t" "v_permlane16_swap_b32 v129, v133\n\t" "v_permlane16_swap_b32 v130, v134\n\t" "v_permlane16_swap_b32 v131, v135\n\t" \
  "v_permlane16_swap_b32 v136, v140\n\t" "v_permlane16_swap_b32 v137, v141\n\t" "v_permlane16_swap_b32 v138, v142\n\t" "v_permlane16_swap_b32 v139, v143\n\t" \
  "v_permlane16_swap_b32 v144, v148\n\t" "v_permlane16_swap_b32 v145, v149\n\t" "v_permlane16_swap_b32 v146, v150\n\t" "v_permlane16_swap_b32 v147, v151\n\t" \
  "v_permlane16_swap_b32 v152, v156\n\t" "v_permlane16_swap_b32 v153, v157\n\t" "v_permlane16_swap_b32 v154, v158\n\t" "v_permlane16_swap_b32 v155, v159\n\t" \
  "v_permlane16_swap_b32 v160, v164\n\t" "v_permlane16_swap_b32 v161, v165\n\t" "v_permlane16_swap_b32 v162, v166\n\t" "v_permlane16_swap_b32 v163, v167\n\t" \
  "v_permlane16_swap_b32 v168, v172\n\t" "v_permlane16_swap_b32 v169, v173\n\t" "v_permlane16_swap_b32 v170, v174\n\t" "v_permlane16_swap_b32 v171, v175\n\t" \
  "v_permlane16_swap_b32 v176, v180\n\t" "v_permlane16_swap_b32 v177, v181\n\t" "v_permlane16_swap_b32 v178, v182\n\t" "v_permlane16_swap_b32 v179, v183\n\t" \
  "v_permlane16_swap_b32 v184, v188\n\t" "v_permlane16_swap_b32 v185, v189\n\t" "v_permlane16_swap_b32 v186, v190\n\t" "v_permlane16_swap_b32 v187, v191\n\t" ""
#define PROLOG0_ASM \
  "s_waitcnt vmcnt(15)\n\t" "s_nop 1\n\t" "v_mfma_f32_32x32x16_bf16 v[208:223], v[96:99], %[q], v[192:207]\n\t" "s_waitcnt vmcnt(13)\n\t" \
  "v_mfma_f32_32x32x16_bf16 v[224:239], v[100:103], %[q], v[192:207]\n\t" "v_permlane16_swap_b32 v128, v132\n\t" "v_permlane16_swap_b32 v129, v133\n\t" \
  "v_permlane16_swap_b32 v130, v134\n\t" "v_permlane16_swap_b32 v131, v135\n\t" "s_nop 6\n\t" "v_exp_f32 v208, v208\n\t" "v_exp_f32 v209, v209\n\t" \
  "v_exp_f32 v210, v210\n\t" "v_exp_f32 v211, v211\n\t" "v_exp_f32 v212, v212\n\t" "v_exp_f32 v213, v213\n\t" "v_exp_f32 v214, v214\n\t" "v_exp_f32 v215, v215\n\t" \
  "v_exp_f32 v216, v216\n\t" "v_exp_f32 v217, v217\n\t" "v_exp_f32 v218, v218\n\t" "v_exp_f32 v219, v219\n\t" "v_exp_f32 v220, v220\n\t" ""
#define PROLOG_ASM \
  "s_nop 1\n\t" "v_mfma_f32_32x32x16_bf16 v[208:223], v[96:99], %[q], v[192:207]\n\t" "v_mfma_f32_32x32x16_bf16 v[224:239], v[100:103], %[q], v[192:207]\n\t" \
  "s_nop 10\n\t" "v_exp_f32 v208, v208\n\t" "v_exp_f32 v209, v209\n\t" "v_exp_f32 v210, v210\n\t" "v_exp_f32 v211, v211\n\t" "v_exp_f32 v212, v212\n\t" \
  "v_exp_f32 v213, v213\n\t" "v_exp_f32 v214, v214\n\t" "v_exp_f32 v215, v215\n\t" "v_exp_f32 v216, v216\n\t" "v_exp_f32 v217, v217\n\t" "v_exp_f32 v218, v218\n\t" \
  "v_exp_f32 v219, v219\n\t" "v_exp_f32 v220, v220\n\t" ""
#define PASS0_ASM \
  "v_mfma_f32_32x32x16_bf16 v[240:255], %[z], %[z], 0\n\t" "v_exp_f32 v221, v221\n\t" "v_exp_f32 v222, v222\n\t" "v_exp_f32 v223, v223\n\t" \
  "v_smfmac_f32_32x32x32_bf16 v[240:255], v[128:131], v[208:215], %[idx]\n\t" "v_exp_f32 v224, v224\n\t" "v_exp_f32 v225, v225\n\t" \
  "v_smfmac_f32_32x32x32_bf16 v[240:255], v[132:135], v[216:223], %[idx]\n\t" "v_exp_f32 v226, v226\n\t" "v_exp_f32 v227, v227\n\t" "v_exp_f32 v228, v228\n\t" \
  "s_waitcnt vmcnt(11)\n\t" "v_mfma_f32_32x32x16_bf16 v[208:223], v[104:107], %[q], v[192:207]\n\t" "v_permlane16_swap_b32 v136, v140\n\t" \
  "v_permlane16_swap_b32 v137, v141\n\t" "v_permlane16_swap_b32 v138, v142\n\t" "v_permlane16_swap_b32 v139, v143\n\t" "v_exp_f32 v229, v229\n\t" \
  "v_exp_f32 v230, v230\n\t" "v_exp_f32 v231, v231\n\t" "v_exp_f32 v232, v232\n\t" "v_exp_f32 v233, v233\n\t" \
  "v_smfmac_f32_32x32x32_bf16 v[240:255], v[136:139], v[224:231], %[idx]\n\t" "v_exp_f32 v234, v234\n\t" "v_exp_f32 v235, v235\n\t" "v_exp_f32 v236, v236\n\t" \
  "v_exp_f32 v237, v237\n\t" "v_exp_f32 v238, v238\n\t" "v_exp_f32 v239, v239\n\t" "v_exp_f32 v208, v208\n\t" "v_exp_f32 v209, v209\n\t" \
  "v_smfmac_f32_32x32x32_bf16 v[240:255], v[140:143], v[232:239], %[idx]\n\t" "v_exp_f32 v210, v210\n\t" "v_exp_f32 v211, v211\n\t" "v_exp_f32 v212, v212\n\t" \
  "s_waitcnt vmcnt(9)\n\t" "v_mfma_f32_32x32x16_bf16 v[224:239], v[108:111], %[q], v[192:207]\n\t" "v_permlane16_swap_b32 v144, v148\n\t" \
  "v_permlane16_swap_b32 v145, v149\n\t" "v_permlane16_swap_b32 v146, v150\n\t" "v_permlane16_swap_b32 v147, v151\n\t" "v_exp_f32 v213, v213\n\t" \
  "v_exp_f32 v214, v214\n\t" "v_exp_f32 v215, v215\n\t" "v_exp_f32 v216, v216\n\t" "v_exp_f32 v217, v217\n\t" \
  "v_smfmac_f32_32x32x32_bf16 v[240:255], v[144:147], v[208:215], %[idx]\n\t" "v_exp_f32 v218, v218\n\t" "v_exp_f32 v219, v219\n\t" "v_exp_f32 v220, v220\n\t" \
  "v_exp_f32 v221, v221\n\t" "v_exp_f32 v222, v222\n\t" "v_exp_f32 v223, v223\n\t" "v_exp_f32 v224, v224\n\t" "v_exp_f32 v225, v225\n\t" \
  "v_smfmac_f32_32x32x32_bf16 v[240:255], v[148:151], v[216:223], %[idx]\n\t" "v_exp_f32 v226, v226\n\t" "v_exp_f32 v227, v227\n\t" "v_exp_f32 v228, v228\n\t" \
  "s_waitcnt vmcnt(7)\n\t" "v_mfma_f32_32x32x16_bf16 v[208:223], v[112:115], %[q], v[192:207]\n\t" "v_permlane16_swap_b32 v152, v156\n\t" \
  "v_permlane16_swap_b32 v153, v157\n\t" "v_permlane16_swap_b32 v154, v158\n\t" "v_permlane16_swap_b32 v155, v159\n\t" "v_exp_f32 v229, v229\n\t" \
  "v_exp_f32 v230, v230\n\t" "v_exp_f32 v231, v231\n\t" "v_exp_f32 v232, v232\n\t" "v_exp_f32 v233, v233\n\t" \
  "v_smfmac_f32_32x32x32_bf16 v[240:255], v[152:155], v[224:231], %[idx]\n\t" "v_exp_f32 v234, v234\n\t" "v_exp_f32 v235, v235\n\t" "v_exp_f32 v236, v236\n\t" \
  "v_exp_f32 v237, v237\n\t" "v_exp_f32 v238, v238\n\t" "v_exp_f32 v239, v239\n\t" "v_exp_f32 v208, v208\n\t" "v_exp_f32 v209, v209\n\t" \
  "v_smfmac_f32_32x32x32_bf16 v[240:255], v[156:159], v[232:239], %[idx]\n\t" "v_exp_f32 v210, v210\n\t" "v_exp_f32 v211, v211\n\t" "v_exp_f32 v212, v212\n\t" \
  "s_waitcnt vmcnt(5)\n\t" "v_mfma_f32_32x32x16_bf16 v[224:239], v[116:119], %[q], v[192:207]\n\t" "v_permlane16_swap_b32 v160, v164\n\t" \
  "v_permlane16_swap_b32 v161, v165\n\t" "v_permlane16_swap_b32 v162, v166\n\t" "v_permlane16_swap_b32 v163, v167\n\t" "v_exp_f32 v213, v213\n\t" \
  "v_exp_f32 v214, v214\n\t" "v_exp_f32 v215, v215\n\t" "v_exp_f32 v216, v216\n\t" "v_exp_f32 v217, v217\n\t" \
  "v_smfmac_f32_32x32x32_bf16 v[240:255], v[160:163], v[208:215], %[idx]\n\t" "v_exp_f32 v218, v218\n\t" "v_exp_f32 v219, v219\n\t" "v_exp_f32 v220, v220\n\t" \
  "v_exp_f32 v221, v221\n\t" "v_exp_f32 v222, v222\n\t" "v_exp_f32 v223, v223\n\t" "v_exp_f32 v224, v224\n\t" "v_exp_f32 v225, v225\n\t" \
  "v_smfmac_f32_32x32x32_bf16 v[240:255], v[164:167], v[216:223], %[idx]\n\t" "v_exp_f32 v226, v226\n\t" "v_exp_f32 v227, v227\n\t" "v_exp_f32 v228, v228\n\t" \
  "s_waitcnt vmcnt(3)\n\t" "v_mfma_f32_32x32x16_bf16 v[208:223], v[120:123], %[q], v[192:207]\n\t" "v_permlane16_swap_b32 v168, v172\n\t" \
  "v_permlane16_swap_b32 v169, v173\n\t" "v_permlane16_swap_b32 v170, v174\n\t" "v_permlane16_swap_b32 v171, v175\n\t" "v_exp_f32 v229, v229\n\t" \
  "v_exp_f32 v230, v230\n\t" "v_exp_f32 v231, v231\n\t" "v_exp_f32 v232, v232\n\t" "v_exp_f32 v233, v233\n\t" \
  "v_smfmac_f32_32x32x32_bf16 v[240:255], v[168:171], v[224:231], %[idx]\n\t" "v_exp_f32 v234, v234\n\t" "v_exp_f32 v235, v235\n\t" "v_exp_f32 v236, v236\n\t" \
  "v_exp_f32 v237, v237\n\t" "v_exp_f32 v238, v238\n\t" "v_exp_f32 v239, v239\n\t" "v_exp_f32 v208, v208\n\t" "v_exp_f32 v209, v209\n\t" \
  "v_smfmac_f32_32x32x32_bf16 v[240:255], v[172:175], v[232:239], %[idx]\n\t" "v_exp_f32 v210, v210\n\t" "v_exp_f32 v211, v211\n\t" "v_exp_f32 v212, v212\n\t" \
  "s_waitcnt vmcnt(1)\n\t" "v_mfma_f32_32x32x16_bf16 v[224:239], v[124:127], %[q], v[192:207]\n\t" "ds_read_b128 %[q], %[qa]\n\t" "v_permlane16_swap_b32 v176, v180\n\t" \
  "v_permlane16_swap_b32 v177, v181\n\t" "v_permlane16_swap_b32 v178, v182\n\t" "v_permlane16_swap_b32 v179, v183\n\t" "v_exp_f32 v213, v213\n\t" \
  "v_exp_f32 v214, v214\n\t" "v_exp_f32 v215, v215\n\t" "v_exp_f32 v216, v216\n\t" "v_exp_f32 v217, v217\n\t" \
  "v_smfmac_f32_32x32x32_bf16 v[240:255], v[176:179], v[208:215], %[idx]\n\t" "v_exp_f32 v218, v218\n\t" "v_exp_f32 v219, v219\n\t" "v_exp_f32 v220, v220\n\t" \
  "v_exp_f32 v221, v221\n\t" "v_exp_f32 v222, v222\n\t" "v_exp_f32 v223, v223\n\t" "v_exp_f32 v224, v224\n\t" "v_exp_f32 v225, v225\n\t" \
  "v_smfmac_f32_32x32x32_bf16 v[240:255], v[180:183], v[216:223], %[idx]\n\t" "v_exp_f32 v226, v226\n\t" "v_exp_f32 v227, v227\n\t" "v_exp_f32 v228, v228\n\t" \
  "s_waitcnt vmcnt(0) lgkmcnt(0)\n\t" "v_mfma_f32_32x32x16_bf16 v[208:223], v[96:99], %[q], v[192:207]\n\t" "v_permlane16_swap_b32 v184, v188\n\t" \
  "v_permlane16_swap_b32 v185, v189\n\t" "v_permlane16_swap_b32 v186, v190\n\t" "v_permlane16_swap_b32 v187, v191\n\t" "v_exp_f32 v229, v229\n\t" \
  "v_exp_f32 v230, v230\n\t" "v_exp_f32 v231, v231\n\t" "v_exp_f32 v232, v232\n\t" "v_exp_f32 v233, v233\n\t" \
  "v_smfmac_f32_32x32x32_bf16 v[240:255], v[184:187], v[224:231], %[idx]\n\t" "v_exp_f32 v234, v234\n\t" "v_exp_f32 v235, v235\n\t" "v_exp_f32 v236, v236\n\t" \
  "v_exp_f32 v237, v237\n\t" "v_exp_f32 v238, v238\n\t" "v_exp_f32 v239, v239\n\t" "v_exp_f32 v208, v208\n\t" "v_exp_f32 v209, v209\n\t" \
  "v_smfmac_f32_32x32x32_bf16 v[240:255], v[188:191], v[232:239], %[idx]\n\t" "v_exp_f32 v210, v210\n\t" "v_exp_f32 v211, v211\n\t" "v_exp_f32 v212, v212\n\t" \
  "v_mfma_f32_32x32x16_bf16 v[224:239], v[100:103], %[q], v[192:207]\n\t" "v_exp_f32 v213, v213\n\t" "v_exp_f32 v214, v214\n\t" "v_exp_f32 v215, v215\n\t" \
  "v_exp_f32 v216, v216\n\t" "v_exp_f32 v217, v217\n\t" "v_exp_f32 v218, v218\n\t" "v_exp_f32 v219, v219\n\t" "v_exp_f32 v220, v220\n\t" ""
#define PASSN_ASM \
  "v_mfma_f32_32x32x16_bf16 v[240:255], %[z], %[z], 0\n\t" "v_exp_f32 v221, v221\n\t" "v_exp_f32 v222, v222\n\t" "v_exp_f32 v223, v223\n\t" \
  "v_smfmac_f32_32x32x32_bf16 v[240:255], v[128:131], v[208:215], %[idx]\n\t" "v_exp_f32 v224, v224\n\t" "v_exp_f32 v225, v225\n\t" \
  "v_smfmac_f32_32x32x32_bf16 v[240:255], v[132:135], v[216:223], %[idx]\n\t" "v_exp_f32 v226, v226\n\t" "v_exp_f32 v227, v227\n\t" "v_exp_f32 v228, v228\n\t" \
  "v_mfma_f32_32x32x16_bf16 v[208:223], v[104:107], %[q], v[192:207]\n\t" "v_exp_f32 v229, v229\n\t" "v_exp_f32 v230, v230\n\t" "v_exp_f32 v231, v231\n\t" \
  "v_exp_f32 v232, v232\n\t" "v_exp_f32 v233, v233\n\t" "v_smfmac_f32_32x32x32_bf16 v[240:255], v[136:139], v[224:231], %[idx]\n\t" "v_exp_f32 v234, v234\n\t" \
  "v_exp_f32 v235, v235\n\t" "v_exp_f32 v236, v236\n\t" "v_exp_f32 v237, v237\n\t" "v_exp_f32 v238, v238\n\t" "v_exp_f32 v239, v239\n\t" "v_exp_f32 v208, v208\n\t" \
  "v_exp_f32 v209, v209\n\t" "v_smfmac_f32_32x32x32_bf16 v[240:255], v[140:143], v[232:239], %[idx]\n\t" "v_exp_f32 v210, v210\n\t" "v_exp_f32 v211, v211\n\t" \
  "v_exp_f32 v212, v212\n\t" "v_mfma_f32_32x32x16_bf16 v[224:239], v[108:111], %[q], v[192:207]\n\t" "v_exp_f32 v213, v213\n\t" "v_exp_f32 v214, v214\n\t" \
  "v_exp_f32 v215, v215\n\t" "v_exp_f32 v216, v216\n\t" "v_exp_f32 v217, v217\n\t" "v_smfmac_f32_32x32x32_bf16 v[240:255], v[144:147], v[208:215], %[idx]\n\t" \
  "v_exp_f32 v218, v218\n\t" "v_exp_f32 v219, v219\n\t" "v_exp_f32 v220, v220\n\t" "v_exp_f32 v221, v221\n\t" "v_exp_f32 v222, v222\n\t" "v_exp_f32 v223, v223\n\t" \
  "v_exp_f32 v224, v224\n\t" "v_exp_f32 v225, v225\n\t" "v_smfmac_f32_32x32x32_bf16 v[240:255], v[148:151], v[216:223], %[idx]\n\t" "v_exp_f32 v226, v226\n\t" \
  "v_exp_f32 v227, v227\n\t" "v_exp_f32 v228, v228\n\t" "v_mfma_f32_32x32x16_bf16 v[208:223], v[112:115], %[q], v[192:207]\n\t" "v_exp_f32 v229, v229\n\t" \
  "v_exp_f32 v230, v230\n\t" "v_exp_f32 v231, v231\n\t" "v_exp_f32 v232, v232\n\t" "v_exp_f32 v233, v233\n\t" \
  "v_smfmac_f32_32x32x32_bf16 v[240:255], v[152:155], v[224:231], %[idx]\n\t" "v_exp_f32 v234, v234\n\t" "v_exp_f32 v235, v235\n\t" "v_exp_f32 v236, v236\n\t" \
  "v_exp_f32 v237, v237\n\t" "v_exp_f32 v238, v238\n\t" "v_exp_f32 v239, v239\n\t" "v_exp_f32 v208, v208\n\t" "v_exp_f32 v209, v209\n\t" \
  "v_smfmac_f32_32x32x32_bf16 v[240:255], v[156:159], v[232:239], %[idx]\n\t" "v_exp_f32 v210, v210\n\t" "v_exp_f32 v211, v211\n\t" "v_exp_f32 v212, v212\n\t" \
  "v_mfma_f32_32x32x16_bf16 v[224:239], v[116:119], %[q], v[192:207]\n\t" "v_exp_f32 v213, v213\n\t" "v_exp_f32 v214, v214\n\t" "v_exp_f32 v215, v215\n\t" \
  "v_exp_f32 v216, v216\n\t" "v_exp_f32 v217, v217\n\t" "v_smfmac_f32_32x32x32_bf16 v[240:255], v[160:163], v[208:215], %[idx]\n\t" "v_exp_f32 v218, v218\n\t" \
  "v_exp_f32 v219, v219\n\t" "v_exp_f32 v220, v220\n\t" "v_exp_f32 v221, v221\n\t" "v_exp_f32 v222, v222\n\t" "v_exp_f32 v223, v223\n\t" "v_exp_f32 v224, v224\n\t" \
  "v_exp_f32 v225, v225\n\t" "v_smfmac_f32_32x32x32_bf16 v[240:255], v[164:167], v[216:223], %[idx]\n\t" "v_exp_f32 v226, v226\n\t" "v_exp_f32 v227, v227\n\t" \
  "v_exp_f32 v228, v228\n\t" "v_mfma_f32_32x32x16_bf16 v[208:223], v[120:123], %[q], v[192:207]\n\t" "v_exp_f32 v229, v229\n\t" "v_exp_f32 v230, v230\n\t" \
  "v_exp_f32 v231, v231\n\t" "v_exp_f32 v232, v232\n\t" "v_exp_f32 v233, v233\n\t" "v_smfmac_f32_32x32x32_bf16 v[240:255], v[168:171], v[224:231], %[idx]\n\t" \
  "v_exp_f32 v234, v234\n\t" "v_exp_f32 v235, v235\n\t" "v_exp_f32 v236, v236\n\t" "v_exp_f32 v237, v237\n\t" "v_exp_f32 v238, v238\n\t" "v_exp_f32 v239, v239\n\t" \
  "v_exp_f32 v208, v208\n\t" "v_exp_f32 v209, v209\n\t" "v_smfmac_f32_32x32x32_bf16 v[240:255], v[172:175], v[232:239], %[idx]\n\t" "v_exp_f32 v210, v210\n\t" \
  "v_exp_f32 v211, v211\n\t" "v_exp_f32 v212, v212\n\t" "v_mfma_f32_32x32x16_bf16 v[224:239], v[124:127], %[q], v[192:207]\n\t" "ds_read_b128 %[q], %[qa]\n\t" \
  "v_exp_f32 v213, v213\n\t" "v_exp_f32 v214, v214\n\t" "v_exp_f32 v215, v215\n\t" "v_exp_f32 v216, v216\n\t" "v_exp_f32 v217, v217\n\t" \
  "v_smfmac_f32_32x32x32_bf16 v[240:255], v[176:179], v[208:215], %[idx]\n\t" "v_exp_f32 v218, v218\n\t" "v_exp_f32 v219, v219\n\t" "v_exp_f32 v220, v220\n\t" \
  "v_exp_f32 v221, v221\n\t" "v_exp_f32 v222, v222\n\t" "v_exp_f32 v223, v223\n\t" "v_exp_f32 v224, v224\n\t" "v_exp_f32 v225, v225\n\t" \
  "v_smfmac_f32_32x32x32_bf16 v[240:255], v[180:183], v[216:223], %[idx]\n\t" "v_exp_f32 v226, v226\n\t" "v_exp_f32 v227, v227\n\t" "v_exp_f32 v228, v228\n\t" \
  "s_waitcnt lgkmcnt(0)\n\t" "v_mfma_f32_32x32x16_bf16 v[208:223], v[96:99], %[q], v[192:207]\n\t" "v_exp_f32 v229, v229\n\t" "v_exp_f32 v230, v230\n\t" \
  "v_exp_f32 v231, v231\n\t" "v_exp_f32 v232, v232\n\t" "v_exp_f32 v233, v233\n\t" "v_smfmac_f32_32x32x32_bf16 v[240:255], v[184:187], v[224:231], %[idx]\n\t" \
  "v_exp_f32 v234, v234\n\t" "v_exp_f32 v235, v235\n\t" "v_exp_f32 v236, v236\n\t" "v_exp_f32 v237, v237\n\t" "v_exp_f32 v238, v238\n\t" "v_exp_f32 v239, v239\n\t" \
  "v_exp_f32 v208, v208\n\t" "v_exp_f32 v209, v209\n\t" "v_smfmac_f32_32x32x32_bf16 v[240:255], v[188:191], v[232:239], %[idx]\n\t" "v_exp_f32 v210, v210\n\t" \
  "v_exp_f32 v211, v211\n\t" "v_exp_f32 v212, v212\n\t" "v_mfma_f32_32x32x16_bf16 v[224:239], v[100:103], %[q], v[192:207]\n\t" "v_exp_f32 v213, v213\n\t" \
  "v_exp_f32 v214, v214\n\t" "v_exp_f32 v215, v215\n\t" "v_exp_f32 v216, v216\n\t" "v_exp_f32 v217, v217\n\t" "v_exp_f32 v218, v218\n\t" "v_exp_f32 v219, v219\n\t" \
  "v_exp_f32 v220, v220\n\t" ""
#define ROWMAX_ASM \
  "s_nop 1\n\t" "v_mfma_f32_32x32x16_bf16 v[224:239], v[96:99], %[q], 0\n\t" "s_nop 11\n\t" "v_max3_f32 %[m], %[m], v224, v225\n\t" \
  "v_max3_f32 %[m], %[m], v226, v227\n\t" "v_max3_f32 %[m], %[m], v228, v229\n\t" "v_max3_f32 %[m], %[m], v230, v231\n\t" "v_max3_f32 %[m], %[m], v232, v233\n\t" \
  "v_max3_f32 %[m], %[m], v234, v235\n\t" "v_max3_f32 %[m], %[m], v236, v237\n\t" "v_max3_f32 %[m], %[m], v238, v239\n\t" \
  "v_mfma_f32_32x32x16_bf16 v[224:239], v[100:103], %[q], 0\n\t" "s_nop 11\n\t" "v_max3_f32 %[m], %[m], v224, v225\n\t" "v_max3_f32 %[m], %[m], v226, v227\n\t" \
  "v_max3_f32 %[m], %[m], v228, v229\n\t" "v_max3_f32 %[m], %[m], v230, v231\n\t" "v_max3_f32 %[m], %[m], v232, v233\n\t" "v_max3_f32 %[m], %[m], v234, v235\n\t" \
  "v_max3_f32 %[m], %[m], v236, v237\n\t" "v_max3_f32 %[m], %[m], v238, v239\n\t" "v_mfma_f32_32x32x16_bf16 v[224:239], v[104:107], %[q], 0\n\t" "s_nop 11\n\t" \
  "v_max3_f32 %[m], %[m], v224, v225\n\t" "v_max3_f32 %[m], %[m], v226, v227\n\t" "v_max3_f32 %[m], %[m], v228, v229\n\t" "v_max3_f32 %[m], %[m], v230, v231\n\t" \
  "v_max3_f32 %[m], %[m], v232, v233\n\t" "v_max3_f32 %[m], %[m], v234, v235\n\t" "v_max3_f32 %[m], %[m], v236, v237\n\t" "v_max3_f32 %[m], %[m], v238, v239\n\t" \
  "v_mfma_f32_32x32x16_bf16 v[224:239], v[108:111], %[q], 0\n\t" "s_nop 11\n\t" "v_max3_f32 %[m], %[m], v224, v225\n\t" "v_max3_f32 %[m], %[m], v226, v227\n\t" \
  "v_max3_f32 %[m], %[m], v228, v229\n\t" "v_max3_f32 %[m], %[m], v230, v231\n\t" "v_max3_f32 %[m], %[m], v232, v233\n\t" "v_max3_f32 %[m], %[m], v234, v235\n\t" \
  "v_max3_f32 %[m], %[m], v236, v237\n\t" "v_max3_f32 %[m], %[m], v238, v239\n\t" "v_mfma_f32_32x32x16_bf16 v[224:239], v[112:115], %[q], 0\n\t" "s_nop 11\n\t" \
  "v_max3_f32 %[m], %[m], v224, v225\n\t" "v_max3_f32 %[m], %[m], v226, v227\n\t" "v_max3_f32 %[m], %[m], v228, v229\n\t" "v_max3_f32 %[m], %[m], v230, v231\n\t" \
  "v_max3_f32 %[m], %[m], v232, v233\n\t" "v_max3_f32 %[m], %[m], v234, v235\n\t" "v_max3_f32 %[m], %[m], v236, v237\n\t" "v_max3_f32 %[m], %[m], v238, v239\n\t" \
  "v_mfma_f32_32x32x16_bf16 v[224:239], v[116:119], %[q], 0\n\t" "s_nop 11\n\t" "v_max3_f32 %[m], %[m], v224, v225\n\t" "v_max3_f32 %[m], %[m], v226, v227\n\t" \
  "v_max3_f32 %[m], %[m], v228, v229\n\t" "v_max3_f32 %[m], %[m], v230, v231\n\t" "v_max3_f32 %[m], %[m], v232, v233\n\t" "v_max3_f32 %[m], %[m], v234, v235\n\t" \
  "v_max3_f32 %[m], %[m], v236, v237\n\t" "v_max3_f32 %[m], %[m], v238, v239\n\t" "v_mfma_f32_32x32x16_bf16 v[224:239], v[120:123], %[q], 0\n\t" "s_nop 11\n\t" \
  "v_max3_f32 %[m], %[m], v224, v225\n\t" "v_max3_f32 %[m], %[m], v226, v227\n\t" "v_max3_f32 %[m], %[m], v228, v229\n\t" "v_max3_f32 %[m], %[m], v230, v231\n\t" \
  "v_max3_f32 %[m], %[m], v232, v233\n\t" "v_max3_f32 %[m], %[m], v234, v235\n\t" "v_max3_f32 %[m], %[m], v236, v237\n\t" "v_max3_f32 %[m], %[m], v238, v239\n\t" \
  "v_mfma_f32_32x32x16_bf16 v[224:239], v[124:127], %[q], 0\n\t" "s_nop 11\n\t" "v_max3_f32 %[m], %[m], v224, v225\n\t" "v_max3_f32 %[m], %[m], v226, v227\n\t" \
  "v_max3_f32 %[m], %[m], v228, v229\n\t" "v_max3_f32 %[m], %[m], v230, v231\n\t" "v_max3_f32 %[m], %[m], v232, v233\n\t" "v_max3_f32 %[m], %[m], v234, v235\n\t" \
  "v_max3_f32 %[m], %[m], v236, v237\n\t" "v_max3_f32 %[m], %[m], v238, v239\n\t" ""
#define POST_ASM \
  "s_add_u32 %[i], %[i], 1\n\t" "s_cmp_eq_u32 %[i], 8\n\t" "s_cbranch_scc1 .Lfb_out%=\n\t" "v_cndmask_b32 %[t1], %[sh], v248, %[lo]\n\t" \
  "v_add_u32 %[t2], %[so], %[l16]\n\t" "ds_write_b128 %[t2], v[240:243]\n\t" "ds_write_b128 %[t2], v[244:247] offset:1024\n\t" "v_add_u32 %[t2], %[so], %[l4]\n\t" \
  "ds_write_b32 %[t2], %[t1] offset:2048\n\t" "s_add_u32 %[so], %[so], 14080\n\t" "s_and_b32 %[so], %[so], 0x1ffff\n\t" "s_add_u32 %[qo], %[qo], 1024\n\t" \
  "s_and_b32 %[qo], %[qo], 0x1fff\n\t" "v_add_u32 %[qa], %[qo], %[qb]\n\t" ""
#define WOLOAD_ASM \
  "s_cmp_lg_u32 %[i], %[iw]\n\t" "s_cbranch_scc1 .Lfb_skip%=\n\t" "s_mov_b64 %[wb2], %[wb]\n\t" "global_load_dwordx4 v[64:67], %[ro], %[wb2]\n\t" \
  "v_add_u32 %[ro], 0x2000, %[ro]\n\t" "global_load_dwordx4 v[68:71], %[ro], %[wb2]\n\t" "v_add_u32 %[ro], 0x2000, %[ro]\n\t" \
  "global_load_dwordx4 v[72:75], %[ro], %[wb2]\n\t" "v_add_u32 %[ro], 0x2000, %[ro]\n\t" "global_load_dwordx4 v[76:79], %[ro], %[wb2]\n\t" \
  "v_add_u32 %[ro], 0x2000, %[ro]\n\t" "global_load_dwordx4 v[80:83], %[ro], %[wb2]\n\t" "v_add_u32 %[ro], 0x2000, %[ro]\n\t" \
  "global_load_dwordx4 v[84:87], %[ro], %[wb2]\n\t" "v_add_u32 %[ro], 0x2000, %[ro]\n\t" "global_load_dwordx4 v[88:91], %[ro], %[wb2]\n\t" \
  "v_add_u32 %[ro], 0x2000, %[ro]\n\t" "global_load_dwordx4 v[92:95], %[ro], %[wb2]\n\t" ""

#define KV_OUT "=&{v[96:127]}"(kv0), "=&{v[128:159]}"(kv1), "=&{v[160:191]}"(kv2)
#define KV_IO "+{v[96:127]}"(kv0), "+{v[128:159]}"(kv1), "+{v[160:191]}"(kv2)
#define KV_IN "{v[96:127]}"(kv0), "{v[128:159]}"(kv1), "{v[160:191]}"(kv2)
#define KV_ADDR [ka0] "v"(kb), [ka1] "v"(kb + 4096), [va0] "v"(vp), [va1] "v"(vp + 4096), [rs] "s"(rs)

__global__ __launch_bounds__(512, 2) void fused_block(const u16* __restrict__ x, const u16* __restrict__ wq, const u16* __restrict__ wk,
                                                      const u16* __restrict__ wv, const u16* __restrict__ wo, const u16* __restrict__ wr,
                                                      const u16* __restrict__ wg, const u16* __restrict__ wu, const u16* __restrict__ wd,
                                                      char* ws, u16* __restrict__ out) {
  __shared__ __attribute__((aligned(16))) char smem[L_TOTAL];
  unsigned pf1, pf2;
  {
    unsigned long long pc; unsigned t;
    asm volatile("s_getpc_b64 %[pc]\n.Lfb_begin:\n\t"
                 "v_and_b32 %[a], 15, %[tid]\n\tv_lshlrev_b32 %[b], 7, %[a]\n\tglobal_load_dword %[b], %[b], %[pc]\n\t"
                 "s_lshr_b32 %[t], %[bx], 3\n\ts_mul_i32 %[t], %[t], 1152\n\ts_add_u32 %[t], %[t], 2048\n\t"
                 "v_min_u32 %[a], 8, %[a]\n\tv_lshl_add_u32 %[a], %[a], 7, %[t]\n\tv_min_u32 %[a], .Lfb_end-.Lfb_begin, %[a]\n\t"
                 "global_load_dword %[a], %[a], %[pc]"
                 : [pc] "=&s"(pc), [t] "=&s"(t), [a] "=&v"(pf1), [b] "=&v"(pf2) : [tid] "v"(threadIdx.x), [bx] "s"(blockIdx.x) : "scc", "memory");
  }
  char* const XI = smem + L_XI;
  char* const WI = smem + L_WI;
  char* const KI = smem + L_KI;
  char* const CTL = smem + L_CTL;
  const int tid = threadIdx.x, lane = tid & 63;
  const int w = __builtin_amdgcn_readfirstlane(tid >> 6);
  const int b = blockIdx.x, g8 = b & 7, tb = b & 31, hg = b >> 5;
  const int rr = lane >> 3, c = lane & 7;
  const int h = lane >> 5, l31 = lane & 31;
  const int isV = hg & 1, lr0 = (hg >> 1) * 16;
  const int pid = tb * 8 + hg;
  const int u = (b & 7) * 28 + (b >> 3);
  const rsrc_t rs = __builtin_amdgcn_make_buffer_rsrc(ws, 0, (int)WS_END, 0x00020000);
  STAMP(0, 0);

  unsigned tag;
  asm volatile("s_load_dword %0, %1, 0x0" : "=s"(tag) : "s"(ws) : "memory");
  const bool kvw = (w == (hg & 6));
  const bool ldw = (w == (hg | 1));
  u32x4 xo0, xo1, wr0, wr1, xe, xv[8];
  f32x32 wov;
  unsigned pf0;
  {
    const u16* p = x + (size_t)(tb * 64 + lr0 + 2 * w) * D_;
    GLD128(xo0, h * 1024 + l31 * 16, p);
    GLD128(xo1, h * 1024 + l31 * 16 + 512, p);
  }
#pragma unroll
  for (int i = 0; i < 2; ++i) glds16((isV ? wv : wk) + (w * 2 + i) * 512, lane * 16, KI + (w * 2 + i) * 1024);
  {
    unsigned long long ta = (unsigned long long)ws + (lane == 0 ? WS_YP + b * 5376 : (lane == 1 ? WS_K : WS_KVF));
    if (lane == 3) ta = (unsigned long long)(out + (size_t)tb * 64 * D_);
    asm volatile("global_load_dword %0, %1, off" : "=v"(pf0) : "v"(ta) : "memory");
  }
  GLD128(wr0, l31 * 16, wr);
  GLD128(wr1, l31 * 16 + 512, wr);
  const unsigned row_off = rr * 1024 + c * 16;
  const unsigned wq_off = rr * 1024 + ((c ^ (((rr >> 1) & 1) << 2)) << 4);
  const u16* xrows = x + (size_t)(tb * 64 + w * 8) * D_;
  if (kvw) {
    __builtin_amdgcn_s_setprio(3);
#pragma unroll
    for (int j = 0; j < 8; ++j)
      st128(rs, WS_O + (unsigned)((tb * 2 + (j >> 2)) * 32 + hg * 4 + (j & 3)) * 1024 + lane * 16, u32x4{0xffffffffu, 0xffffffffu, 0xffffffffu, 0xffffffffu});
  } else if (ldw) {
#pragma unroll
    for (int pc = 0; pc < 8; ++pc) glds16(wq + pc * 8 * D_ + hg * 64, wq_off, WI + pc * 1024);
  } else {
#pragma unroll
    for (int i = 0; i < 8; ++i) asm volatile("global_load_dwordx4 %0, %1, %2 offset:%3" : "=v"(xv[i]) : "v"(row_off), "s"(xrows), "i"(i * 128) : "memory");
  }
  VMWAIT(13);
  asm volatile("" : "+v"(xo0), "+v"(xo1), "+v"(pf1), "+v"(pf2));
  STAMP(0, 1);
  {
    float mean, var;
    stats32(xo0, xo1, mean, var);
    const float mb = rbf(mean), sd = ln_sd(var), inv = __builtin_amdgcn_rcpf(sd);
#pragma unroll
    for (int j = 0; j < 4; ++j) { xo0[j] = ln_pair(xo0[j], mb, inv); xo1[j] = ln_pair(xo1[j], mb, inv); }
    const int lrow = lr0 + 2 * w + h;
    char* xr = XI + lrow * 1024 + ((l31 ^ (lrow & 15)) << 4);
    *(u32x4*)xr = xo0; *(u32x4*)(xr + 512) = xo1;
    if (l31 == 0) *(f32x2*)(CTL + C_STAT + lrow * 8) = f32x2{mb, sd};
    if (tid < 2) *(unsigned*)(CTL + tid * 4) = 0u;
    VMWAIT(11);
    asm volatile("s_waitcnt lgkmcnt(0)" : "+s"(tag) :: "memory");
    __builtin_amdgcn_s_barrier();
    CBAR();
  }
  tag += 1u;
  STAMP(0, 2);
  if (kvw) {
    const int g = lane >> 4, q = (lane & 15) >> 2, p = lane & 3;
    const char* xb = XI + (lr0 + (lane & 15)) * 1024;
    const char* wb = opq(KI + (8 * g + q) * 32 + p * 8);
    f32x4 acc = {}, act = {};
    bf16x8 fx[2][4], fw[2][4];
#define KV_LD(bf, k0) _Pragma("unroll") for (int i = 0; i < 4; ++i) { const int ks = (k0) + i; \
      fx[bf][i] = *(const bf16x8*)(xb + ((((4 * i + g) ^ (lane & 15)) << 4) + (ks >> 2) * 256)); \
      fw[bf][i] = tr_frag(wb + ks * 1024, wb + ks * 1024 + 128); }
    KV_LD(0, 0)
#pragma unroll
    for (int kb = 0; kb < 4; ++kb) {
      if (kb < 3) { KV_LD((kb + 1) & 1, 4 * kb + 4) }
      SB();
#pragma unroll
      for (int i = 0; i < 4; ++i) {
        acc = __builtin_amdgcn_mfma_f32_16x16x32_bf16(fw[kb & 1][i], fx[kb & 1][i], acc, 0, 0, 0);
        act = __builtin_amdgcn_mfma_f32_16x16x32_bf16(fx[kb & 1][i], fw[kb & 1][i], act, 0, 0, 0);
      }
      SB();
    }
    if (isV) acc = act;
    const u32x2 pk = {pack_bf(acc[0], acc[1]), pack_bf(acc[2], acc[3])};
    float n = 0.f;
    if (!isV) {
      st64(rs, WS_K + (unsigned)(tb * 64 + lr0 + (lane & 15)) * 32 + (((g & 1) << 1) | (g >> 1)) * 8, pk);
      n = wave_maxpos(add32(add16(acc[0] * acc[0] + acc[1] * acc[1] + acc[2] * acc[2] + acc[3] * acc[3])));
    } else {
      st64(rs, WS_V + (unsigned)(pid - 1 + (g >> 1)) * VHALF + (lane & 15) * 16 + (g & 1) * 8, pk);
    }
    DRAIN();
    if (lane < 8) st64(rs, WS_KVF + (unsigned)(lane * 256 + pid) * 8, u32x2{__float_as_uint(n), tag});
    __builtin_amdgcn_s_setprio(0);
    STAMP(0, 6);
  } else if (ldw) {
#pragma unroll 4
    for (int pc = 8; pc < 28; ++pc) glds16(wq + pc * 8 * D_ + hg * 64, wq_off, WI + pc * 1024);
  } else {
    const int w6 = w - (w > hg ? 2 : 0);
#pragma unroll
    for (int i = 0; i < 6; ++i) { const int pc = 28 + i * 6 + w6; glds16(wq + pc * 8 * D_ + hg * 64, wq_off, WI + pc * 1024); }
    asm volatile("s_waitcnt vmcnt(6)" : "+v"(xv[0]), "+v"(xv[1]), "+v"(xv[2]), "+v"(xv[3]), "+v"(xv[4]), "+v"(xv[5]), "+v"(xv[6]), "+v"(xv[7]) :: "memory");
    float s = 0.f, q = 0.f;
#pragma unroll
    for (int i = 0; i < 8; ++i)
#pragma unroll
      for (int j = 0; j < 4; ++j) { s = dot2(xv[i][j], 0x3f803f80u, s); q = dot2(xv[i][j], xv[i][j], q); }
    const float mean = sum8(s) * (1.0f / 512.0f);
    float var = fmaf(-mean, mean, sum8(q) * (1.0f / 512.0f));
    if (__builtin_expect(__any(!(var >= mean * mean * 0.015625f)), 0)) {
      float v = 0.f;
#pragma unroll
      for (int i = 0; i < 8; ++i)
#pragma unroll
        for (int j = 0; j < 4; ++j) { const float a = bf_lo(xv[i][j]) - mean, bb = bf_hi(xv[i][j]) - mean; v = fmaf(a, a, v); v = fmaf(bb, bb, v); }
      var = sum8(v) * (1.0f / 512.0f);
    }
    const float st_m = rbf(mean), st_s = ln_sd(var);
    const float inv = __builtin_amdgcn_rcpf(st_s), nb = -st_m * inv;
    const int lrow = w * 8 + rr;
#pragma unroll
    for (int i = 0; i < 8; ++i) {
      u32x4 o;
#pragma unroll
      for (int j = 0; j < 4; ++j) o[j] = pack_bf(fmaf(bf_lo(xv[i][j]), inv, nb), fmaf(bf_hi(xv[i][j]), inv, nb));
      *(u32x4*)(XI + lrow * 1024 + (((i * 8 + c) ^ (lrow & 15)) << 4)) = o;
    }
    if (c == 0) *(f32x2*)(CTL + C_STAT + lrow * 8) = f32x2{st_m, st_s};
    STAMP(0, 3);
  }
  VMWAIT(28);
  asm volatile("" : "+v"(pf0), "+v"(wr0), "+v"(wr1));
  if ((w >> 2) == isV) {
    float r = 0.f;
#pragma unroll
    for (int j = 0; j < 4; ++j) { r = dot2(xo0[j], wr0[j], r); r = dot2(xo1[j], wr1[j], r); }
    const unsigned rkey = make_key(rbf(sum32(r)), pid * 8 + 2 * (w & 3) + h);
    if (l31 == 0) *(unsigned*)(CTL + C_R + (2 * (w & 3) + h) * 4) = rkey;
  }
  if (!kvw) DRAIN();
  RAW_BARRIER();
  STAMP(0, 4);
  if (w < 4) __builtin_amdgcn_s_setprio(2); else __builtin_amdgcn_s_setprio(1);
  if (w == 4) {
    const unsigned key = lane < 8 ? *(const unsigned*)(CTL + C_R + lane * 4) : 0u;
    const unsigned k1 = wave_maxu(key), k2 = wave_maxu(key == k1 ? 0u : key);
    if (lane < 8) st128(rs, WS_CAND + (unsigned)(lane * 256 + pid) * 16, u32x4{k1, tag, k2, tag});
  }
  const int blk = (lane >> 4) & 1, q4 = (lane & 15) >> 2, p4 = lane & 3;
  if (w < 4) {
    const int nt = w & 1, tt = w >> 1;
    const int tok = tt * 32 + l31;
    const char* xb = XI + tok * 1024;
    const char* wb = WI + (8 * h + q4) * 128 + (((4 * nt + 2 * blk + (p4 >> 1)) ^ (((q4 >> 1) & 1) << 2)) << 4) + 8 * (p4 & 1);
    f32x16 acc = {};
    bf16x8 fa[2][4], fb[2][4];
#define Q_LD(bf, k0) _Pragma("unroll") for (int i = 0; i < 4; ++i) { const int ks = (k0) + i; \
      fa[bf][i] = tr_frag(wb + ks * 2048, wb + ks * 2048 + 512); \
      fb[bf][i] = *(const bf16x8*)(xb + ((((2 * (ks & 7) + h) ^ (tok & 15)) << 4) + (ks >> 3) * 256)); }
    Q_LD(0, 0)
    SB();
#pragma unroll
    for (int kb = 0; kb < 8; ++kb) {
      if (kb < 7) { Q_LD((kb + 1) & 1, 4 * kb + 4) }
#pragma unroll
      for (int i = 0; i < 4; ++i) acc = __builtin_amdgcn_mfma_f32_32x32x16_bf16(fa[kb & 1][i], fb[kb & 1][i], acc, 0, 0, 0);
#pragma unroll
      for (int i = 0; i < 4; ++i) { SGB(0x8, 1); SGB(0x100, 3); }
      SB();
    }
    const float cs = 0.25f * 1.4426950408889634f;
    float n0 = 0.f, n1 = 0.f;
#pragma unroll
    for (int r = 0; r < 8; ++r) { acc[r] *= cs; acc[r + 8] *= cs; n0 = fmaf(acc[r], acc[r], n0); n1 = fmaf(acc[r + 8], acc[r + 8], n1); }
    char* qi = smem + L_QI + (tt * 4 + nt * 2) * 1024 + lane * 16;
    *(u32x4*)qi = u32x4{pack_bf(acc[0], acc[1]), pack_bf(acc[2], acc[3]), pack_bf(acc[4], acc[5]), pack_bf(acc[6], acc[7])};
    *(u32x4*)(qi + 1024) = u32x4{pack_bf(acc[8], acc[9]), pack_bf(acc[10], acc[11]), pack_bf(acc[12], acc[13]), pack_bf(acc[14], acc[15])};
    const float nm = wave_maxpos(fmaxf(add32(n0), add32(n1)));
    if (lane == 0) {
      *(float*)(CTL + C_QMAX + w * 4) = nm;
      asm volatile("s_waitcnt lgkmcnt(0)\n\tds_add_u32 %0, %1" :: "v"((unsigned)(uintptr_t)(LAS char*)(CTL + C_CNT)), "v"(1u) : "memory");
    }
  }
  STAMP(0, 5);

  xe = *(const u32x4*)((const char*)(xrows + hg * 64) + row_off);
  f32x32 kv0, kv1, kv2;
  asm volatile(VCONST_ASM : KV_OUT : [cst] "v"((lane & 15) ? 0u : 0x3f803f80u));
  float kmax;
  {
    u32x2 f;
    SPIN {
      f = ld64(rs, WS_KVF + (unsigned)(g8 * 256 + w * 32 + l31) * 8);
      if (__all(f[1] == tag)) break;
      __builtin_amdgcn_s_sleep(1);
    }
    CBAR();
    kmax = __uint_as_float(f[0]);
  }
  STAMP(1, 0);
  {
    const unsigned kb = WS_K + (unsigned)(w * 256 + l31) * 32 + h * 16;
    const unsigned vp = WS_V + (unsigned)(w * 32 + 2 * blk + h) * VHALF + (lane & 15) * 16;
    asm volatile(LOADS_ASM : KV_IO : KV_ADDR : "memory");
  }
  kmax = wave_maxpos(kmax);
  u32x4 gatep;
  {
    const f32x2 st = *(const f32x2*)(CTL + C_STAT + (w * 8 + rr) * 8);
    const float inv = __builtin_amdgcn_rcpf(st[1]);
#pragma unroll
    for (int j = 0; j < 4; ++j) gatep[j] = silu_pair(ln_pair(xe[j], st[0], inv));
    asm volatile("" : "+v"(gatep));
  }
  lds_wait_ge(CTL + C_CNT, 4u);
  STAMP(1, 1);
  const f32x4 qn = *(const f32x4*)(CTL + C_QMAX);
  const float qmax = fmaxf(fmaxf(qn[0], qn[1]), fmaxf(qn[2], qn[3]));
  const float bound = __builtin_amdgcn_sqrtf(qmax * kmax) * 1.02f + 0.01f;
  int fasti = __builtin_amdgcn_readfirstlane(bound <= 48.0f ? 1 : 0);
  asm volatile("" : "+s"(fasti));
  const bool fast = fasti != 0;
  const int idx = (int)0xDDDDDDDDu;
  bf16x8 zf = {};
  asm volatile("" : "+v"(zf));
  const f32x16 z16 = {};
  const char* qbase = smem + L_QI + lane * 16;
  f32x16 o;
  float shift = bound;
  bf16x8 qf = *(const bf16x8*)(qbase + ((w + 1) & 7) * 1024);
  const unsigned lds0 = (unsigned)(uintptr_t)(LAS char*)smem;
  unsigned qa = lds0 + L_QI + lane * 16 + ((w + 2) & 7) * 1024;
  f32x16 cc = splat16(-bound);
  f32x32 sab;
  int i = 0;
  unsigned so = ((w + 1) & 7) * PREG + 6 * PSLOT, qo = ((w + 2) & 7) * 1024; float t1_; unsigned t2_;
#define RUN_OUT "=&{v[240:255]}"(o), "+{v[192:207]}"(cc), [q] "+v"(qf), [qa] "+v"(qa), [i] "+s"(i), [so] "+s"(so), [qo] "+s"(qo), [t1] "=&v"(t1_), [t2] "=&v"(t2_)
#define RUN_IN [z] "v"(zf), [idx] "v"(idx), [sh] "v"(shift), [lo] "s"(0xffffffffull), [l16] "v"(lds0 + lane * 16), [l4] "v"(lds0 + lane * 4), \
               [qb] "v"(lds0 + L_QI + lane * 16)
  STAMP(3, 0);
  asm volatile("s_cmp_eq_u32 %[fast], 0\n\ts_cbranch_scc1 .Lfb_slow0\n\t" PROLOG0_ASM PASS0_ASM POST_ASM
               "s_branch .Lfb_out%=\n.Lfb_slow0:\n\ts_waitcnt vmcnt(0)\n\t" SWAPS_ASM "\n.Lfb_out%=:"
               : "=&{v[208:239]}"(sab), RUN_OUT, KV_IO : RUN_IN, [fast] "s"(fasti) : "scc", "memory");

  if (w == 2) {
        int lane = tid & 63; asm volatile("" : "+v"(lane));
        unsigned m1 = 0, m2 = 0;
        SPIN {
          bool ok = true; m1 = 0; m2 = 0;
#pragma unroll
          for (int j = 0; j < 4; ++j) {
            const u32x4 cd = ld128(rs, WS_CAND + (unsigned)(g8 * 256 + j * 64 + lane) * 16);
            ok = ok && cd[1] == tag && cd[3] == tag;
            m2 = max(max(m2, cd[2]), min(m1, cd[0])); m1 = max(m1, cd[0]);
          }
          if (__all(ok)) break;
          __builtin_amdgcn_s_sleep(1);
        }
        CBAR();
        const unsigned k1 = __builtin_amdgcn_readfirstlane(wave_maxu(m1));
        const unsigned k2 = __builtin_amdgcn_readfirstlane(wave_maxu(m1 == k1 ? m2 : m1));
        if (lane == 0) *(u32x2*)(CTL + C_K1) = u32x2{k1, k2};
        if (b < NU) {
          const int h = lane >> 5, l31 = lane & 31;
          const u16* xr = x + key_tok(h ? k2 : k1) * D_ + l31 * 8;
        {
          const u32x4 xs0 = *(const u32x4*)xr, xs1 = *(const u32x4*)(xr + 256);
          float mean, var;
          stats32(xs0, xs1, mean, var);
          const float mb = rbf(mean), inv = __builtin_amdgcn_rcpf(ln_sd(var));
          u32x4 o0;
#pragma unroll
          for (int j = 0; j < 4; ++j) o0[j] = ln_pair((u & 4) ? xs1[j] : xs0[j], mb, inv);
          *(u32x4*)(smem + L_XS + h * 1024 + l31 * 16) = o0;
          asm volatile("s_waitcnt lgkmcnt(0)" ::: "memory");
          if (lane == 0) *(volatile LAS unsigned*)(CTL + C_SEL) = 1u;
        }
        }
  }
  if (w == 0 && b < NU) {
    int ln = tid; asm volatile("" : "+v"(ln));
    const unsigned go = (ln >> 3) * (F_ * 2) + (((ln & 7) ^ (((ln >> 4) & 1) << 2)) << 4);
#pragma unroll
    for (int j = 0; j < 16; ++j) glds16((j < 8 ? wg : wu) + ((u & 7) * 64 + (j & 7) * 8) * F_ + (u >> 3) * 64, go, smem + L_GU + j * 1024);
  }
  if (w == 1 && b < NU) {
#pragma unroll
    for (int f = 0; f < 8; ++f) glds16(wd + (u * 8 + f) * D_, lane * 16, smem + (f < 6 ? L_DN + f * 1088 : L_DN2 + (f - 6) * 1088));
  }
  asm volatile("" : "=&{v[64:95]}"(wov));
  const int iw = (0x33222453 >> (4 * w)) & 15;
  unsigned ro = row_off;
  unsigned long long wb2_;

#pragma nounroll
  while (i < 8) {
    if (b < NU && ((w == 0 && i == 2) || (w == 1 && i == 4))) {
      int lane = tid & 63, u = (b & 7) * 28 + (b >> 3);
      asm volatile("" : "+v"(lane), "+s"(u));
      const int h = lane >> 5, l31 = lane & 31, blk = (lane >> 4) & 1, q4 = (lane & 15) >> 2, p4 = lane & 3;
      if (i == 2) {
        DRAIN();
        lds_wait_ge(CTL + C_SEL, 1u);
        const char* xb = opq(smem + L_XS + (lane & 1) * 1024 + (u & 3) * 128 + h * 16);
        bf16x8 fx[4];
#pragma unroll
        for (int ks = 0; ks < 4; ++ks) fx[ks] = *(const bf16x8*)(xb + ks * 32);
#pragma unroll
        for (int nt = 0; nt < 2; ++nt) {
          const char* wb = opq(smem + L_GU + (8 * h + q4) * 128 + (((4 * nt + 2 * blk + (p4 >> 1)) ^ (((q4 >> 1) & 1) << 2)) << 4) + 8 * (p4 & 1));
#pragma unroll
          for (int mat = 0; mat < 2; ++mat) {
            f32x16 z = z16;
#pragma unroll
            for (int ks = 0; ks < 4; ++ks) z = QK(fx[ks], tr_frag(wb + mat * 8192 + ks * 2048, wb + mat * 8192 + ks * 2048 + 512), z);
            if (lane < 32) st128(rs, WS_P1 + (unsigned)u * 2048 + (mat * 64 + nt * 32 + lane) * 16, u32x4{__float_as_uint(z[0]), tag, __float_as_uint(z[1]), tag});
            SB();
          }
        }
      } else {
        const unsigned po = WS_P1 + (unsigned)((u & ~7) + (lane >> 3)) * 2048 + ((u & 7) * 8 + (lane & 7)) * 16;
        u32x4 pg, pu;
        SPIN {
          pg = ld128(rs, po); pu = ld128(rs, po + 1024);
          if (__all(pg[1] == tag && pg[3] == tag && pu[1] == tag && pu[3] == tag)) break;
          __builtin_amdgcn_s_sleep(2);
        }
        DRAIN();
        float t[4] = {__uint_as_float(pg[0]), __uint_as_float(pg[2]), __uint_as_float(pu[0]), __uint_as_float(pu[2])};
#pragma unroll
        for (int j = 0; j < 4; ++j) { t[j] += dpp_f<0x128>(t[j]); t[j] = add32(add16(t[j])); }
        const unsigned hp = pack_bf(rbf(silu_f(rbf(t[0]))) * rbf(t[2]), rbf(silu_f(rbf(t[1]))) * rbf(t[3]));
        if (lane < 8) { *(u16*)(smem + L_HS + lane * 2) = (u16)hp; *(u16*)(smem + L_HS + 16 + lane * 2) = (u16)(hp >> 16); }
        u32x4 ha = *(const u32x4*)(smem + L_HS + (lane & 1) * 16);
        if (lane >= 2) ha = u32x4{0u, 0u, 0u, 0u};
        const bf16x8 A = __builtin_bit_cast(bf16x8, ha);
        const int co = (2 * blk + (p4 >> 1)) * 16 + 8 * (p4 & 1);
        const char* db0 = opq(smem + L_DN + q4 * 1088 + co);
        const char* db1 = opq(smem + (q4 < 2 ? L_DN + (4 + q4) * 1088 : L_DN2 + (q4 - 2) * 1088) + co);
#pragma unroll 2
        for (int ct = 0; ct < 16; ++ct) {
          const bf16x8 bw = tr_frag(db0 + ct * 64, db1 + ct * 64);
          const f32x16 z = QK(A, bw, z16);
          auto sw = __builtin_amdgcn_permlane32_swap(__float_as_uint(z[0]), __float_as_uint(z[1]), false, false);
          st32(rs, WS_YP + (unsigned)(h * 64 + ct * 4 + (l31 >> 3)) * (NU * 32) + u * 32 + (l31 & 7) * 4, sw[0]);
        }
        DRAIN();
        if (lane < 8) st32(rs, WS_YF + lane * 1024 + u * 4, tag);
      }
    }
  if (w < 4 && i == 7 && b < NU) __builtin_amdgcn_s_setprio(0);
    STAMP(3, i);
    int stop = 8;
    if (b < NU) {
      if (w < 4 && i < 7) stop = 7;
      if (w == 0 && i < 2) stop = 2;
      if (w == 1 && i < 4) stop = 4;
    }
    if (!fast) {
      stop = i + 1;
      float m = -3.0e38f;
      asm volatile(ROWMAX_ASM : [m] "+v"(m), "=&{v[208:239]}"(sab) : [q] "v"(qf), KV_IN);
      shift = max32(m);
      cc = splat16(-shift);
      asm volatile(PROLOG_ASM : "=&{v[208:239]}"(sab), "+{v[192:207]}"(cc) : [q] "v"(qf), KV_IN);
    }
    asm volatile(".Lfb_loop%=:\n\t" PASSN_ASM POST_ASM WOLOAD_ASM "\n.Lfb_skip%=:\n\ts_cmp_lt_u32 %[i], %[stop]\n\ts_cbranch_scc1 .Lfb_loop%=\n.Lfb_out%=:"
                 : "+{v[208:239]}"(sab), RUN_OUT, "+{v[64:95]}"(wov), [ro] "+v"(ro), [wb2] "=&s"(wb2_)
                 : RUN_IN, [stop] "s"(stop), [iw] "s"(iw), [wb] "s"(wo + (size_t)(w * 64) * D_ + hg * 64), KV_IN : "scc", "memory");
  }
  {
  int tid = threadIdx.x; asm volatile("" : "+v"(tid));
  const int lane = tid & 63, rr = lane >> 3, c = lane & 7, h = lane >> 5, l31 = lane & 31, blk = (lane >> 4) & 1, q4 = (lane & 15) >> 2, p4 = lane & 3;
  if (w < 4 && b >= NU) {
    SPIN {
      const u32x4 f = ld128(rs, WS_YF + g8 * 1024 + min(lane, 55) * 16);
      if (__all(f[0] == tag && f[1] == tag && f[2] == tag && f[3] == tag)) break;
      __builtin_amdgcn_s_sleep(8);
    }
    CBAR();
    const unsigned rw = (unsigned)((b - NU) * 4 + w);
    const unsigned yo = WS_YP + rw * (NU * 32) + lane * 16;
    f32x4 y = {};
#pragma unroll
    for (int j = 0; j < 7; ++j) y += __builtin_bit_cast(f32x4, ld128(rs, yo + j * 1024));
#pragma unroll
    for (int j = 0; j < 4; ++j) { float t = y[j]; t += dpp_f<0x4E>(t); t += dpp_f<0x124>(t); t += dpp_f<0x128>(t); y[j] = add32(add16(t)); }
    if (lane < 2) {
      st128(rs, WS_YG + rw * 64 + lane * 32, u32x4{__float_as_uint(y[0]), tag, __float_as_uint(y[1]), tag});
      st128(rs, WS_YG + rw * 64 + lane * 32 + 16, u32x4{__float_as_uint(y[2]), tag, __float_as_uint(y[3]), tag});
    }
  }
  __builtin_amdgcn_s_setprio(0);
  DRAIN();
  const u32x4 xe = *(const u32x4*)(x + (size_t)(tb * 64 + (tid >> 3)) * D_ + hg * 64 + c * 8);
  STAMP(1, 2);
  RAW_BARRIER();
  STAMP(1, 3);
  const unsigned fr = (unsigned)((tb * 2 + (w >> 2)) * 32 + hg * 4 + (w & 3));
  {
    float a[8], pl[7], pm[7];
    auto sw = __builtin_amdgcn_permlane32_swap(__float_as_uint(o[8]), __float_as_uint(o[8]), false, false);
    float l = __uint_as_float(sw[0]);
    float mx = shift;
#pragma unroll
    for (int sidx = 0; sidx < 7; ++sidx) {
      const char* sl = smem + w * PREG + sidx * PSLOT + 2048 + l31 * 4;
      pl[sidx] = *(const float*)sl; pm[sidx] = *(const float*)(sl + 128);
      mx = fmaxf(mx, pm[sidx]);
    }
    const float f0 = __builtin_amdgcn_exp2f(shift - mx);
    l *= f0;
#pragma unroll
    for (int r = 0; r < 8; ++r) a[r] = o[r] * f0;
#pragma unroll
    for (int sidx = 0; sidx < 7; ++sidx) {
      const char* sl = smem + w * PREG + sidx * PSLOT;
      const f32x4 p0 = *(const f32x4*)(sl + lane * 16), p1 = *(const f32x4*)(sl + 1024 + lane * 16);
      const float f = __builtin_amdgcn_exp2f(pm[sidx] - mx);
#pragma unroll
      for (int r = 0; r < 4; ++r) { a[r] = fmaf(p0[r], f, a[r]); a[r + 4] = fmaf(p1[r], f, a[r + 4]); }
      l = fmaf(pl[sidx], f, l);
    }
    const float inv = __builtin_amdgcn_rcpf(l);
    const u32x4 of = {min(pack_bf(a[0] * inv, a[1] * inv), 0xfffffffeu), min(pack_bf(a[2] * inv, a[3] * inv), 0xfffffffeu),
                      min(pack_bf(a[4] * inv, a[5] * inv), 0xfffffffeu), min(pack_bf(a[6] * inv, a[7] * inv), 0xfffffffeu)};
    st128(rs, WS_O + fr * 1024 + lane * 16, of);
    *(u32x4*)(smem + L_QI + w * 1024 + lane * 16) = of;
  }
  asm volatile("s_waitcnt lgkmcnt(0)\n\tds_write_b128 %0, v[64:67]\n\tds_write_b128 %0, v[68:71] offset:1024\n\tds_write_b128 %0, v[72:75] offset:2048\n\t"
               "ds_write_b128 %0, v[76:79] offset:3072\n\tds_write_b128 %0, v[80:83] offset:4096\n\tds_write_b128 %0, v[84:87] offset:5120\n\t"
               "ds_write_b128 %0, v[88:91] offset:6144\n\tds_write_b128 %0, v[92:95] offset:7168"
               :: "v"((unsigned)(uintptr_t)(LAS char*)smem + w * PREG + rr * 128 + ((c ^ (((rr >> 1) & 1) << 2)) << 4)), "{v[64:95]}"(wov) : "memory");
  if (b == 0 && tid == 0) st32(rs, WS_EPOCH, tag);
  STAMP(1, 4);
  RAW_BARRIER();
  const int etok = tid >> 3, gtok = tb * 64 + etok;
  {
    const int tt = w >> 2, kq = w & 3;
    bf16x8 fa[2][8];
#pragma unroll
    for (int nt = 0; nt < 2; ++nt) {
      const char* wb = opq(smem + 2 * kq * PREG + (4 * h + q4) * 128 + (((4 * nt + 2 * blk + (p4 >> 1)) ^ (((q4 >> 1) & 1) << 2)) << 4) + 8 * (p4 & 1));
#pragma unroll
      for (int i = 0; i < 8; ++i) fa[nt][i] = tr_frag(wb + (i >> 2) * PREG + (i & 3) * 2048, wb + (i >> 2) * PREG + (i & 3) * 2048 + 1024);
    }
    SB();
    const unsigned fo = WS_O + (unsigned)((tb * 2 + tt) * 32 + kq * 8) * 1024 + lane * 16;
    u32x4 fb[8];
    if (hg != 2 * kq) {
#pragma unroll
      for (int i = 0; i < 4; ++i) fb[i] = ld128(rs, fo + i * 1024);
    } else {
#pragma unroll
      for (int i = 0; i < 4; ++i) fb[i] = *(const u32x4*)(smem + L_QI + (tt * 4 + i) * 1024 + lane * 16);
    }
    if (hg != 2 * kq + 1) {
#pragma unroll
      for (int i = 4; i < 8; ++i) fb[i] = ld128(rs, fo + i * 1024);
    } else {
#pragma unroll
      for (int i = 0; i < 4; ++i) fb[4 + i] = *(const u32x4*)(smem + L_QI + (tt * 4 + i) * 1024 + lane * 16);
    }
    SB();
    STAMP(2, 0);
    {
      const unsigned k1 = *(const unsigned*)(CTL + C_K1), k2 = *(const unsigned*)(CTL + C_K2);
      const int sel = (gtok == key_tok(k1)) ? 0 : ((gtok == key_tok(k2)) ? 1 : -1);
      if (__builtin_expect(__any(sel >= 0), 0)) {
        const f32x2 st = *(const f32x2*)(CTL + C_STAT + etok * 8);
        const float inv = __builtin_amdgcn_rcpf(st[1]);
        const float rsg = rbf(sigm_f(key_val(sel > 0 ? k2 : k1)));
        const unsigned go = WS_YG + (unsigned)((sel > 0 ? 512 : 0) + hg * 64 + c * 8) * 8;
        u32x4 gq[4];
        SPIN {
          bool ok = true;
#pragma unroll
          for (int j = 0; j < 4; ++j) { gq[j] = ld128(rs, go + j * 16); ok = ok && gq[j][1] == tag && gq[j][3] == tag; }
          if (__all(ok)) break;
          __builtin_amdgcn_s_sleep(1);
        }
#pragma unroll
        for (int j = 0; j < 4; ++j) {
          const unsigned xn = ln_pair(xe[j], st[0], inv);
          const float u0 = rbf(rbf(__uint_as_float(gq[j][0])) * rsg), u1 = rbf(rbf(__uint_as_float(gq[j][2])) * rsg);
          if (sel >= 0) gatep[j] = silu_pair(pack_bf(bf_lo(xn) + u0, bf_hi(xn) + u1));
        }
      }
    }
    SB();
    STAMP(2, 1);
    f32x16 acc0 = {}, acc1 = {};
#define FB_MAX(i) max(max(fb[i][0], fb[i][1]), max(fb[i][2], fb[i][3]))
#pragma unroll
    for (int hf = 0; hf < 2; ++hf) {
      if (__builtin_expect(__any(max(max(FB_MAX(4 * hf), FB_MAX(4 * hf + 1)), max(FB_MAX(4 * hf + 2), FB_MAX(4 * hf + 3))) == 0xffffffffu), 0)) {
        SPIN {
          unsigned bad = 0;
#pragma unroll
          for (int i = 4 * hf; i < 8; ++i) bad |= __any(FB_MAX(i) == 0xffffffffu) ? 1u << i : 0u;
          const unsigned need = bad & (15u << (4 * hf));
          if (!need) break;
          SPIN {
            const unsigned pv = ld32(rs, fo - lane * 16 + (lane & 7) * 1024 + 1020);
            if (!__any(pv == 0xffffffffu && ((need >> (lane & 7)) & 1u))) break;
            __builtin_amdgcn_s_sleep(1);
          }
#pragma unroll
          for (int i = 4 * hf; i < 8; ++i) if ((bad >> i) & 1u) fb[i] = ld128(rs, fo + i * 1024);
        }
      }
#pragma unroll
      for (int i = 4 * hf; i < 4 * hf + 4; ++i) {
        acc0 = QK(fa[0][i], __builtin_bit_cast(bf16x8, fb[i]), acc0);
        acc1 = QK(fa[1][i], __builtin_bit_cast(bf16x8, fb[i]), acc1);
      }
      SB();
    }
    STAMP(2, 2);
    char* pr = smem + w * PREG + 8192 + l31 * 256;
#pragma unroll
    for (int g = 0; g < 4; ++g) {
      *(f32x4*)(pr + (((2 * g + h) ^ (l31 & 7)) << 4)) = f32x4{acc0[4 * g], acc0[4 * g + 1], acc0[4 * g + 2], acc0[4 * g + 3]};
      *(f32x4*)(pr + 128 + (((2 * g + h) ^ (l31 & 7)) << 4)) = f32x4{acc1[4 * g], acc1[4 * g + 1], acc1[4 * g + 2], acc1[4 * g + 3]};
    }
  }
  RAW_BARRIER();
  {
    f32x4 s0v = {}, s1v = {};
#pragma unroll
    for (int kq = 0; kq < 4; ++kq) {
      const char* pr = smem + ((w >> 2) * 4 + kq) * PREG + 8192 + (etok & 31) * 256 + (c >> 2) * 128;
      s0v += *(const f32x4*)(pr + ((((2 * c) & 7) ^ rr) << 4)); s1v += *(const f32x4*)(pr + ((((2 * c + 1) & 7) ^ rr) << 4));
    }
    u32x4 ov;
#pragma unroll
    for (int j = 0; j < 4; ++j) {
      const unsigned sv = j < 2 ? pack_bf(s0v[2 * j], s0v[2 * j + 1]) : pack_bf(s1v[2 * j - 4], s1v[2 * j - 3]);
      const unsigned pr2 = pack_bf(bf_lo(sv) * bf_lo(gatep[j]), bf_hi(sv) * bf_hi(gatep[j]));
      ov[j] = pack_bf(bf_lo(xe[j]) + bf_lo(pr2), bf_hi(xe[j]) + bf_hi(pr2));
    }
    *(u32x4*)(out + (size_t)gtok * D_ + hg * 64 + c * 8) = ov;
  }
  }
  asm volatile(".Lfb_end:");
  STAMP(2, 3);
}

extern "C" void kernel_launch(void* const* d_in, const int* in_sizes, int n_in, void* d_out, int out_size, void* d_ws, size_t ws_size,
                              hipStream_t stream) {
  fused_block<<<256, 512, 0, stream>>>((const u16*)d_in[0], (const u16*)d_in[1], (const u16*)d_in[2], (const u16*)d_in[3], (const u16*)d_in[4],
                                       (const u16*)d_in[6], (const u16*)d_in[7], (const u16*)d_in[8], (const u16*)d_in[9], (char*)d_ws, (u16*)d_out);
}
```

```cpp
#include <hip/hip_runtime.h>
#include <hip/hip_bf16.h>
#include <stdint.h>

typedef __attribute__((ext_vector_type(2))) __bf16 bf16x2;
typedef __attribute__((ext_vector_type(8))) __bf16 bf16x8;
typedef __attribute__((ext_vector_type(4))) short s16x4;
typedef __attribute__((ext_vector_type(8))) short s16x8;
typedef __attribute__((ext_vector_type(2))) float f32x2;
typedef __attribute__((ext_vector_type(4))) float f32x4;
typedef __attribute__((ext_vector_type(16))) float f32x16;
typedef __attribute__((ext_vector_type(32))) float f32x32;
typedef __attribute__((ext_vector_type(2))) unsigned u32x2;
typedef __attribute__((ext_vector_type(4))) unsigned u32x4;
typedef unsigned short u16;
typedef __amdgpu_buffer_rsrc_t rsrc_t;
#define LAS __attribute__((address_space(3)))
#define DI __device__ __forceinline__

constexpr int S_ = 2048, D_ = 512, F_ = 1792;
constexpr int NU = 224;
constexpr int VHALF = 256;
constexpr int SC1 = 16;
constexpr unsigned SPIN_MAX = 1u << 18;

constexpr unsigned WS_EPOCH = 0;
constexpr unsigned WS_KVF = 1024;
constexpr unsigned WS_CAND = WS_KVF + 8 * 256 * 8;
constexpr unsigned WS_YF = WS_CAND + 8 * 512 * 8;
constexpr unsigned WS_YG = WS_YF + 8 * 1024;
constexpr unsigned WS_K = WS_YG + 1024 * 8;
constexpr unsigned WS_V = WS_K + S_ * 32;
constexpr unsigned WS_YP = WS_V + 256 * VHALF;
constexpr unsigned WS_P1 = WS_YP + NU * 2 * D_ * 4;
constexpr unsigned WS_O = WS_P1 + NU * 2048;
constexpr unsigned WS_END = WS_O + S_ * D_ * 2;

constexpr int L_WI = 0;
constexpr int L_XI = 65536;
constexpr int L_KI = 131072;
constexpr int PSLOT = 2304;
constexpr int PREG = 16384;
constexpr int L_XS = 7 * PREG;
constexpr int L_GU = 131072;
constexpr int L_QI = 147456;
constexpr int L_CTL = L_QI + 8192;
constexpr int L_DN = L_CTL + 1024;
constexpr int L_DN2 = 7 * PREG + PSLOT + 128;
constexpr int L_HS = 0;
constexpr int L_TOTAL = L_DN + 6 * 1088;
constexpr int C_CNT = 0;
constexpr int C_SEL = 4;
constexpr int C_K1 = 8, C_K2 = 12;
constexpr int C_QMAX = 16;
constexpr int C_R = 32;
constexpr int C_STAT = 64;

#define STAMP(kid, i)

DI float bf_lo(unsigned u) { return __uint_as_float(u << 16); }
DI float bf_hi(unsigned u) { return __uint_as_float(u & 0xffff0000u); }
DI unsigned pack_bf(float a, float b) { f32x2 f = {a, b}; return __builtin_bit_cast(unsigned, __builtin_convertvector(f, bf16x2)); }
DI float rbf(float a) { return (float)(__bf16)a; }
DI float dot2(unsigned a, unsigned b, float c) { return __builtin_amdgcn_fdot2_f32_bf16(__builtin_bit_cast(bf16x2, a), __builtin_bit_cast(bf16x2, b), c, false); }
DI void glds16(const void* sbase, unsigned voff, void* lds_wave_base) {
  unsigned keep; const unsigned dst = (unsigned)(uintptr_t)(LAS char*)lds_wave_base;
  asm volatile("s_mov_b32 %0, m0\n\ts_mov_b32 m0, %2\n\ts_nop 0\n\tglobal_load_lds_dwordx4 %1, %3\n\ts_mov_b32 m0, %0"
               : "=&s"(keep) : "v"(voff), "s"(dst), "s"(sbase) : "memory");
}
#define GLD128(dst, voff, sbase) asm volatile("global_load_dwordx4 %0, %1, %2" : "=v"(dst) : "v"(voff), "s"(sbase) : "memory")
DI const char* opq(const char* p) { unsigned a = (unsigned)(uintptr_t)(LAS char*)p; asm volatile("" : "+v"(a)); return (const char*)(LAS char*)(uintptr_t)a; }
DI s16x4 tr_read(const char* p) { return __builtin_amdgcn_ds_read_tr16_b64_v4i16((LAS s16x4*)p); }
DI bf16x8 tr_frag(const char* p0, const char* p1) {
  s16x4 a = tr_read(p0), b = tr_read(p1);
  s16x8 v = __builtin_shufflevector(a, b, 0, 1, 2, 3, 4, 5, 6, 7);
  return __builtin_bit_cast(bf16x8, v);
}
template <int CTRL> DI float dpp_f(float v) { return __int_as_float(__builtin_amdgcn_update_dpp(0, __float_as_int(v), CTRL, 0xf, 0xf, true)); }
template <int CTRL> DI unsigned dpp_u(unsigned v) { return (unsigned)__builtin_amdgcn_update_dpp(0, (int)v, CTRL, 0xf, 0xf, true); }
DI float sum8(float v) {
  v += dpp_f<0xB1>(v); v += dpp_f<0x4E>(v); v += dpp_f<0x141>(v);
  return v;
}
DI float add16(float v) { auto r = __builtin_amdgcn_permlane16_swap(__float_as_uint(v), __float_as_uint(v), false, false); return __uint_as_float(r[0]) + __uint_as_float(r[1]); }
DI float add32(float v) { auto r = __builtin_amdgcn_permlane32_swap(__float_as_uint(v), __float_as_uint(v), false, false); return __uint_as_float(r[0]) + __uint_as_float(r[1]); }
DI float max16(float v) { auto r = __builtin_amdgcn_permlane16_swap(__float_as_uint(v), __float_as_uint(v), false, false); return fmaxf(__uint_as_float(r[0]), __uint_as_float(r[1])); }
DI float max32(float v) { auto r = __builtin_amdgcn_permlane32_swap(__float_as_uint(v), __float_as_uint(v), false, false); return fmaxf(__uint_as_float(r[0]), __uint_as_float(r[1])); }
DI float sum32(float v) { v = sum8(v); v += dpp_f<0x140>(v); return add16(v); }
DI float wave_sum(float v) { return add32(sum32(v)); }
DI float wave_maxf(float v) {
  v = fmaxf(v, dpp_f<0xB1>(v)); v = fmaxf(v, dpp_f<0x4E>(v)); v = fmaxf(v, dpp_f<0x141>(v)); v = fmaxf(v, dpp_f<0x140>(v));
  return max32(max16(v));
}
DI unsigned wave_maxu(unsigned v) {
  v = max(v, dpp_u<0xB1>(v)); v = max(v, dpp_u<0x4E>(v)); v = max(v, dpp_u<0x141>(v)); v = max(v, dpp_u<0x140>(v));
  auto r = __builtin_amdgcn_permlane16_swap(v, v, false, false); v = max(r[0], r[1]);
  auto q = __builtin_amdgcn_permlane32_swap(v, v, false, false); return max(q[0], q[1]);
}
DI float wave_maxpos(float v) { return __uint_as_float(wave_maxu(__float_as_uint(v))); }
DI float silu_f(float v) { return v * __builtin_amdgcn_rcpf(1.0f + __builtin_amdgcn_exp2f(-1.4426950408889634f * v)); }
DI float sigm_f(float v) { return __builtin_amdgcn_rcpf(1.0f + __builtin_amdgcn_exp2f(-1.4426950408889634f * v)); }
DI float ln_sd(float var) { return rbf(__builtin_amdgcn_sqrtf(rbf(rbf(var) + 1e-5f))); }
DI unsigned ln_pair(unsigned xu, float mb, float inv) {
  const unsigned d = pack_bf(bf_lo(xu) - mb, bf_hi(xu) - mb);
  return pack_bf(bf_lo(d) * inv, bf_hi(d) * inv);
}
DI unsigned silu_pair(unsigned v) { return pack_bf(silu_f(bf_lo(v)), silu_f(bf_hi(v))); }

DI void stats32(const u32x4& a, const u32x4& b2, float& mean, float& var) {
  float s = 0.f, q = 0.f;
#pragma unroll
  for (int j = 0; j < 4; ++j) { s = dot2(a[j], 0x3f803f80u, s); s = dot2(b2[j], 0x3f803f80u, s); q = dot2(a[j], a[j], q); q = dot2(b2[j], b2[j], q); }
  mean = sum32(s) * (1.0f / 512.0f);
  var = fmaf(-mean, mean, sum32(q) * (1.0f / 512.0f));
  if (__builtin_expect(__any(!(var >= mean * mean * 0.015625f)), 0)) {
    float v = 0.f;
#pragma unroll
    for (int j = 0; j < 4; ++j) {
      const float a0 = bf_lo(a[j]) - mean, a1 = bf_hi(a[j]) - mean, a2 = bf_lo(b2[j]) - mean, a3 = bf_hi(b2[j]) - mean;
      v = fmaf(a0, a0, v); v = fmaf(a1, a1, v); v = fmaf(a2, a2, v); v = fmaf(a3, a3, v);
    }
    var = sum32(v) * (1.0f / 512.0f);
  }
}

DI unsigned make_key(float r_bf, int token) {
  unsigned b = __float_as_uint(r_bf) >> 16;
  b = (b & 0x8000u) ? (~b & 0xffffu) : (b | 0x8000u);
  return (b << 16) | (unsigned)(2047 - token);
}
DI float key_val(unsigned key) {
  unsigned b = key >> 16;
  b = (b & 0x8000u) ? (b & 0x7fffu) : (~b & 0xffffu);
  return __uint_as_float(b << 16);
}
DI int key_tok(unsigned key) { return 2047 - (int)(key & 0x7ffu); }

DI u32x4 ld128(rsrc_t rs, unsigned off) { return __builtin_amdgcn_raw_buffer_load_b128(rs, (int)off, 0, SC1); }
DI u32x2 ld64(rsrc_t rs, unsigned off) { return __builtin_amdgcn_raw_buffer_load_b64(rs, (int)off, 0, SC1); }
DI unsigned ld32(rsrc_t rs, unsigned off) { return __builtin_amdgcn_raw_buffer_load_b32(rs, (int)off, 0, SC1); }
DI void st128(rsrc_t rs, unsigned off, u32x4 v) { __builtin_amdgcn_raw_buffer_store_b128(v, rs, (int)off, 0, SC1); }
DI void st64(rsrc_t rs, unsigned off, u32x2 v) { __builtin_amdgcn_raw_buffer_store_b64(v, rs, (int)off, 0, SC1); }
DI void st32(rsrc_t rs, unsigned off, unsigned v) { __builtin_amdgcn_raw_buffer_store_b32(v, rs, (int)off, 0, SC1); }
#define DRAIN() asm volatile("s_waitcnt vmcnt(0)" ::: "memory")
#define VMWAIT(n) asm volatile("s_waitcnt vmcnt(" #n ")" ::: "memory")
#define CBAR() asm volatile("" ::: "memory")
#define RAW_BARRIER() do { asm volatile("s_waitcnt lgkmcnt(0)" ::: "memory"); __builtin_amdgcn_s_barrier(); asm volatile("" ::: "memory"); } while (0)
#define SPIN _Pragma("nounroll") for (unsigned s_ = 0; s_ < SPIN_MAX; ++s_)
DI unsigned lds_ld(const char* p) { return __hip_atomic_load((LAS unsigned*)p, __ATOMIC_RELAXED, __HIP_MEMORY_SCOPE_WORKGROUP); }
DI void lds_wait_ge(const char* p, unsigned want) {
  SPIN {
    if ((unsigned)__builtin_amdgcn_readfirstlane(lds_ld(p)) >= want) break;
    __builtin_amdgcn_s_sleep(1);
  }
  CBAR();
}
#define QK(KF, QF, C) __builtin_amdgcn_mfma_f32_32x32x16_bf16((KF), (QF), (C), 0, 0, 0)
DI f32x16 splat16(float v) { f32x16 r;
#pragma unroll
  for (int i = 0; i < 16; ++i) r[i] = v;
  return r; }
#define SB() __builtin_amdgcn_sched_barrier(0)
#define SGB(mask, n) __builtin_amdgcn_sched_group_barrier(mask, n, 0)

#define VCONST_ASM \
  "v_mov_b32 v132, %[cst]\n\t" "v_mov_b32 v133, %[cst]\n\t" "v_mov_b32 v134, %[cst]\n\t" "v_mov_b32 v135, %[cst]\n\t" "v_mov_b32 v140, %[cst]\n\t" \
  "v_mov_b32 v141, %[cst]\n\t" "v_mov_b32 v142, %[cst]\n\t" "v_mov_b32 v143, %[cst]\n\t" "v_mov_b32 v148, %[cst]\n\t" "v_mov_b32 v149, %[cst]\n\t" \
  "v_mov_b32 v150, %[cst]\n\t" "v_mov_b32 v151, %[cst]\n\t" "v_mov_b32 v156, %[cst]\n\t" "v_mov_b32 v157, %[cst]\n\t" "v_mov_b32 v158, %[cst]\n\t" \
  "v_mov_b32 v159, %[cst]\n\t" "v_mov_b32 v164, %[cst]\n\t" "v_mov_b32 v165, %[cst]\n\t" "v_mov_b32 v166, %[cst]\n\t" "v_mov_b32 v167, %[cst]\n\t" \
  "v_mov_b32 v172, %[cst]\n\t" "v_mov_b32 v173, %[cst]\n\t" "v_mov_b32 v174, %[cst]\n\t" "v_mov_b32 v175, %[cst]\n\t" "v_mov_b32 v180, %[cst]\n\t" \
  "v_mov_b32 v181, %[cst]\n\t" "v_mov_b32 v182, %[cst]\n\t" "v_mov_b32 v183, %[cst]\n\t" "v_mov_b32 v188, %[cst]\n\t" "v_mov_b32 v189, %[cst]\n\t" \
  "v_mov_b32 v190, %[cst]\n\t" "v_mov_b32 v191, %[cst]\n\t" ""
#define LOADS_ASM \
  "buffer_load_dwordx4 v[96:99], %[ka0], %[rs], 0 offen offset:0 sc1\n\t" "buffer_load_dwordx4 v[128:131], %[va0], %[rs], 0 offen offset:0 sc1\n\t" \
  "buffer_load_dwordx4 v[100:103], %[ka0], %[rs], 0 offen offset:1024 sc1\n\t" "buffer_load_dwordx4 v[136:139], %[va0], %[rs], 0 offen offset:1024 sc1\n\t" \
  "buffer_load_dwordx4 v[104:107], %[ka0], %[rs], 0 offen offset:2048 sc1\n\t" "buffer_load_dwordx4 v[144:147], %[va0], %[rs], 0 offen offset:2048 sc1\n\t" \
  "buffer_load_dwordx4 v[108:111], %[ka0], %[rs], 0 offen offset:3072 sc1\n\t" "buffer_load_dwordx4 v[152:155], %[va0], %[rs], 0 offen offset:3072 sc1\n\t" \
  "buffer_load_dwordx4 v[112:115], %[ka1], %[rs], 0 offen offset:0 sc1\n\t" "buffer_load_dwordx4 v[160:163], %[va1], %[rs], 0 offen offset:0 sc1\n\t" \
  "buffer_load_dwordx4 v[116:119], %[ka1], %[rs], 0 offen offset:1024 sc1\n\t" "buffer_load_dwordx4 v[168:171], %[va1], %[rs], 0 offen offset:1024 sc1\n\t" \
  "buffer_load_dwordx4 v[120:123], %[ka1], %[rs], 0 offen offset:2048 sc1\n\t" "buffer_load_dwordx4 v[176:179], %[va1], %[rs], 0 offen offset:2048 sc1\n\t" \
  "buffer_load_dwordx4 v[124:127], %[ka1], %[rs], 0 offen offset:3072 sc1\n\t" "buffer_load_dwordx4 v[184:187], %[va1], %[rs], 0 offen offset:3072 sc1\n\t" ""
#define SWAPS_ASM \
  "v_permlane16_swap_b32 v128, v132\n\t" "v_permlane16_swap_b32 v129, v133\n\t" "v_permlane16_swap_b32 v130, v134\n\t" "v_permlane16_swap_b32 v131, v135\n\t" \
  "v_permlane16_swap_b32 v136, v140\n\t" "v_permlane16_swap_b32 v137, v141\n\t" "v_permlane16_swap_b32 v138, v142\n\t" "v_permlane16_swap_b32 v139, v143\n\t" \
  "v_permlane16_swap_b32 v144, v148\n\t" "v_permlane16_swap_b32 v145, v149\n\t" "v_permlane16_swap_b32 v146, v150\n\t" "v_permlane16_swap_b32 v147, v151\n\t" \
  "v_permlane16_swap_b32 v152, v156\n\t" "v_permlane16_swap_b32 v153, v157\n\t" "v_permlane16_swap_b32 v154, v158\n\t" "v_permlane16_swap_b32 v155, v159\n\t" \
  "v_permlane16_swap_b32 v160, v164\n\t" "v_permlane16_swap_b32 v161, v165\n\t" "v_permlane16_swap_b32 v162, v166\n\t" "v_permlane16_swap_b32 v163, v167\n\t" \
  "v_permlane16_swap_b32 v168, v172\n\t" "v_permlane16_swap_b32 v169, v173\n\t" "v_permlane16_swap_b32 v170, v174\n\t" "v_permlane16_swap_b32 v171, v175\n\t" \
  "v_permlane16_swap_b32 v176, v180\n\t" "v_permlane16_swap_b32 v177, v181\n\t" "v_permlane16_swap_b32 v178, v182\n\t" "v_permlane16_swap_b32 v179, v183\n\t" \
  "v_permlane16_swap_b32 v184, v188\n\t" "v_permlane16_swap_b32 v185, v189\n\t" "v_permlane16_swap_b32 v186, v190\n\t" "v_permlane16_swap_b32 v187, v191\n\t" ""
#define PROLOG0_ASM \
  "s_waitcnt vmcnt(15)\n\t" "s_nop 1\n\t" "v_mfma_f32_32x32x16_bf16 v[208:223], v[96:99], %[q], v[192:207]\n\t" "s_waitcnt vmcnt(13)\n\t" \
  "v_mfma_f32_32x32x16_bf16 v[224:239], v[100:103], %[q], v[192:207]\n\t" "v_permlane16_swap_b32 v128, v132\n\t" "v_permlane16_swap_b32 v129, v133\n\t" \
  "v_permlane16_swap_b32 v130, v134\n\t" "v_permlane16_swap_b32 v131, v135\n\t" "s_nop 6\n\t" "v_exp_f32 v208, v208\n\t" "v_exp_f32 v209, v209\n\t" \
  "v_exp_f32 v210, v210\n\t" "v_exp_f32 v211, v211\n\t" "v_exp_f32 v212, v212\n\t" "v_exp_f32 v213, v213\n\t" "v_exp_f32 v214, v214\n\t" "v_exp_f32 v215, v215\n\t" \
  "v_exp_f32 v216, v216\n\t" "v_exp_f32 v217, v217\n\t" "v_exp_f32 v218, v218\n\t" "v_exp_f32 v219, v219\n\t" "v_exp_f32 v220, v220\n\t" ""
#define PROLOG_ASM \
  "s_nop 1\n\t" "v_mfma_f32_32x32x16_bf16 v[208:223], v[96:99], %[q], v[192:207]\n\t" "v_mfma_f32_32x32x16_bf16 v[224:239], v[100:103], %[q], v[192:207]\n\t" \
  "s_nop 10\n\t" "v_exp_f32 v208, v208\n\t" "v_exp_f32 v209, v209\n\t" "v_exp_f32 v210, v210\n\t" "v_exp_f32 v211, v211\n\t" "v_exp_f32 v212, v212\n\t" \
  "v_exp_f32 v213, v213\n\t" "v_exp_f32 v214, v214\n\t" "v_exp_f32 v215, v215\n\t" "v_exp_f32 v216, v216\n\t" "v_exp_f32 v217, v217\n\t" "v_exp_f32 v218, v218\n\t" \
  "v_exp_f32 v219, v219\n\t" "v_exp_f32 v220, v220\n\t" ""
#define PASS0_ASM \
  "v_mfma_f32_32x32x16_bf16 v[240:255], %[z], %[z], 0\n\t" "v_exp_f32 v221, v221\n\t" "v_exp_f32 v222, v222\n\t" "v_exp_f32 v223, v223\n\t" \
  "v_smfmac_f32_32x32x32_bf16 v[240:255], v[128:131], v[208:215], %[idx]\n\t" "v_exp_f32 v224, v224\n\t" "v_exp_f32 v225, v225\n\t" \
  "v_smfmac_f32_32x32x32_bf16 v[240:255], v[132:135], v[216:223], %[idx]\n\t" "v_exp_f32 v226, v226\n\t" "v_exp_f32 v227, v227\n\t" "v_exp_f32 v228, v228\n\t" \
  "s_waitcnt vmcnt(11)\n\t" "v_mfma_f32_32x32x16_bf16 v[208:223], v[104:107], %[q], v[192:207]\n\t" "v_permlane16_swap_b32 v136, v140\n\t" \
  "v_permlane16_swap_b32 v137, v141\n\t" "v_permlane16_swap_b32 v138, v142\n\t" "v_permlane16_swap_b32 v139, v143\n\t" "v_exp_f32 v229, v229\n\t" \
  "v_exp_f32 v230, v230\n\t" "v_exp_f32 v231, v231\n\t" "v_exp_f32 v232, v232\n\t" "v_exp_f32 v233, v233\n\t" \
  "v_smfmac_f32_32x32x32_bf16 v[240:255], v[136:139], v[224:231], %[idx]\n\t" "v_exp_f32 v234, v234\n\t" "v_exp_f32 v235, v235\n\t" "v_exp_f32 v236, v236\n\t" \
  "v_exp_f32 v237, v237\n\t" "v_exp_f32 v238, v238\n\t" "v_exp_f32 v239, v239\n\t" "v_exp_f32 v208, v208\n\t" "v_exp_f32 v209, v209\n\t" \
  "v_smfmac_f32_32x32x32_bf16 v[240:255], v[140:143], v[232:239], %[idx]\n\t" "v_exp_f32 v210, v210\n\t" "v_exp_f32 v211, v211\n\t" "v_exp_f32 v212, v212\n\t" \
  "s_waitcnt vmcnt(9)\n\t" "v_mfma_f32_32x32x16_bf16 v[224:239], v[108:111], %[q], v[192:207]\n\t" "v_permlane16_swap_b32 v144, v148\n\t" \
  "v_permlane16_swap_b32 v145, v149\n\t" "v_permlane16_swap_b32 v146, v150\n\t" "v_permlane16_swap_b32 v147, v151\n\t" "v_exp_f32 v213, v213\n\t" \
  "v_exp_f32 v214, v214\n\t" "v_exp_f32 v215, v215\n\t" "v_exp_f32 v216, v216\n\t" "v_exp_f32 v217, v217\n\t" \
  "v_smfmac_f32_32x32x32_bf16 v[240:255], v[144:147], v[208:215], %[idx]\n\t" "v_exp_f32 v218, v218\n\t" "v_exp_f32 v219, v219\n\t" "v_exp_f32 v220, v220\n\t" \
  "v_exp_f32 v221, v221\n\t" "v_exp_f32 v222, v222\n\t" "v_exp_f32 v223, v223\n\t" "v_exp_f32 v224, v224\n\t" "v_exp_f32 v225, v225\n\t" \
  "v_smfmac_f32_32x32x32_bf16 v[240:255], v[148:151], v[216:223], %[idx]\n\t" "v_exp_f32 v226, v226\n\t" "v_exp_f32 v227, v227\n\t" "v_exp_f32 v228, v228\n\t" \
  "s_waitcnt vmcnt(7)\n\t" "v_mfma_f32_32x32x16_bf16 v[208:223], v[112:115], %[q], v[192:207]\n\t" "v_permlane16_swap_b32 v152, v156\n\t" \
  "v_permlane16_swap_b32 v153, v157\n\t" "v_permlane16_swap_b32 v154, v158\n\t" "v_permlane16_swap_b32 v155, v159\n\t" "v_exp_f32 v229, v229\n\t" \
  "v_exp_f32 v230, v230\n\t" "v_exp_f32 v231, v231\n\t" "v_exp_f32 v232, v232\n\t" "v_exp_f32 v233, v233\n\t" \
  "v_smfmac_f32_32x32x32_bf16 v[240:255], v[152:155], v[224:231], %[idx]\n\t" "v_exp_f32 v234, v234\n\t" "v_exp_f32 v235, v235\n\t" "v_exp_f32 v236, v236\n\t" \
  "v_exp_f32 v237, v237\n\t" "v_exp_f32 v238, v238\n\t" "v_exp_f32 v239, v239\n\t" "v_exp_f32 v208, v208\n\t" "v_exp_f32 v209, v209\n\t" \
  "v_smfmac_f32_32x32x32_bf16 v[240:255], v[156:159], v[232:239], %[idx]\n\t" "v_exp_f32 v210, v210\n\t" "v_exp_f32 v211, v211\n\t" "v_exp_f32 v212, v212\n\t" \
  "s_waitcnt vmcnt(5)\n\t" "v_mfma_f32_32x32x16_bf16 v[224:239], v[116:119], %[q], v[192:207]\n\t" "v_permlane16_swap_b32 v160, v164\n\t" \
  "v_permlane16_swap_b32 v161, v165\n\t" "v_permlane16_swap_b32 v162, v166\n\t" "v_permlane16_swap_b32 v163, v167\n\t" "v_exp_f32 v213, v213\n\t" \
  "v_exp_f32 v214, v214\n\t" "v_exp_f32 v215, v215\n\t" "v_exp_f32 v216, v216\n\t" "v_exp_f32 v217, v217\n\t" \
  "v_smfmac_f32_32x32x32_bf16 v[240:255], v[160:163], v[208:215], %[idx]\n\t" "v_exp_f32 v218, v218\n\t" "v_exp_f32 v219, v219\n\t" "v_exp_f32 v220, v220\n\t" \
  "v_exp_f32 v221, v221\n\t" "v_exp_f32 v222, v222\n\t" "v_exp_f32 v223, v223\n\t" "v_exp_f32 v224, v224\n\t" "v_exp_f32 v225, v225\n\t" \
  "v_smfmac_f32_32x32x32_bf16 v[240:255], v[164:167], v[216:223], %[idx]\n\t" "v_exp_f32 v226, v226\n\t" "v_exp_f32 v227, v227\n\t" "v_exp_f32 v228, v228\n\t" \
  "s_waitcnt vmcnt(3)\n\t" "v_mfma_f32_32x32x16_bf16 v[208:223], v[120:123], %[q], v[192:207]\n\t" "v_permlane16_swap_b32 v168, v172\n\t" \
  "v_permlane16_swap_b32 v169, v173\n\t" "v_permlane16_swap_b32 v170, v174\n\t" "v_permlane16_swap_b32 v171, v175\n\t" "v_exp_f32 v229, v229\n\t" \
  "v_exp_f32 v230, v230\n\t" "v_exp_f32 v231, v231\n\t" "v_exp_f32 v232, v232\n\t" "v_exp_f32 v233, v233\n\t" \
  "v_smfmac_f32_32x32x32_bf16 v[240:255], v[168:171], v[224:231], %[idx]\n\t" "v_exp_f32 v234, v234\n\t" "v_exp_f32 v235, v235\n\t" "v_exp_f32 v236, v236\n\t" \
  "v_exp_f32 v237, v237\n\t" "v_exp_f32 v238, v238\n\t" "v_exp_f32 v239, v239\n\t" "v_exp_f32 v208, v208\n\t" "v_exp_f32 v209, v209\n\t" \
  "v_smfmac_f32_32x32x32_bf16 v[240:255], v[172:175], v[232:239], %[idx]\n\t" "v_exp_f32 v210, v210\n\t" "v_exp_f32 v211, v211\n\t" "v_exp_f32 v212, v212\n\t" \
  "s_waitcnt vmcnt(1)\n\t" "v_mfma_f32_32x32x16_bf16 v[224:239], v[124:127], %[q], v[192:207]\n\t" "ds_read_b128 %[q], %[qa]\n\t" "v_permlane16_swap_b32 v176, v180\n\t" \
  "v_permlane16_swap_b32 v177, v181\n\t" "v_permlane16_swap_b32 v178, v182\n\t" "v_permlane16_swap_b32 v179, v183\n\t" "v_exp_f32 v213, v213\n\t" \
  "v_exp_f32 v214, v214\n\t" "v_exp_f32 v215, v215\n\t" "v_exp_f32 v216, v216\n\t" "v_exp_f32 v217, v217\n\t" \
  "v_smfmac_f32_32x32x32_bf16 v[240:255], v[176:179], v[208:215], %[idx]\n\t" "v_exp_f32 v218, v218\n\t" "v_exp_f32 v219, v219\n\t" "v_exp_f32 v220, v220\n\t" \
  "v_exp_f32 v221, v221\n\t" "v_exp_f32 v222, v222\n\t" "v_exp_f32 v223, v223\n\t" "v_exp_f32 v224, v224\n\t" "v_exp_f32 v225, v225\n\t" \
  "v_smfmac_f32_32x32x32_bf16 v[240:255], v[180:183], v[216:223], %[idx]\n\t" "v_exp_f32 v226, v226\n\t" "v_exp_f32 v227, v227\n\t" "v_exp_f32 v228, v228\n\t" \
  "s_waitcnt vmcnt(0) lgkmcnt(0)\n\t" "v_mfma_f32_32x32x16_bf16 v[208:223], v[96:99], %[q], v[192:207]\n\t" "v_permlane16_swap_b32 v184, v188\n\t" \
  "v_permlane16_swap_b32 v185, v189\n\t" "v_permlane16_swap_b32 v186, v190\n\t" "v_permlane16_swap_b32 v187, v191\n\t" "v_exp_f32 v229, v229\n\t" \
  "v_exp_f32 v230, v230\n\t" "v_exp_f32 v231, v231\n\t" "v_exp_f32 v232, v232\n\t" "v_exp_f32 v233, v233\n\t" \
  "v_smfmac_f32_32x32x32_bf16 v[240:255], v[184:187], v[224:231], %[idx]\n\t" "v_exp_f32 v234, v234\n\t" "v_exp_f32 v235, v235\n\t" "v_exp_f32 v236, v236\n\t" \
  "v_exp_f32 v237, v237\n\t" "v_exp_f32 v238, v238\n\t" "v_exp_f32 v239, v239\n\t" "v_exp_f32 v208, v208\n\t" "v_exp_f32 v209, v209\n\t" \
  "v_smfmac_f32_32x32x32_bf16 v[240:255], v[188:191], v[232:239], %[idx]\n\t" "v_exp_f32 v210, v210\n\t" "v_exp_f32 v211, v211\n\t" "v_exp_f32 v212, v212\n\t" \
  "v_mfma_f32_32x32x16_bf16 v[224:239], v[100:103], %[q], v[192:207]\n\t" "v_exp_f32 v213, v213\n\t" "v_exp_f32 v214, v214\n\t" "v_exp_f32 v215, v215\n\t" \
  "v_exp_f32 v216, v216\n\t" "v_exp_f32 v217, v217\n\t" "v_exp_f32 v218, v218\n\t" "v_exp_f32 v219, v219\n\t" "v_exp_f32 v220, v220\n\t" ""
#define PASSN_ASM \
  "v_mfma_f32_32x32x16_bf16 v[240:255], %[z], %[z], 0\n\t" "v_exp_f32 v221, v221\n\t" "v_exp_f32 v222, v222\n\t" "v_exp_f32 v223, v223\n\t" \
  "v_smfmac_f32_32x32x32_bf16 v[240:255], v[128:131], v[208:215], %[idx]\n\t" "v_exp_f32 v224, v224\n\t" "v_exp_f32 v225, v225\n\t" \
  "v_smfmac_f32_32x32x32_bf16 v[240:255], v[132:135], v[216:223], %[idx]\n\t" "v_exp_f32 v226, v226\n\t" "v_exp_f32 v227, v227\n\t" "v_exp_f32 v228, v228\n\t" \
  "v_mfma_f32_32x32x16_bf16 v[208:223], v[104:107], %[q], v[192:207]\n\t" "v_exp_f32 v229, v229\n\t" "v_exp_f32 v230, v230\n\t" "v_exp_f32 v231, v231\n\t" \
  "v_exp_f32 v232, v232\n\t" "v_exp_f32 v233, v233\n\t" "v_smfmac_f32_32x32x32_bf16 v[240:255], v[136:139], v[224:231], %[idx]\n\t" "v_exp_f32 v234, v234\n\t" \
  "v_exp_f32 v235, v235\n\t" "v_exp_f32 v236, v236\n\t" "v_exp_f32 v237, v237\n\t" "v_exp_f32 v238, v238\n\t" "v_exp_f32 v239, v239\n\t" "v_exp_f32 v208, v208\n\t" \
  "v_exp_f32 v209, v209\n\t" "v_smfmac_f32_32x32x32_bf16 v[240:255], v[140:143], v[232:239], %[idx]\n\t" "v_exp_f32 v210, v210\n\t" "v_exp_f32 v211, v211\n\t" \
  "v_exp_f32 v212, v212\n\t" "v_mfma_f32_32x32x16_bf16 v[224:239], v[108:111], %[q], v[192:207]\n\t" "v_exp_f32 v213, v213\n\t" "v_exp_f32 v214, v214\n\t" \
  "v_exp_f32 v215, v215\n\t" "v_exp_f32 v216, v216\n\t" "v_exp_f32 v217, v217\n\t" "v_smfmac_f32_32x32x32_bf16 v[240:255], v[144:147], v[208:215], %[idx]\n\t" \
  "v_exp_f32 v218, v218\n\t" "v_exp_f32 v219, v219\n\t" "v_exp_f32 v220, v220\n\t" "v_exp_f32 v221, v221\n\t" "v_exp_f32 v222, v222\n\t" "v_exp_f32 v223, v223\n\t" \
  "v_exp_f32 v224, v224\n\t" "v_exp_f32 v225, v225\n\t" "v_smfmac_f32_32x32x32_bf16 v[240:255], v[148:151], v[216:223], %[idx]\n\t" "v_exp_f32 v226, v226\n\t" \
  "v_exp_f32 v227, v227\n\t" "v_exp_f32 v228, v228\n\t" "v_mfma_f32_32x32x16_bf16 v[208:223], v[112:115], %[q], v[192:207]\n\t" "v_exp_f32 v229, v229\n\t" \
  "v_exp_f32 v230, v230\n\t" "v_exp_f32 v231, v231\n\t" "v_exp_f32 v232, v232\n\t" "v_exp_f32 v233, v233\n\t" \
  "v_smfmac_f32_32x32x32_bf16 v[240:255], v[152:155], v[224:231], %[idx]\n\t" "v_exp_f32 v234, v234\n\t" "v_exp_f32 v235, v235\n\t" "v_exp_f32 v236, v236\n\t" \
  "v_exp_f32 v237, v237\n\t" "v_exp_f32 v238, v238\n\t" "v_exp_f32 v239, v239\n\t" "v_exp_f32 v208, v208\n\t" "v_exp_f32 v209, v209\n\t" \
  "v_smfmac_f32_32x32x32_bf16 v[240:255], v[156:159], v[232:239], %[idx]\n\t" "v_exp_f32 v210, v210\n\t" "v_exp_f32 v211, v211\n\t" "v_exp_f32 v212, v212\n\t" \
  "v_mfma_f32_32x32x16_bf16 v[224:239], v[116:119], %[q], v[192:207]\n\t" "v_exp_f32 v213, v213\n\t" "v_exp_f32 v214, v214\n\t" "v_exp_f32 v215, v215\n\t" \
  "v_exp_f32 v216, v216\n\t" "v_exp_f32 v217, v217\n\t" "v_smfmac_f32_32x32x32_bf16 v[240:255], v[160:163], v[208:215], %[idx]\n\t" "v_exp_f32 v218, v218\n\t" \
  "v_exp_f32 v219, v219\n\t" "v_exp_f32 v220, v220\n\t" "v_exp_f32 v221, v221\n\t" "v_exp_f32 v222, v222\n\t" "v_exp_f32 v223, v223\n\t" "v_exp_f32 v224, v224\n\t" \
  "v_exp_f32 v225, v225\n\t" "v_smfmac_f32_32x32x32_bf16 v[240:255], v[164:167], v[216:223], %[idx]\n\t" "v_exp_f32 v226, v226\n\t" "v_exp_f32 v227, v227\n\t" \
  "v_exp_f32 v228, v228\n\t" "v_mfma_f32_32x32x16_bf16 v[208:223], v[120:123], %[q], v[192:207]\n\t" "v_exp_f32 v229, v229\n\t" "v_exp_f32 v230, v230\n\t" \
  "v_exp_f32 v231, v231\n\t" "v_exp_f32 v232, v232\n\t" "v_exp_f32 v233, v233\n\t" "v_smfmac_f32_32x32x32_bf16 v[240:255], v[168:171], v[224:231], %[idx]\n\t" \
  "v_exp_f32 v234, v234\n\t" "v_exp_f32 v235, v235\n\t" "v_exp_f32 v236, v236\n\t" "v_exp_f32 v237, v237\n\t" "v_exp_f32 v238, v238\n\t" "v_exp_f32 v239, v239\n\t" \
  "v_exp_f32 v208, v208\n\t" "v_exp_f32 v209, v209\n\t" "v_smfmac_f32_32x32x32_bf16 v[240:255], v[172:175], v[232:239], %[idx]\n\t" "v_exp_f32 v210, v210\n\t" \
  "v_exp_f32 v211, v211\n\t" "v_exp_f32 v212, v212\n\t" "v_mfma_f32_32x32x16_bf16 v[224:239], v[124:127], %[q], v[192:207]\n\t" "ds_read_b128 %[q], %[qa]\n\t" \
  "v_exp_f32 v213, v213\n\t" "v_exp_f32 v214, v214\n\t" "v_exp_f32 v215, v215\n\t" "v_exp_f32 v216, v216\n\t" "v_exp_f32 v217, v217\n\t" \
  "v_smfmac_f32_32x32x32_bf16 v[240:255], v[176:179], v[208:215], %[idx]\n\t" "v_exp_f32 v218, v218\n\t" "v_exp_f32 v219, v219\n\t" "v_exp_f32 v220, v220\n\t" \
  "v_exp_f32 v221, v221\n\t" "v_exp_f32 v222, v222\n\t" "v_exp_f32 v223, v223\n\t" "v_exp_f32 v224, v224\n\t" "v_exp_f32 v225, v225\n\t" \
  "v_smfmac_f32_32x32x32_bf16 v[240:255], v[180:183], v[216:223], %[idx]\n\t" "v_exp_f32 v226, v226\n\t" "v_exp_f32 v227, v227\n\t" "v_exp_f32 v228, v228\n\t" \
  "s_waitcnt lgkmcnt(0)\n\t" "v_mfma_f32_32x32x16_bf16 v[208:223], v[96:99], %[q], v[192:207]\n\t" "v_exp_f32 v229, v229\n\t" "v_exp_f32 v230, v230\n\t" \
  "v_exp_f32 v231, v231\n\t" "v_exp_f32 v232, v232\n\t" "v_exp_f32 v233, v233\n\t" "v_smfmac_f32_32x32x32_bf16 v[240:255], v[184:187], v[224:231], %[idx]\n\t" \
  "v_exp_f32 v234, v234\n\t" "v_exp_f32 v235, v235\n\t" "v_exp_f32 v236, v236\n\t" "v_exp_f32 v237, v237\n\t" "v_exp_f32 v238, v238\n\t" "v_exp_f32 v239, v239\n\t" \
  "v_exp_f32 v208, v208\n\t" "v_exp_f32 v209, v209\n\t" "v_smfmac_f32_32x32x32_bf16 v[240:255], v[188:191], v[232:239], %[idx]\n\t" "v_exp_f32 v210, v210\n\t" \
  "v_exp_f32 v211, v211\n\t" "v_exp_f32 v212, v212\n\t" "v_mfma_f32_32x32x16_bf16 v[224:239], v[100:103], %[q], v[192:207]\n\t" "v_exp_f32 v213, v213\n\t" \
  "v_exp_f32 v214, v214\n\t" "v_exp_f32 v215, v215\n\t" "v_exp_f32 v216, v216\n\t" "v_exp_f32 v217, v217\n\t" "v_exp_f32 v218, v218\n\t" "v_exp_f32 v219, v219\n\t" \
  "v_exp_f32 v220, v220\n\t" ""
#define ROWMAX_ASM \
  "s_nop 1\n\t" "v_mfma_f32_32x32x16_bf16 v[224:239], v[96:99], %[q], 0\n\t" "s_nop 11\n\t" "v_max3_f32 %[m], %[m], v224, v225\n\t" \
  "v_max3_f32 %[m], %[m], v226, v227\n\t" "v_max3_f32 %[m], %[m], v228, v229\n\t" "v_max3_f32 %[m], %[m], v230, v231\n\t" "v_max3_f32 %[m], %[m], v232, v233\n\t" \
  "v_max3_f32 %[m], %[m], v234, v235\n\t" "v_max3_f32 %[m], %[m], v236, v237\n\t" "v_max3_f32 %[m], %[m], v238, v239\n\t" \
  "v_mfma_f32_32x32x16_bf16 v[224:239], v[100:103], %[q], 0\n\t" "s_nop 11\n\t" "v_max3_f32 %[m], %[m], v224, v225\n\t" "v_max3_f32 %[m], %[m], v226, v227\n\t" \
  "v_max3_f32 %[m], %[m], v228, v229\n\t" "v_max3_f32 %[m], %[m], v230, v231\n\t" "v_max3_f32 %[m], %[m], v232, v233\n\t" "v_max3_f32 %[m], %[m], v234, v235\n\t" \
  "v_max3_f32 %[m], %[m], v236, v237\n\t" "v_max3_f32 %[m], %[m], v238, v239\n\t" "v_mfma_f32_32x32x16_bf16 v[224:239], v[104:107], %[q], 0\n\t" "s_nop 11\n\t" \
  "v_max3_f32 %[m], %[m], v224, v225\n\t" "v_max3_f32 %[m], %[m], v226, v227\n\t" "v_max3_f32 %[m], %[m], v228, v229\n\t" "v_max3_f32 %[m], %[m], v230, v231\n\t" \
  "v_max3_f32 %[m], %[m], v232, v233\n\t" "v_max3_f32 %[m], %[m], v234, v235\n\t" "v_max3_f32 %[m], %[m], v236, v237\n\t" "v_max3_f32 %[m], %[m], v238, v239\n\t" \
  "v_mfma_f32_32x32x16_bf16 v[224:239], v[108:111], %[q], 0\n\t" "s_nop 11\n\t" "v_max3_f32 %[m], %[m], v224, v225\n\t" "v_max3_f32 %[m], %[m], v226, v227\n\t" \
  "v_max3_f32 %[m], %[m], v228, v229\n\t" "v_max3_f32 %[m], %[m], v230, v231\n\t" "v_max3_f32 %[m], %[m], v232, v233\n\t" "v_max3_f32 %[m], %[m], v234, v235\n\t" \
  "v_max3_f32 %[m], %[m], v236, v237\n\t" "v_max3_f32 %[m], %[m], v238, v239\n\t" "v_mfma_f32_32x32x16_bf16 v[224:239], v[112:115], %[q], 0\n\t" "s_nop 11\n\t" \
  "v_max3_f32 %[m], %[m], v224, v225\n\t" "v_max3_f32 %[m], %[m], v226, v227\n\t" "v_max3_f32 %[m], %[m], v228, v229\n\t" "v_max3_f32 %[m], %[m], v230, v231\n\t" \
  "v_max3_f32 %[m], %[m], v232, v233\n\t" "v_max3_f32 %[m], %[m], v234, v235\n\t" "v_max3_f32 %[m], %[m], v236, v237\n\t" "v_max3_f32 %[m], %[m], v238, v239\n\t" \
  "v_mfma_f32_32x32x16_bf16 v[224:239], v[116:119], %[q], 0\n\t" "s_nop 11\n\t" "v_max3_f32 %[m], %[m], v224, v225\n\t" "v_max3_f32 %[m], %[m], v226, v227\n\t" \
  "v_max3_f32 %[m], %[m], v228, v229\n\t" "v_max3_f32 %[m], %[m], v230, v231\n\t" "v_max3_f32 %[m], %[m], v232, v233\n\t" "v_max3_f32 %[m], %[m], v234, v235\n\t" \
  "v_max3_f32 %[m], %[m], v236, v237\n\t" "v_max3_f32 %[m], %[m], v238, v239\n\t" "v_mfma_f32_32x32x16_bf16 v[224:239], v[120:123], %[q], 0\n\t" "s_nop 11\n\t" \
  "v_max3_f32 %[m], %[m], v224, v225\n\t" "v_max3_f32 %[m], %[m], v226, v227\n\t" "v_max3_f32 %[m], %[m], v228, v229\n\t" "v_max3_f32 %[m], %[m], v230, v231\n\t" \
  "v_max3_f32 %[m], %[m], v232, v233\n\t" "v_max3_f32 %[m], %[m], v234, v235\n\t" "v_max3_f32 %[m], %[m], v236, v237\n\t" "v_max3_f32 %[m], %[m], v238, v239\n\t" \
  "v_mfma_f32_32x32x16_bf16 v[224:239], v[124:127], %[q], 0\n\t" "s_nop 11\n\t" "v_max3_f32 %[m], %[m], v224, v225\n\t" "v_max3_f32 %[m], %[m], v226, v227\n\t" \
  "v_max3_f32 %[m], %[m], v228, v229\n\t" "v_max3_f32 %[m], %[m], v230, v231\n\t" "v_max3_f32 %[m], %[m], v232, v233\n\t" "v_max3_f32 %[m], %[m], v234, v235\n\t" \
  "v_max3_f32 %[m], %[m], v236, v237\n\t" "v_max3_f32 %[m], %[m], v238, v239\n\t" ""
#define POST_ASM \
  "s_add_u32 %[i], %[i], 1\n\t" "s_cmp_eq_u32 %[i], 8\n\t" "s_cbranch_scc1 .Lfb_out%=\n\t" "v_cndmask_b32 %[t1], %[sh], v248, %[lo]\n\t" \
  "v_add_u32 %[t2], %[so], %[l16]\n\t" "ds_write_b128 %[t2], v[240:243]\n\t" "ds_write_b128 %[t2], v[244:247] offset:1024\n\t" "v_add_u32 %[t2], %[so], %[l4]\n\t" \
  "ds_write_b32 %[t2], %[t1] offset:2048\n\t" "s_add_u32 %[so], %[so], 14080\n\t" "s_and_b32 %[so], %[so], 0x1ffff\n\t" "s_add_u32 %[qo], %[qo], 1024\n\t" \
  "s_and_b32 %[qo], %[qo], 0x1fff\n\t" "v_add_u32 %[qa], %[qo], %[qb]\n\t" ""
#define WOLOAD_ASM \
  "s_cmp_lg_u32 %[i], %[iw]\n\t" "s_cbranch_scc1 .Lfb_skip%=\n\t" "s_mov_b64 %[wb2], %[wb]\n\t" "global_load_dwordx4 v[64:67], %[ro], %[wb2]\n\t" \
  "v_add_u32 %[ro], 0x2000, %[ro]\n\t" "global_load_dwordx4 v[68:71], %[ro], %[wb2]\n\t" "v_add_u32 %[ro], 0x2000, %[ro]\n\t" \
  "global_load_dwordx4 v[72:75], %[ro], %[wb2]\n\t" "v_add_u32 %[ro], 0x2000, %[ro]\n\t" "global_load_dwordx4 v[76:79], %[ro], %[wb2]\n\t" \
  "v_add_u32 %[ro], 0x2000, %[ro]\n\t" "global_load_dwordx4 v[80:83], %[ro], %[wb2]\n\t" "v_add_u32 %[ro], 0x2000, %[ro]\n\t" \
  "global_load_dwordx4 v[84:87], %[ro], %[wb2]\n\t" "v_add_u32 %[ro], 0x2000, %[ro]\n\t" "global_load_dwordx4 v[88:91], %[ro], %[wb2]\n\t" \
  "v_add_u32 %[ro], 0x2000, %[ro]\n\t" "global_load_dwordx4 v[92:95], %[ro], %[wb2]\n\t" ""

#define KV_OUT "=&{v[96:127]}"(kv0), "=&{v[128:159]}"(kv1), "=&{v[160:191]}"(kv2)
#define KV_IO "+{v[96:127]}"(kv0), "+{v[128:159]}"(kv1), "+{v[160:191]}"(kv2)
#define KV_IN "{v[96:127]}"(kv0), "{v[128:159]}"(kv1), "{v[160:191]}"(kv2)
#define KV_ADDR [ka0] "v"(kb), [ka1] "v"(kb + 4096), [va0] "v"(vp), [va1] "v"(vp + 4096), [rs] "s"(rs)

__global__ __launch_bounds__(512, 2) void fused_block(const u16* __restrict__ x, const u16* __restrict__ wq, const u16* __restrict__ wk,
                                                      const u16* __restrict__ wv, const u16* __restrict__ wo, const u16* __restrict__ wr,
                                                      const u16* __restrict__ wg, const u16* __restrict__ wu, const u16* __restrict__ wd,
                                                      char* ws, u16* __restrict__ out) {
  __shared__ __attribute__((aligned(16))) char smem[L_TOTAL];
  unsigned pf1, pf2;
  {
    unsigned long long pc; unsigned t;
    asm volatile("s_getpc_b64 %[pc]\n.Lfb_begin:\n\tv_readfirstlane_b32 %[t], %[tid]\n\ts_cmp_lg_u32 %[t], 0\n\ts_cbranch_scc1 .Lfb_nopf\n\t"
                 "v_and_b32 %[a], 15, %[tid]\n\tv_lshlrev_b32 %[b], 7, %[a]\n\tglobal_load_dword %[b], %[b], %[pc]\n\t"
                 "s_lshr_b32 %[t], %[bx], 3\n\ts_mul_i32 %[t], %[t], 1152\n\ts_add_u32 %[t], %[t], 2048\n\t"
                 "v_min_u32 %[a], 8, %[a]\n\tv_lshl_add_u32 %[a], %[a], 7, %[t]\n\tv_min_u32 %[a], .Lfb_end-.Lfb_begin, %[a]\n\t"
                 "global_load_dword %[a], %[a], %[pc]\n.Lfb_nopf:"
                 : [pc] "=&s"(pc), [t] "=&s"(t), [a] "=&v"(pf1), [b] "=&v"(pf2) : [tid] "v"(threadIdx.x), [bx] "s"(blockIdx.x) : "scc", "memory");
  }
  char* const XI = smem + L_XI;
  char* const WI = smem + L_WI;
  char* const KI = smem + L_KI;
  char* const CTL = smem + L_CTL;
  const int tid = threadIdx.x, lane = tid & 63;
  const int w = __builtin_amdgcn_readfirstlane(tid >> 6);
  const int b = blockIdx.x, g8 = b & 7, tb = b & 31, hg = b >> 5;
  const int rr = lane >> 3, c = lane & 7;
  const int h = lane >> 5, l31 = lane & 31;
  const int isV = hg & 1, lr0 = (hg >> 1) * 16;
  const int pid = tb * 8 + hg;
  const int u = (b & 7) * 28 + (b >> 3);
  const rsrc_t rs = __builtin_amdgcn_make_buffer_rsrc(ws, 0, (int)WS_END, 0x00020000);
  STAMP(0, 0);

  unsigned tag;
  asm volatile("s_load_dword %0, %1, 0x0" : "=s"(tag) : "s"(ws) : "memory");
  const bool kvw = (w == (hg & 6));
  const bool ldw = (w == (hg | 1));
  u32x4 xo0, xo1, wr0, wr1, xe, xv[8];
  f32x32 wov;
  unsigned pf0 = 0;
  {
    const u16* p = x + (size_t)(tb * 64 + lr0 + 2 * w) * D_;
    GLD128(xo0, h * 1024 + l31 * 16, p);
    GLD128(xo1, h * 1024 + l31 * 16 + 512, p);
  }
#pragma unroll
  for (int i = 0; i < 2; ++i) glds16((isV ? wv : wk) + (w * 2 + i) * 512, lane * 16, KI + (w * 2 + i) * 1024);
  const unsigned row_off = rr * 1024 + c * 16;
  const unsigned wq_off = rr * 1024 + ((c ^ (((rr >> 1) & 1) << 2)) << 4);
  const u16* xrows = x + (size_t)(tb * 64 + w * 8) * D_;
  if (kvw) {
    __builtin_amdgcn_s_setprio(3);
  {
    unsigned long long ta = (unsigned long long)ws + (lane == 0 ? WS_YP + b * 5376 : (lane == 1 ? WS_K : WS_KVF));
    if (lane == 3) ta = (unsigned long long)(out + (size_t)tb * 64 * D_);
    asm volatile("global_load_dword %0, %1, off" : "=v"(pf0) : "v"(ta) : "memory");
  }
#pragma unroll
    for (int j = 0; j < 8; ++j)
      st128(rs, WS_O + (unsigned)((tb * 2 + (j >> 2)) * 32 + hg * 4 + (j & 3)) * 1024 + lane * 16, u32x4{0xffffffffu, 0xffffffffu, 0xffffffffu, 0xffffffffu});
  } else if (ldw) {
#pragma unroll
    for (int pc = 0; pc < 8; ++pc) glds16(wq + pc * 8 * D_ + hg * 64, wq_off, WI + pc * 1024);
  } else {
#pragma unroll
    for (int i = 0; i < 8; ++i) asm volatile("global_load_dwordx4 %0, %1, %2 offset:%3" : "=v"(xv[i]) : "v"(row_off), "s"(xrows), "i"(i * 128) : "memory");
  }
  VMWAIT(10);
  asm volatile("" : "+v"(xo0), "+v"(xo1), "+v"(pf1), "+v"(pf2));
  STAMP(0, 1);
  {
    float mean, var;
    stats32(xo0, xo1, mean, var);
    asm volatile("" : "+v"(xo0), "+v"(xo1));
    const float mb = rbf(mean), sd = ln_sd(var), inv = __builtin_amdgcn_rcpf(sd);
#pragma unroll
    for (int j = 0; j < 4; ++j) { xo0[j] = ln_pair(xo0[j], mb, inv); xo1[j] = ln_pair(xo1[j], mb, inv); }
    const int lrow = lr0 + 2 * w + h;
    char* xr = XI + lrow * 1024 + ((l31 ^ (lrow & 15)) << 4);
    *(u32x4*)xr = xo0; *(u32x4*)(xr + 512) = xo1;
    if (l31 == 0) *(f32x2*)(CTL + C_STAT + lrow * 8) = f32x2{mb, sd};
    if (tid < 2) *(unsigned*)(CTL + tid * 4) = 0u;
    if (kvw) VMWAIT(9); else VMWAIT(8);
    asm volatile("s_waitcnt lgkmcnt(0)" : "+s"(tag) :: "memory");
    __builtin_amdgcn_s_barrier();
    CBAR();
  }
  tag += 1u;
  STAMP(0, 2);
  if ((w >> 2) == isV) { GLD128(wr0, l31 * 16, wr); GLD128(wr1, l31 * 16 + 512, wr); }
  if (kvw) {
    const int g = lane >> 4, q = (lane & 15) >> 2, p = lane & 3;
    const char* xb = XI + (lr0 + (lane & 15)) * 1024;
    const char* wb = opq(KI + (8 * g + q) * 32 + p * 8);
    f32x4 acc = {}, act = {};
    bf16x8 fx[2][4], fw[2][4];
#define KV_LD(bf, k0) _Pragma("unroll") for (int i = 0; i < 4; ++i) { const int ks = (k0) + i; \
      fx[bf][i] = *(const bf16x8*)(xb + ((((4 * i + g) ^ (lane & 15)) << 4) + (ks >> 2) * 256)); \
      fw[bf][i] = tr_frag(wb + ks * 1024, wb + ks * 1024 + 128); }
    KV_LD(0, 0)
#pragma unroll
    for (int kb = 0; kb < 4; ++kb) {
      if (kb < 3) { KV_LD((kb + 1) & 1, 4 * kb + 4) }
      SB();
#pragma unroll
      for (int i = 0; i < 4; ++i) {
        acc = __builtin_amdgcn_mfma_f32_16x16x32_bf16(fw[kb & 1][i], fx[kb & 1][i], acc, 0, 0, 0);
        act = __builtin_amdgcn_mfma_f32_16x16x32_bf16(fx[kb & 1][i], fw[kb & 1][i], act, 0, 0, 0);
      }
      SB();
    }
    if (isV) acc = act;
    const u32x2 pk = {pack_bf(acc[0], acc[1]), pack_bf(acc[2], acc[3])};
    float n = 0.f;
    if (!isV) {
      st64(rs, WS_K + (unsigned)(tb * 64 + lr0 + (lane & 15)) * 32 + (((g & 1) << 1) | (g >> 1)) * 8, pk);
      n = wave_maxpos(add32(add16(acc[0] * acc[0] + acc[1] * acc[1] + acc[2] * acc[2] + acc[3] * acc[3])));
    } else {
      st64(rs, WS_V + (unsigned)(pid - 1 + (g >> 1)) * VHALF + (lane & 15) * 16 + (g & 1) * 8, pk);
    }
    DRAIN();
    if (lane < 8) st64(rs, WS_KVF + (unsigned)(lane * 256 + pid) * 8, u32x2{__float_as_uint(n), tag});
    __builtin_amdgcn_s_setprio(0);
    STAMP(0, 6);
  } else if (ldw) {
#pragma unroll 4
    for (int pc = 8; pc < 28; ++pc) glds16(wq + pc * 8 * D_ + hg * 64, wq_off, WI + pc * 1024);
  } else {
    const int w6 = w - (w > hg ? 2 : 0);
#pragma unroll
    for (int i = 0; i < 6; ++i) { const int pc = 28 + i * 6 + w6; glds16(wq + pc * 8 * D_ + hg * 64, wq_off, WI + pc * 1024); }
    asm volatile("s_waitcnt vmcnt(6)" : "+v"(xv[0]), "+v"(xv[1]), "+v"(xv[2]), "+v"(xv[3]), "+v"(xv[4]), "+v"(xv[5]), "+v"(xv[6]), "+v"(xv[7]) :: "memory");
    float s = 0.f, q = 0.f;
    unsigned ones = 0x3f803f80u; asm volatile("" : "+v"(ones));
#pragma unroll
    for (int i = 0; i < 8; ++i)
#pragma unroll
      for (int j = 0; j < 4; ++j) { s = dot2(xv[i][j], ones, s); q = dot2(xv[i][j], xv[i][j], q); }
    const float mean = sum8(s) * (1.0f / 512.0f);
    float var = fmaf(-mean, mean, sum8(q) * (1.0f / 512.0f));
    if (__builtin_expect(__any(!(var >= mean * mean * 0.015625f)), 0)) {
      float v = 0.f;
#pragma unroll
      for (int i = 0; i < 8; ++i)
#pragma unroll
        for (int j = 0; j < 4; ++j) { const float a = bf_lo(xv[i][j]) - mean, bb = bf_hi(xv[i][j]) - mean; v = fmaf(a, a, v); v = fmaf(bb, bb, v); }
      var = sum8(v) * (1.0f / 512.0f);
    }
    asm volatile("" : "+v"(xv[0]), "+v"(xv[1]), "+v"(xv[2]), "+v"(xv[3]), "+v"(xv[4]), "+v"(xv[5]), "+v"(xv[6]), "+v"(xv[7]));
    const float st_m = rbf(mean), st_s = ln_sd(var);
    const float inv = __builtin_amdgcn_rcpf(st_s), nb = -st_m * inv;
    const int lrow = w * 8 + rr;
#pragma unroll
    for (int i = 0; i < 8; ++i) {
      u32x4 o;
#pragma unroll
      for (int j = 0; j < 4; ++j) o[j] = pack_bf(fmaf(bf_lo(xv[i][j]), inv, nb), fmaf(bf_hi(xv[i][j]), inv, nb));
      *(u32x4*)(XI + lrow * 1024 + (((i * 8 + c) ^ (lrow & 15)) << 4)) = o;
    }
    if (c == 0) *(f32x2*)(CTL + C_STAT + lrow * 8) = f32x2{st_m, st_s};
    STAMP(0, 3);
  }
  VMWAIT(20);
  asm volatile("" : "+v"(pf0), "+v"(wr0), "+v"(wr1));
  if ((w >> 2) == isV) {
    float r = 0.f;
#pragma unroll
    for (int j = 0; j < 4; ++j) { r = dot2(xo0[j], wr0[j], r); r = dot2(xo1[j], wr1[j], r); }
    const unsigned rkey = make_key(rbf(sum32(r)), pid * 8 + 2 * (w & 3) + h);
    if (l31 == 0) *(unsigned*)(CTL + C_R + (2 * (w & 3) + h) * 4) = rkey;
  }
  if (!kvw) DRAIN();
  RAW_BARRIER();
  STAMP(0, 4);
  if (w < 4) __builtin_amdgcn_s_setprio(2); else __builtin_amdgcn_s_setprio(1);
  if (w == 4) {
    const unsigned key = lane < 8 ? *(const unsigned*)(CTL + C_R + lane * 4) : 0u;
    const unsigned k1 = wave_maxu(key), k2 = wave_maxu(key == k1 ? 0u : key);
    if (lane < 8) st128(rs, WS_CAND + (unsigned)(lane * 256 + pid) * 16, u32x4{k1, tag, k2, tag});
  }
  const int blk = (lane >> 4) & 1, q4 = (lane & 15) >> 2, p4 = lane & 3;
  if (w < 4) {
    const int nt = w & 1, tt = w >> 1;
    const int tok = tt * 32 + l31;
    const char* xb = XI + tok * 1024;
    const char* wb = WI + (8 * h + q4) * 128 + (((4 * nt + 2 * blk + (p4 >> 1)) ^ (((q4 >> 1) & 1) << 2)) << 4) + 8 * (p4 & 1);
    f32x16 acc = {};
    bf16x8 fa[2][4], fb[2][4];
#define Q_LD(bf, k0) _Pragma("unroll") for (int i = 0; i < 4; ++i) { const int ks = (k0) + i; \
      fa[bf][i] = tr_frag(wb + ks * 2048, wb + ks * 2048 + 512); \
      fb[bf][i] = *(const bf16x8*)(xb + ((((2 * (ks & 7) + h) ^ (tok & 15)) << 4) + (ks >> 3) * 256)); }
    Q_LD(0, 0)
    SB();
#pragma unroll
    for (int kb = 0; kb < 8; ++kb) {
      if (kb < 7) { Q_LD((kb + 1) & 1, 4 * kb + 4) }
#pragma unroll
      for (int i = 0; i < 4; ++i) acc = __builtin_amdgcn_mfma_f32_32x32x16_bf16(fa[kb & 1][i], fb[kb & 1][i], acc, 0, 0, 0);
#pragma unroll
      for (int i = 0; i < 4; ++i) { SGB(0x8, 1); SGB(0x100, 3); }
      SB();
    }
    const float cs = 0.25f * 1.4426950408889634f;
    float n0 = 0.f, n1 = 0.f;
#pragma unroll
    for (int r = 0; r < 8; ++r) { acc[r] *= cs; acc[r + 8] *= cs; n0 = fmaf(acc[r], acc[r], n0); n1 = fmaf(acc[r + 8], acc[r + 8], n1); }
    char* qi = smem + L_QI + (tt * 4 + nt * 2) * 1024 + lane * 16;
    *(u32x4*)qi = u32x4{pack_bf(acc[0], acc[1]), pack_bf(acc[2], acc[3]), pack_bf(acc[4], acc[5]), pack_bf(acc[6], acc[7])};
    *(u32x4*)(qi + 1024) = u32x4{pack_bf(acc[8], acc[9]), pack_bf(acc[10], acc[11]), pack_bf(acc[12], acc[13]), pack_bf(acc[14], acc[15])};
    const float nm = wave_maxpos(fmaxf(add32(n0), add32(n1)));
    if (lane == 0) {
      *(float*)(CTL + C_QMAX + w * 4) = nm;
      asm volatile("s_waitcnt lgkmcnt(0)\n\tds_add_u32 %0, %1" :: "v"((unsigned)(uintptr_t)(LAS char*)(CTL + C_CNT)), "v"(1u) : "memory");
    }
  }
  STAMP(0, 5);

  xe = *(const u32x4*)((const char*)(xrows + hg * 64) + row_off);
  f32x32 kv0, kv1, kv2;
  asm volatile(VCONST_ASM : KV_OUT : [cst] "v"((lane & 15) ? 0u : 0x3f803f80u));
  float kmax;
  {
    u32x2 f;
    SPIN {
      f = ld64(rs, WS_KVF + (unsigned)(g8 * 256 + w * 32 + l31) * 8);
      if (__all(f[1] == tag)) break;
      __builtin_amdgcn_s_sleep(1);
    }
    CBAR();
    kmax = __uint_as_float(f[0]);
  }
  STAMP(1, 0);
  {
    const unsigned kb = WS_K + (unsigned)(w * 256 + l31) * 32 + h * 16;
    const unsigned vp = WS_V + (unsigned)(w * 32 + 2 * blk + h) * VHALF + (lane & 15) * 16;
    asm volatile(LOADS_ASM : KV_IO : KV_ADDR : "memory");
  }
  kmax = wave_maxpos(kmax);
  u32x4 gatep;
  {
    const f32x2 st = *(const f32x2*)(CTL + C_STAT + (w * 8 + rr) * 8);
    const float inv = __builtin_amdgcn_rcpf(st[1]);
#pragma unroll
    for (int j = 0; j < 4; ++j) gatep[j] = silu_pair(ln_pair(xe[j], st[0], inv));
    asm volatile("" : "+v"(gatep));
  }
  lds_wait_ge(CTL + C_CNT, 4u);
  STAMP(1, 1);
  const f32x4 qn = *(const f32x4*)(CTL + C_QMAX);
  const float qmax = fmaxf(fmaxf(qn[0], qn[1]), fmaxf(qn[2], qn[3]));
  const float bound = __builtin_amdgcn_sqrtf(qmax * kmax) * 1.02f + 0.01f;
  int fasti = __builtin_amdgcn_readfirstlane(bound <= 48.0f ? 1 : 0);
  asm volatile("" : "+s"(fasti));
  const bool fast = fasti != 0;
  const int idx = (int)0xDDDDDDDDu;
  bf16x8 zf = {};
  asm volatile("" : "+v"(zf));
  const f32x16 z16 = {};
  const char* qbase = smem + L_QI + lane * 16;
  f32x16 o;
  float shift = bound;
  bf16x8 qf = *(const bf16x8*)(qbase + ((w + 1) & 7) * 1024);
  const unsigned lds0 = (unsigned)(uintptr_t)(LAS char*)smem;
  unsigned qa = lds0 + L_QI + lane * 16 + ((w + 2) & 7) * 1024;
  f32x16 cc = splat16(-bound);
  f32x32 sab;
  int i = 0;
  unsigned so = ((w + 1) & 7) * PREG + 6 * PSLOT, qo = ((w + 2) & 7) * 1024; float t1_; unsigned t2_;
#define RUN_OUT "=&{v[240:255]}"(o), "+{v[192:207]}"(cc), [q] "+v"(qf), [qa] "+v"(qa), [i] "+s"(i), [so] "+s"(so), [qo] "+s"(qo), [t1] "=&v"(t1_), [t2] "=&v"(t2_)
#define RUN_IN [z] "v"(zf), [idx] "v"(idx), [sh] "v"(shift), [lo] "s"(0xffffffffull), [l16] "v"(lds0 + lane * 16), [l4] "v"(lds0 + lane * 4), \
               [qb] "v"(lds0 + L_QI + lane * 16)
  STAMP(3, 0);
  asm volatile("s_cmp_eq_u32 %[fast], 0\n\ts_cbranch_scc1 .Lfb_slow0\n\t" PROLOG0_ASM PASS0_ASM POST_ASM
               "s_branch .Lfb_out%=\n.Lfb_slow0:\n\ts_waitcnt vmcnt(0)\n\t" SWAPS_ASM "\n.Lfb_out%=:"
               : "=&{v[208:239]}"(sab), RUN_OUT, KV_IO : RUN_IN, [fast] "s"(fasti) : "scc", "memory");

  if (w == 2) {
        int lane = tid & 63; asm volatile("" : "+v"(lane));
        unsigned m1 = 0, m2 = 0;
        SPIN {
          bool ok = true; m1 = 0; m2 = 0;
#pragma unroll
          for (int j = 0; j < 4; ++j) {
            const u32x4 cd = ld128(rs, WS_CAND + (unsigned)(g8 * 256 + j * 64 + lane) * 16);
            ok = ok && cd[1] == tag && cd[3] == tag;
            m2 = max(max(m2, cd[2]), min(m1, cd[0])); m1 = max(m1, cd[0]);
          }
          if (__all(ok)) break;
          __builtin_amdgcn_s_sleep(1);
        }
        CBAR();
        const unsigned k1 = __builtin_amdgcn_readfirstlane(wave_maxu(m1));
        const unsigned k2 = __builtin_amdgcn_readfirstlane(wave_maxu(m1 == k1 ? m2 : m1));
        if (lane == 0) *(u32x2*)(CTL + C_K1) = u32x2{k1, k2};
        if (b < NU) {
          const int h = lane >> 5, l31 = lane & 31;
          const u16* xr = x + key_tok(h ? k2 : k1) * D_ + l31 * 8;
        {
          const u32x4 xs0 = *(const u32x4*)xr, xs1 = *(const u32x4*)(xr + 256);
          float mean, var;
          stats32(xs0, xs1, mean, var);
          const float mb = rbf(mean), inv = __builtin_amdgcn_rcpf(ln_sd(var));
          u32x4 o0;
#pragma unroll
          for (int j = 0; j < 4; ++j) o0[j] = ln_pair((u & 4) ? xs1[j] : xs0[j], mb, inv);
          *(u32x4*)(smem + L_XS + h * 1024 + l31 * 16) = o0;
          asm volatile("s_waitcnt lgkmcnt(0)" ::: "memory");
          if (lane == 0) *(volatile LAS unsigned*)(CTL + C_SEL) = 1u;
        }
        }
  }
  if (w == 0 && b < NU) {
    int ln = tid; asm volatile("" : "+v"(ln));
    const unsigned go = (ln >> 3) * (F_ * 2) + (((ln & 7) ^ (((ln >> 4) & 1) << 2)) << 4);
#pragma unroll
    for (int j = 0; j < 16; ++j) glds16((j < 8 ? wg : wu) + ((u & 7) * 64 + (j & 7) * 8) * F_ + (u >> 3) * 64, go, smem + L_GU + j * 1024);
  }
  if (w == 1 && b < NU) {
#pragma unroll
    for (int f = 0; f < 8; ++f) glds16(wd + (u * 8 + f) * D_, lane * 16, smem + (f < 6 ? L_DN + f * 1088 : L_DN2 + (f - 6) * 1088));
  }
  asm volatile("" : "=&{v[64:95]}"(wov));
  const int iw = (0x33222453 >> (4 * w)) & 15;
  unsigned ro = row_off;
  unsigned long long wb2_;

#pragma nounroll
  while (i < 8) {
    if (b < NU && ((w == 0 && i == 2) || (w == 1 && i == 4))) {
      int lane = tid & 63, u = (b & 7) * 28 + (b >> 3);
      asm volatile("" : "+v"(lane), "+s"(u));
      const int h = lane >> 5, l31 = lane & 31, blk = (lane >> 4) & 1, q4 = (lane & 15) >> 2, p4 = lane & 3;
      if (i == 2) {
        DRAIN();
        lds_wait_ge(CTL + C_SEL, 1u);
        const char* xb = opq(smem + L_XS + (lane & 1) * 1024 + (u & 3) * 128 + h * 16);
        bf16x8 fx[4];
#pragma unroll
        for (int ks = 0; ks < 4; ++ks) fx[ks] = *(const bf16x8*)(xb + ks * 32);
#pragma unroll
        for (int nt = 0; nt < 2; ++nt) {
          const char* wb = opq(smem + L_GU + (8 * h + q4) * 128 + (((4 * nt + 2 * blk + (p4 >> 1)) ^ (((q4 >> 1) & 1) << 2)) << 4) + 8 * (p4 & 1));
#pragma unroll
          for (int mat = 0; mat < 2; ++mat) {
            f32x16 z = z16;
#pragma unroll
            for (int ks = 0; ks < 4; ++ks) z = QK(fx[ks], tr_frag(wb + mat * 8192 + ks * 2048, wb + mat * 8192 + ks * 2048 + 512), z);
            if (lane < 32) st128(rs, WS_P1 + (unsigned)u * 2048 + (mat * 64 + nt * 32 + lane) * 16, u32x4{__float_as_uint(z[0]), tag, __float_as_uint(z[1]), tag});
            SB();
          }
        }
      } else {
        const unsigned po = WS_P1 + (unsigned)((u & ~7) + (lane >> 3)) * 2048 + ((u & 7) * 8 + (lane & 7)) * 16;
        u32x4 pg, pu;
        SPIN {
          pg = ld128(rs, po); pu = ld128(rs, po + 1024);
          if (__all(pg[1] == tag && pg[3] == tag && pu[1] == tag && pu[3] == tag)) break;
          __builtin_amdgcn_s_sleep(2);
        }
        DRAIN();
        float t[4] = {__uint_as_float(pg[0]), __uint_as_float(pg[2]), __uint_as_float(pu[0]), __uint_as_float(pu[2])};
#pragma unroll
        for (int j = 0; j < 4; ++j) { t[j] += dpp_f<0x128>(t[j]); t[j] = add32(add16(t[j])); }
        const unsigned hp = pack_bf(rbf(silu_f(rbf(t[0]))) * rbf(t[2]), rbf(silu_f(rbf(t[1]))) * rbf(t[3]));
        if (lane < 8) { *(u16*)(smem + L_HS + lane * 2) = (u16)hp; *(u16*)(smem + L_HS + 16 + lane * 2) = (u16)(hp >> 16); }
        u32x4 ha = *(const u32x4*)(smem + L_HS + (lane & 1) * 16);
        if (lane >= 2) ha = u32x4{0u, 0u, 0u, 0u};
        const bf16x8 A = __builtin_bit_cast(bf16x8, ha);
        const int co = (2 * blk + (p4 >> 1)) * 16 + 8 * (p4 & 1);
        const char* db0 = opq(smem + L_DN + q4 * 1088 + co);
        const char* db1 = opq(smem + (q4 < 2 ? L_DN + (4 + q4) * 1088 : L_DN2 + (q4 - 2) * 1088) + co);
#pragma unroll 2
        for (int ct = 0; ct < 16; ++ct) {
          const bf16x8 bw = tr_frag(db0 + ct * 64, db1 + ct * 64);
          const f32x16 z = QK(A, bw, z16);
          auto sw = __builtin_amdgcn_permlane32_swap(__float_as_uint(z[0]), __float_as_uint(z[1]), false, false);
          st32(rs, WS_YP + (unsigned)(h * 64 + ct * 4 + (l31 >> 3)) * (NU * 32) + u * 32 + (l31 & 7) * 4, sw[0]);
        }
        DRAIN();
        if (lane < 8) st32(rs, WS_YF + lane * 1024 + u * 4, tag);
      }
    }
  if (w < 4 && i == 7 && b < NU) __builtin_amdgcn_s_setprio(0);
    STAMP(3, i);
    int stop = 8;
    if (b < NU) {
      if (w < 4 && i < 7) stop = 7;
      if (w == 0 && i < 2) stop = 2;
      if (w == 1 && i < 4) stop = 4;
    }
    if (!fast) {
      stop = i + 1;
      float m = -3.0e38f;
      asm volatile(ROWMAX_ASM : [m] "+v"(m), "=&{v[208:239]}"(sab) : [q] "v"(qf), KV_IN);
      shift = max32(m);
      cc = splat16(-shift);
      asm volatile(PROLOG_ASM : "=&{v[208:239]}"(sab), "+{v[192:207]}"(cc) : [q] "v"(qf), KV_IN);
    }
    asm volatile(".Lfb_loop%=:\n\t" PASSN_ASM POST_ASM WOLOAD_ASM "\n.Lfb_skip%=:\n\ts_cmp_lt_u32 %[i], %[stop]\n\ts_cbranch_scc1 .Lfb_loop%=\n.Lfb_out%=:"
                 : "+{v[208:239]}"(sab), RUN_OUT, "+{v[64:95]}"(wov), [ro] "+v"(ro), [wb2] "=&s"(wb2_)
                 : RUN_IN, [stop] "s"(stop), [iw] "s"(iw), [wb] "s"(wo + (size_t)(w * 64) * D_ + hg * 64), KV_IN : "scc", "memory");
  }
  {
  int tid = threadIdx.x; asm volatile("" : "+v"(tid));
  const int lane = tid & 63, rr = lane >> 3, c = lane & 7, h = lane >> 5, l31 = lane & 31, blk = (lane >> 4) & 1, q4 = (lane & 15) >> 2, p4 = lane & 3;
  if (w < 4 && b >= NU) {
    SPIN {
      const u32x4 f = ld128(rs, WS_YF + g8 * 1024 + min(lane, 55) * 16);
      if (__all(f[0] == tag && f[1] == tag && f[2] == tag && f[3] == tag)) break;
      __builtin_amdgcn_s_sleep(8);
    }
    CBAR();
    const unsigned rw = (unsigned)((b - NU) * 4 + w);
    const unsigned yo = WS_YP + rw * (NU * 32) + lane * 16;
    f32x4 y = {};
#pragma unroll
    for (int j = 0; j < 7; ++j) y += __builtin_bit_cast(f32x4, ld128(rs, yo + j * 1024));
#pragma unroll
    for (int j = 0; j < 4; ++j) { float t = y[j]; t += dpp_f<0x4E>(t); t += dpp_f<0x124>(t); t += dpp_f<0x128>(t); y[j] = add32(add16(t)); }
    if (lane < 2) {
      st128(rs, WS_YG + rw * 64 + lane * 32, u32x4{__float_as_uint(y[0]), tag, __float_as_uint(y[1]), tag});
      st128(rs, WS_YG + rw * 64 + lane * 32 + 16, u32x4{__float_as_uint(y[2]), tag, __float_as_uint(y[3]), tag});
    }
  }
  __builtin_amdgcn_s_setprio(0);
  DRAIN();
  const u32x4 xe = *(const u32x4*)(x + (size_t)(tb * 64 + (tid >> 3)) * D_ + hg * 64 + c * 8);
  STAMP(1, 2);
  RAW_BARRIER();
  STAMP(1, 3);
  const unsigned fr = (unsigned)((tb * 2 + (w >> 2)) * 32 + hg * 4 + (w & 3));
  {
    float a[8], pl[7], pm[7];
    auto sw = __builtin_amdgcn_permlane32_swap(__float_as_uint(o[8]), __float_as_uint(o[8]), false, false);
    float l = __uint_as_float(sw[0]);
    float mx = shift;
#pragma unroll
    for (int sidx = 0; sidx < 7; ++sidx) {
      const char* sl = smem + w * PREG + sidx * PSLOT + 2048 + l31 * 4;
      pl[sidx] = *(const float*)sl; pm[sidx] = *(const float*)(sl + 128);
      mx = fmaxf(mx, pm[sidx]);
    }
    const float f0 = __builtin_amdgcn_exp2f(shift - mx);
    l *= f0;
#pragma unroll
    for (int r = 0; r < 8; ++r) a[r] = o[r] * f0;
#pragma unroll
    for (int sidx = 0; sidx < 7; ++sidx) {
      const char* sl = smem + w * PREG + sidx * PSLOT;
      const f32x4 p0 = *(const f32x4*)(sl + lane * 16), p1 = *(const f32x4*)(sl + 1024 + lane * 16);
      const float f = __builtin_amdgcn_exp2f(pm[sidx] - mx);
#pragma unroll
      for (int r = 0; r < 4; ++r) { a[r] = fmaf(p0[r], f, a[r]); a[r + 4] = fmaf(p1[r], f, a[r + 4]); }
      l = fmaf(pl[sidx], f, l);
    }
    const float inv = __builtin_amdgcn_rcpf(l);
    const u32x4 of = {min(pack_bf(a[0] * inv, a[1] * inv), 0xfffffffeu), min(pack_bf(a[2] * inv, a[3] * inv), 0xfffffffeu),
                      min(pack_bf(a[4] * inv, a[5] * inv), 0xfffffffeu), min(pack_bf(a[6] * inv, a[7] * inv), 0xfffffffeu)};
    st128(rs, WS_O + fr * 1024 + lane * 16, of);
    *(u32x4*)(smem + L_QI + w * 1024 + lane * 16) = of;
  }
  asm volatile("s_waitcnt lgkmcnt(0)\n\tds_write_b128 %0, v[64:67]\n\tds_write_b128 %0, v[68:71] offset:1024\n\tds_write_b128 %0, v[72:75] offset:2048\n\t"
               "ds_write_b128 %0, v[76:79] offset:3072\n\tds_write_b128 %0, v[80:83] offset:4096\n\tds_write_b128 %0, v[84:87] offset:5120\n\t"
               "ds_write_b128 %0, v[88:91] offset:6144\n\tds_write_b128 %0, v[92:95] offset:7168"
               :: "v"((unsigned)(uintptr_t)(LAS char*)smem + w * PREG + rr * 128 + ((c ^ (((rr >> 1) & 1) << 2)) << 4)), "{v[64:95]}"(wov) : "memory");
  if (b == 0 && tid == 0) st32(rs, WS_EPOCH, tag);
  STAMP(1, 4);
  RAW_BARRIER();
  const int etok = tid >> 3, gtok = tb * 64 + etok;
  {
    const int tt = w >> 2, kq = w & 3;
    bf16x8 fa[2][8];
#pragma unroll
    for (int nt = 0; nt < 2; ++nt) {
      const char* wb = opq(smem + 2 * kq * PREG + (4 * h + q4) * 128 + (((4 * nt + 2 * blk + (p4 >> 1)) ^ (((q4 >> 1) & 1) << 2)) << 4) + 8 * (p4 & 1));
#pragma unroll
      for (int i = 0; i < 8; ++i) fa[nt][i] = tr_frag(wb + (i >> 2) * PREG + (i & 3) * 2048, wb + (i >> 2) * PREG + (i & 3) * 2048 + 1024);
    }
    SB();
    const unsigned fo = WS_O + (unsigned)((tb * 2 + tt) * 32 + kq * 8) * 1024 + lane * 16;
    u32x4 fb[8];
    if (hg != 2 * kq) {
#pragma unroll
      for (int i = 0; i < 4; ++i) fb[i] = ld128(rs, fo + i * 1024);
    } else {
#pragma unroll
      for (int i = 0; i < 4; ++i) fb[i] = *(const u32x4*)(smem + L_QI + (tt * 4 + i) * 1024 + lane * 16);
    }
    if (hg != 2 * kq + 1) {
#pragma unroll
      for (int i = 4; i < 8; ++i) fb[i] = ld128(rs, fo + i * 1024);
    } else {
#pragma unroll
      for (int i = 0; i < 4; ++i) fb[4 + i] = *(const u32x4*)(smem + L_QI + (tt * 4 + i) * 1024 + lane * 16);
    }
    SB();
    STAMP(2, 0);
    {
      const unsigned k1 = *(const unsigned*)(CTL + C_K1), k2 = *(const unsigned*)(CTL + C_K2);
      const int sel = (gtok == key_tok(k1)) ? 0 : ((gtok == key_tok(k2)) ? 1 : -1);
      if (__builtin_expect(__any(sel >= 0), 0)) {
        const f32x2 st = *(const f32x2*)(CTL + C_STAT + etok * 8);
        const float inv = __builtin_amdgcn_rcpf(st[1]);
        const float rsg = rbf(sigm_f(key_val(sel > 0 ? k2 : k1)));
        const unsigned go = WS_YG + (unsigned)((sel > 0 ? 512 : 0) + hg * 64 + c * 8) * 8;
        u32x4 gq[4];
        SPIN {
          bool ok = true;
#pragma unroll
          for (int j = 0; j < 4; ++j) { gq[j] = ld128(rs, go + j * 16); ok = ok && gq[j][1] == tag && gq[j][3] == tag; }
          if (__all(ok)) break;
          __builtin_amdgcn_s_sleep(1);
        }
#pragma unroll
        for (int j = 0; j < 4; ++j) {
          const unsigned xn = ln_pair(xe[j], st[0], inv);
          const float u0 = rbf(rbf(__uint_as_float(gq[j][0])) * rsg), u1 = rbf(rbf(__uint_as_float(gq[j][2])) * rsg);
          if (sel >= 0) gatep[j] = silu_pair(pack_bf(bf_lo(xn) + u0, bf_hi(xn) + u1));
        }
      }
    }
    SB();
    STAMP(2, 1);
    f32x16 acc0 = {}, acc1 = {};
#define FB_MAX(i) max(max(fb[i][0], fb[i][1]), max(fb[i][2], fb[i][3]))
#pragma unroll
    for (int hf = 0; hf < 2; ++hf) {
      if (__builtin_expect(__any(max(max(FB_MAX(4 * hf), FB_MAX(4 * hf + 1)), max(FB_MAX(4 * hf + 2), FB_MAX(4 * hf + 3))) == 0xffffffffu), 0)) {
        SPIN {
          unsigned bad = 0;
#pragma unroll
          for (int i = 4 * hf; i < 8; ++i) bad |= __any(FB_MAX(i) == 0xffffffffu) ? 1u << i : 0u;
          const unsigned need = bad & (15u << (4 * hf));
          if (!need) break;
          SPIN {
            const unsigned pv = ld32(rs, fo - lane * 16 + (lane & 7) * 1024 + 1020);
            if (!__any(pv == 0xffffffffu && ((need >> (lane & 7)) & 1u))) break;
            __builtin_amdgcn_s_sleep(1);
          }
#pragma unroll
          for (int i = 4 * hf; i < 8; ++i) if ((bad >> i) & 1u) fb[i] = ld128(rs, fo + i * 1024);
        }
      }
#pragma unroll
      for (int i = 4 * hf; i < 4 * hf + 4; ++i) {
        acc0 = QK(fa[0][i], __builtin_bit_cast(bf16x8, fb[i]), acc0);
        acc1 = QK(fa[1][i], __builtin_bit_cast(bf16x8, fb[i]), acc1);
      }
      SB();
    }
    STAMP(2, 2);
    char* pr = smem + w * PREG + 8192 + l31 * 256;
#pragma unroll
    for (int g = 0; g < 4; ++g) {
      *(f32x4*)(pr + (((2 * g + h) ^ (l31 & 7)) << 4)) = f32x4{acc0[4 * g], acc0[4 * g + 1], acc0[4 * g + 2], acc0[4 * g + 3]};
      *(f32x4*)(pr + 128 + (((2 * g + h) ^ (l31 & 7)) << 4)) = f32x4{acc1[4 * g], acc1[4 * g + 1], acc1[4 * g + 2], acc1[4 * g + 3]};
    }
  }
  RAW_BARRIER();
  {
    f32x4 s0v = {}, s1v = {};
#pragma unroll
    for (int kq = 0; kq < 4; ++kq) {
      const char* pr = smem + ((w >> 2) * 4 + kq) * PREG + 8192 + (etok & 31) * 256 + (c >> 2) * 128;
      s0v += *(const f32x4*)(pr + ((((2 * c) & 7) ^ rr) << 4)); s1v += *(const f32x4*)(pr + ((((2 * c + 1) & 7) ^ rr) << 4));
    }
    u32x4 ov;
#pragma unroll
    for (int j = 0; j < 4; ++j) {
      const unsigned sv = j < 2 ? pack_bf(s0v[2 * j], s0v[2 * j + 1]) : pack_bf(s1v[2 * j - 4], s1v[2 * j - 3]);
      const unsigned pr2 = pack_bf(bf_lo(sv) * bf_lo(gatep[j]), bf_hi(sv) * bf_hi(gatep[j]));
      ov[j] = pack_bf(bf_lo(xe[j]) + bf_lo(pr2), bf_hi(xe[j]) + bf_hi(pr2));
    }
    __builtin_amdgcn_raw_buffer_store_b128(ov, __builtin_amdgcn_make_buffer_rsrc(out, 0, S_ * D_ * 2, 0x00020000), (gtok * D_ + hg * 64 + c * 8) * 2, 0, 17);
  }
  }
  asm volatile(".Lfb_end:");
  STAMP(2, 3);
}

extern "C" void kernel_launch(void* const* d_in, const int* in_sizes, int n_in, void* d_out, int out_size, void* d_ws, size_t ws_size,
                              hipStream_t stream) {
  fused_block<<<256, 512, 0, stream>>>((const u16*)d_in[0], (const u16*)d_in[1], (const u16*)d_in[2], (const u16*)d_in[3], (const u16*)d_in[4],
                                       (const u16*)d_in[6], (const u16*)d_in[7], (const u16*)d_in[8], (const u16*)d_in[9], (char*)d_ws, (u16*)d_out);
}
```

```cpp
#include <hip/hip_runtime.h>
#include <hip/hip_bf16.h>
#include <stdint.h>

typedef __attribute__((ext_vector_type(2))) __bf16 bf16x2;
typedef __attribute__((ext_vector_type(8))) __bf16 bf16x8;
typedef __attribute__((ext_vector_type(4))) short s16x4;
typedef __attribute__((ext_vector_type(8))) short s16x8;
typedef __attribute__((ext_vector_type(2))) float f32x2;
typedef __attribute__((ext_vector_type(4))) float f32x4;
typedef __attribute__((ext_vector_type(16))) float f32x16;
typedef __attribute__((ext_vector_type(32))) float f32x32;
typedef __attribute__((ext_vector_type(2))) unsigned u32x2;
typedef __attribute__((ext_vector_type(4))) unsigned u32x4;
typedef unsigned short u16;
typedef __amdgpu_buffer_rsrc_t rsrc_t;
#define LAS __attribute__((address_space(3)))
#define DI __device__ __forceinline__

constexpr int S_ = 2048, D_ = 512, F_ = 1792;
constexpr int NU = 224;
constexpr int VHALF = 256;
constexpr int SC1 = 16;
constexpr unsigned SPIN_MAX = 1u << 20;

constexpr unsigned WS_EPOCH = 0;
constexpr unsigned WS_KVF = 1024;
constexpr unsigned WS_CAND = WS_KVF + 8 * 256 * 8;
constexpr unsigned WS_YF = WS_CAND + 8 * 512 * 8;
constexpr unsigned WS_YG = WS_YF + 8 * 1024;
constexpr unsigned WS_K = WS_YG + 1024 * 8;
constexpr unsigned WS_V = WS_K + S_ * 32;
constexpr unsigned WS_YP = WS_V + 256 * VHALF;
constexpr unsigned WS_P1 = WS_YP + NU * 2 * D_ * 4;
constexpr unsigned WS_O = WS_P1 + NU * 2048;
constexpr unsigned WS_END = WS_O + S_ * D_ * 2;

constexpr int L_WI = 0;
constexpr int L_XI = 65536;
constexpr int L_KI = 131072;
constexpr int PSLOT = 2304;
constexpr int PREG = 16384;
constexpr int L_XS = 7 * PREG;
constexpr int L_GU = 131072;
constexpr int L_QI = 147456;
constexpr int L_CTL = L_QI + 8192;
constexpr int L_DN = L_CTL + 1024;
constexpr int L_DN2 = 7 * PREG + PSLOT + 128;
constexpr int L_HS = 0;
constexpr int L_TOTAL = L_DN + 6 * 1088;
constexpr int C_CNT = 0;
constexpr int C_SEL = 4;
constexpr int C_K1 = 8, C_K2 = 12;
constexpr int C_QMAX = 16;
constexpr int C_R = 32;
constexpr int C_STAT = 64;

#define STAMP(kid, i)

DI float bf_lo(unsigned u) { return __uint_as_float(u << 16); }
DI float bf_hi(unsigned u) { return __uint_as_float(u & 0xffff0000u); }
DI unsigned pack_bf(float a, float b) { f32x2 f = {a, b}; return __builtin_bit_cast(unsigned, __builtin_convertvector(f, bf16x2)); }
DI float rbf(float a) { return (float)(__bf16)a; }
DI float dot2(unsigned a, unsigned b, float c) { return __builtin_amdgcn_fdot2_f32_bf16(__builtin_bit_cast(bf16x2, a), __builtin_bit_cast(bf16x2, b), c, false); }
DI void glds16(const void* sbase, unsigned voff, void* lds_wave_base) {
  unsigned keep; const unsigned dst = (unsigned)(uintptr_t)(LAS char*)lds_wave_base;
  asm volatile("s_mov_b32 %0, m0\n\ts_mov_b32 m0, %2\n\ts_nop 0\n\tglobal_load_lds_dwordx4 %1, %3\n\ts_mov_b32 m0, %0"
               : "=&s"(keep) : "v"(voff), "s"(dst), "s"(sbase) : "memory");
}
#define GLD128(dst, voff, sbase) asm volatile("global_load_dwordx4 %0, %1, %2" : "=v"(dst) : "v"(voff), "s"(sbase) : "memory")
DI const char* opq(const char* p) { unsigned a = (unsigned)(uintptr_t)(LAS char*)p; asm volatile("" : "+v"(a)); return (const char*)(LAS char*)(uintptr_t)a; }
DI s16x4 tr_read(const char* p) { return __builtin_amdgcn_ds_read_tr16_b64_v4i16((LAS s16x4*)p); }
DI bf16x8 tr_frag(const char* p0, const char* p1) {
  s16x4 a = tr_read(p0), b = tr_read(p1);
  s16x8 v = __builtin_shufflevector(a, b, 0, 1, 2, 3, 4, 5, 6, 7);
  return __builtin_bit_cast(bf16x8, v);
}
template <int CTRL> DI float dpp_f(float v) { return __int_as_float(__builtin_amdgcn_update_dpp(0, __float_as_int(v), CTRL, 0xf, 0xf, true)); }
template <int CTRL> DI unsigned dpp_u(unsigned v) { return (unsigned)__builtin_amdgcn_update_dpp(0, (int)v, CTRL, 0xf, 0xf, true); }
DI float sum8(float v) {
  v += dpp_f<0xB1>(v); v += dpp_f<0x4E>(v); v += dpp_f<0x141>(v);
  return v;
}
DI float add16(float v) { auto r = __builtin_amdgcn_permlane16_swap(__float_as_uint(v), __float_as_uint(v), false, false); return __uint_as_float(r[0]) + __uint_as_float(r[1]); }
DI float add32(float v) { auto r = __builtin_amdgcn_permlane32_swap(__float_as_uint(v), __float_as_uint(v), false, false); return __uint_as_float(r[0]) + __uint_as_float(r[1]); }
DI float max16(float v) { auto r = __builtin_amdgcn_permlane16_swap(__float_as_uint(v), __float_as_uint(v), false, false); return fmaxf(__uint_as_float(r[0]), __uint_as_float(r[1])); }
DI float max32(float v) { auto r = __builtin_amdgcn_permlane32_swap(__float_as_uint(v), __float_as_uint(v), false, false); return fmaxf(__uint_as_float(r[0]), __uint_as_float(r[1])); }
DI float sum32(float v) { v = sum8(v); v += dpp_f<0x140>(v); return add16(v); }
DI float wave_sum(float v) { return add32(sum32(v)); }
DI float wave_maxf(float v) {
  v = fmaxf(v, dpp_f<0xB1>(v)); v = fmaxf(v, dpp_f<0x4E>(v)); v = fmaxf(v, dpp_f<0x141>(v)); v = fmaxf(v, dpp_f<0x140>(v));
  return max32(max16(v));
}
DI unsigned wave_maxu(unsigned v) {
  v = max(v, dpp_u<0xB1>(v)); v = max(v, dpp_u<0x4E>(v)); v = max(v, dpp_u<0x141>(v)); v = max(v, dpp_u<0x140>(v));
  auto r = __builtin_amdgcn_permlane16_swap(v, v, false, false); v = max(r[0], r[1]);
  auto q = __builtin_amdgcn_permlane32_swap(v, v, false, false); return max(q[0], q[1]);
}
DI float wave_maxpos(float v) { return __uint_as_float(wave_maxu(__float_as_uint(v))); }
DI float silu_f(float v) { return v * __builtin_amdgcn_rcpf(1.0f + __builtin_amdgcn_exp2f(-1.4426950408889634f * v)); }
DI float sigm_f(float v) { return __builtin_amdgcn_rcpf(1.0f + __builtin_amdgcn_exp2f(-1.4426950408889634f * v)); }
DI float ln_sd(float var) { return rbf(__builtin_amdgcn_sqrtf(rbf(rbf(var) + 1e-5f))); }
DI unsigned ln_pair(unsigned xu, float mb, float inv) {
  const unsigned d = pack_bf(bf_lo(xu) - mb, bf_hi(xu) - mb);
  return pack_bf(bf_lo(d) * inv, bf_hi(d) * inv);
}
DI unsigned silu_pair(unsigned v) { return pack_bf(silu_f(bf_lo(v)), silu_f(bf_hi(v))); }

DI void stats32(const u32x4& a, const u32x4& b2, float& mean, float& var) {
  float s = 0.f, q = 0.f;
#pragma unroll
  for (int j = 0; j < 4; ++j) { s = dot2(a[j], 0x3f803f80u, s); s = dot2(b2[j], 0x3f803f80u, s); q = dot2(a[j], a[j], q); q = dot2(b2[j], b2[j], q); }
  mean = sum32(s) * (1.0f / 512.0f);
  var = fmaf(-mean, mean, sum32(q) * (1.0f / 512.0f));
  if (__builtin_expect(__any(!(var >= mean * mean * 0.015625f)), 0)) {
    float v = 0.f;
#pragma unroll
    for (int j = 0; j < 4; ++j) {
      const float a0 = bf_lo(a[j]) - mean, a1 = bf_hi(a[j]) - mean, a2 = bf_lo(b2[j]) - mean, a3 = bf_hi(b2[j]) - mean;
      v = fmaf(a0, a0, v); v = fmaf(a1, a1, v); v = fmaf(a2, a2, v); v = fmaf(a3, a3, v);
    }
    var = sum32(v) * (1.0f / 512.0f);
  }
}

DI unsigned make_key(float r_bf, int token) {
  unsigned b = __float_as_uint(r_bf) >> 16;
  b = (b & 0x8000u) ? (~b & 0xffffu) : (b | 0x8000u);
  return (b << 16) | (unsigned)(2047 - token);
}
DI float key_val(unsigned key) {
  unsigned b = key >> 16;
  b = (b & 0x8000u) ? (b & 0x7fffu) : (~b & 0xffffu);
  return __uint_as_float(b << 16);
}
DI int key_tok(unsigned key) { return 2047 - (int)(key & 0x7ffu); }

DI u32x4 ld128(rsrc_t rs, unsigned off) { return __builtin_amdgcn_raw_buffer_load_b128(rs, (int)off, 0, SC1); }
DI u32x2 ld64(rsrc_t rs, unsigned off) { return __builtin_amdgcn_raw_buffer_load_b64(rs, (int)off, 0, SC1); }
DI unsigned ld32(rsrc_t rs, unsigned off) { return __builtin_amdgcn_raw_buffer_load_b32(rs, (int)off, 0, SC1); }
DI void st128(rsrc_t rs, unsigned off, u32x4 v) { __builtin_amdgcn_raw_buffer_store_b128(v, rs, (int)off, 0, SC1); }
DI void st64(rsrc_t rs, unsigned off, u32x2 v) { __builtin_amdgcn_raw_buffer_store_b64(v, rs, (int)off, 0, SC1); }
DI void st32(rsrc_t rs, unsigned off, unsigned v) { __builtin_amdgcn_raw_buffer_store_b32(v, rs, (int)off, 0, SC1); }
#define DRAIN() asm volatile("s_waitcnt vmcnt(0)" ::: "memory")
#define VMWAIT(n) asm volatile("s_waitcnt vmcnt(" #n ")" ::: "memory")
#define CBAR() asm volatile("" ::: "memory")
#define RAW_BARRIER() do { asm volatile("s_waitcnt lgkmcnt(0)" ::: "memory"); __builtin_amdgcn_s_barrier(); asm volatile("" ::: "memory"); } while (0)
#define SLEEP(n) do { __builtin_amdgcn_s_sleep(n); asm volatile("" ::: "memory"); } while (0)
#define SPIN _Pragma("nounroll") for (unsigned s_ = 0; s_ < SPIN_MAX; ++s_)
DI unsigned lds_ld(const char* p) { return __hip_atomic_load((LAS unsigned*)p, __ATOMIC_RELAXED, __HIP_MEMORY_SCOPE_WORKGROUP); }
DI void lds_wait_ge(const char* p, unsigned want) {
  SPIN {
    if ((unsigned)__builtin_amdgcn_readfirstlane(lds_ld(p)) >= want) break;
    SLEEP(1);
  }
  CBAR();
}
#define QK(KF, QF, C) __builtin_amdgcn_mfma_f32_32x32x16_bf16((KF), (QF), (C), 0, 0, 0)
DI f32x16 splat16(float v) { f32x16 r;
#pragma unroll
  for (int i = 0; i < 16; ++i) r[i] = v;
  return r; }
#define SB() __builtin_amdgcn_sched_barrier(0)
#define SGB(mask, n) __builtin_amdgcn_sched_group_barrier(mask, n, 0)

#define VCONST_ASM \
  "v_mov_b32 v132, %[cst]\n\t" "v_mov_b32 v133, %[cst]\n\t" "v_mov_b32 v134, %[cst]\n\t" "v_mov_b32 v135, %[cst]\n\t" "v_mov_b32 v140, %[cst]\n\t" \
  "v_mov_b32 v141, %[cst]\n\t" "v_mov_b32 v142, %[cst]\n\t" "v_mov_b32 v143, %[cst]\n\t" "v_mov_b32 v148, %[cst]\n\t" "v_mov_b32 v149, %[cst]\n\t" \
  "v_mov_b32 v150, %[cst]\n\t" "v_mov_b32 v151, %[cst]\n\t" "v_mov_b32 v156, %[cst]\n\t" "v_mov_b32 v157, %[cst]\n\t" "v_mov_b32 v158, %[cst]\n\t" \
  "v_mov_b32 v159, %[cst]\n\t" "v_mov_b32 v164, %[cst]\n\t" "v_mov_b32 v165, %[cst]\n\t" "v_mov_b32 v166, %[cst]\n\t" "v_mov_b32 v167, %[cst]\n\t" \
  "v_mov_b32 v172, %[cst]\n\t" "v_mov_b32 v173, %[cst]\n\t" "v_mov_b32 v174, %[cst]\n\t" "v_mov_b32 v175, %[cst]\n\t" "v_mov_b32 v180, %[cst]\n\t" \
  "v_mov_b32 v181, %[cst]\n\t" "v_mov_b32 v182, %[cst]\n\t" "v_mov_b32 v183, %[cst]\n\t" "v_mov_b32 v188, %[cst]\n\t" "v_mov_b32 v189, %[cst]\n\t" \
  "v_mov_b32 v190, %[cst]\n\t" "v_mov_b32 v191, %[cst]\n\t" ""
#define LOADS_ASM \
  "buffer_load_dwordx4 v[96:99], %[ka0], %[rs], 0 offen offset:0 sc1\n\t" "buffer_load_dwordx4 v[128:131], %[va0], %[rs], 0 offen offset:0 sc1\n\t" \
  "buffer_load_dwordx4 v[100:103], %[ka0], %[rs], 0 offen offset:1024 sc1\n\t" "buffer_load_dwordx4 v[136:139], %[va0], %[rs], 0 offen offset:1024 sc1\n\t" \
  "buffer_load_dwordx4 v[104:107], %[ka0], %[rs], 0 offen offset:2048 sc1\n\t" "buffer_load_dwordx4 v[144:147], %[va0], %[rs], 0 offen offset:2048 sc1\n\t" \
  "buffer_load_dwordx4 v[108:111], %[ka0], %[rs], 0 offen offset:3072 sc1\n\t" "buffer_load_dwordx4 v[152:155], %[va0], %[rs], 0 offen offset:3072 sc1\n\t" \
  "buffer_load_dwordx4 v[112:115], %[ka1], %[rs], 0 offen offset:0 sc1\n\t" "buffer_load_dwordx4 v[160:163], %[va1], %[rs], 0 offen offset:0 sc1\n\t" \
  "buffer_load_dwordx4 v[116:119], %[ka1], %[rs], 0 offen offset:1024 sc1\n\t" "buffer_load_dwordx4 v[168:171], %[va1], %[rs], 0 offen offset:1024 sc1\n\t" \
  "buffer_load_dwordx4 v[120:123], %[ka1], %[rs], 0 offen offset:2048 sc1\n\t" "buffer_load_dwordx4 v[176:179], %[va1], %[rs], 0 offen offset:2048 sc1\n\t" \
  "buffer_load_dwordx4 v[124:127], %[ka1], %[rs], 0 offen offset:3072 sc1\n\t" "buffer_load_dwordx4 v[184:187], %[va1], %[rs], 0 offen offset:3072 sc1\n\t" ""
#define SWAPS_ASM \
  "v_permlane16_swap_b32 v128, v132\n\t" "v_permlane16_swap_b32 v129, v133\n\t" "v_permlane16_swap_b32 v130, v134\n\t" "v_permlane16_swap_b32 v131, v135\n\t" \
  "v_permlane16_swap_b32 v136, v140\n\t" "v_permlane16_swap_b32 v137, v141\n\t" "v_permlane16_swap_b32 v138, v142\n\t" "v_permlane16_swap_b32 v139, v143\n\t" \
  "v_permlane16_swap_b32 v144, v148\n\t" "v_permlane16_swap_b32 v145, v149\n\t" "v_permlane16_swap_b32 v146, v150\n\t" "v_permlane16_swap_b32 v147, v151\n\t" \
  "v_permlane16_swap_b32 v152, v156\n\t" "v_permlane16_swap_b32 v153, v157\n\t" "v_permlane16_swap_b32 v154, v158\n\t" "v_permlane16_swap_b32 v155, v159\n\t" \
  "v_permlane16_swap_b32 v160, v164\n\t" "v_permlane16_swap_b32 v161, v165\n\t" "v_permlane16_swap_b32 v162, v166\n\t" "v_permlane16_swap_b32 v163, v167\n\t" \
  "v_permlane16_swap_b32 v168, v172\n\t" "v_permlane16_swap_b32 v169, v173\n\t" "v_permlane16_swap_b32 v170, v174\n\t" "v_permlane16_swap_b32 v171, v175\n\t" \
  "v_permlane16_swap_b32 v176, v180\n\t" "v_permlane16_swap_b32 v177, v181\n\t" "v_permlane16_swap_b32 v178, v182\n\t" "v_permlane16_swap_b32 v179, v183\n\t" \
  "v_permlane16_swap_b32 v184, v188\n\t" "v_permlane16_swap_b32 v185, v189\n\t" "v_permlane16_swap_b32 v186, v190\n\t" "v_permlane16_swap_b32 v187, v191\n\t" ""
#define PROLOG0_ASM \
  "s_waitcnt vmcnt(15)\n\t" "s_nop 1\n\t" "v_mfma_f32_32x32x16_bf16 v[208:223], v[96:99], %[q], v[192:207]\n\t" "s_waitcnt vmcnt(13)\n\t" \
  "v_mfma_f32_32x32x16_bf16 v[224:239], v[100:103], %[q], v[192:207]\n\t" "v_permlane16_swap_b32 v128, v132\n\t" "v_permlane16_swap_b32 v129, v133\n\t" \
  "v_permlane16_swap_b32 v130, v134\n\t" "v_permlane16_swap_b32 v131, v135\n\t" "s_nop 6\n\t" "v_exp_f32 v208, v208\n\t" "v_exp_f32 v209, v209\n\t" \
  "v_exp_f32 v210, v210\n\t" "v_exp_f32 v211, v211\n\t" "v_exp_f32 v212, v212\n\t" "v_exp_f32 v213, v213\n\t" "v_exp_f32 v214, v214\n\t" "v_exp_f32 v215, v215\n\t" \
  "v_exp_f32 v216, v216\n\t" "v_exp_f32 v217, v217\n\t" "v_exp_f32 v218, v218\n\t" "v_exp_f32 v219, v219\n\t" "v_exp_f32 v220, v220\n\t" ""
#define PROLOG_ASM \
  "s_nop 1\n\t" "v_mfma_f32_32x32x16_bf16 v[208:223], v[96:99], %[q], v[192:207]\n\t" "v_mfma_f32_32x32x16_bf16 v[224:239], v[100:103], %[q], v[192:207]\n\t" \
  "s_nop 10\n\t" "v_exp_f32 v208, v208\n\t" "v_exp_f32 v209, v209\n\t" "v_exp_f32 v210, v210\n\t" "v_exp_f32 v211, v211\n\t" "v_exp_f32 v212, v212\n\t" \
  "v_exp_f32 v213, v213\n\t" "v_exp_f32 v214, v214\n\t" "v_exp_f32 v215, v215\n\t" "v_exp_f32 v216, v216\n\t" "v_exp_f32 v217, v217\n\t" "v_exp_f32 v218, v218\n\t" \
  "v_exp_f32 v219, v219\n\t" "v_exp_f32 v220, v220\n\t" ""
#define PASS0_ASM \
  "v_mfma_f32_32x32x16_bf16 v[240:255], %[z], %[z], 0\n\t" "v_exp_f32 v221, v221\n\t" "v_exp_f32 v222, v222\n\t" "v_exp_f32 v223, v223\n\t" \
  "v_smfmac_f32_32x32x32_bf16 v[240:255], v[128:131], v[208:215], %[idx]\n\t" "v_exp_f32 v224, v224\n\t" "v_exp_f32 v225, v225\n\t" \
  "v_smfmac_f32_32x32x32_bf16 v[240:255], v[132:135], v[216:223], %[idx]\n\t" "v_exp_f32 v226, v226\n\t" "v_exp_f32 v227, v227\n\t" "v_exp_f32 v228, v228\n\t" \
  "s_waitcnt vmcnt(11)\n\t" "v_mfma_f32_32x32x16_bf16 v[208:223], v[104:107], %[q], v[192:207]\n\t" "v_permlane16_swap_b32 v136, v140\n\t" \
  "v_permlane16_swap_b32 v137, v141\n\t" "v_permlane16_swap_b32 v138, v142\n\t" "v_permlane16_swap_b32 v139, v143\n\t" "v_exp_f32 v229, v229\n\t" \
  "v_exp_f32 v230, v230\n\t" "v_exp_f32 v231, v231\n\t" "v_exp_f32 v232, v232\n\t" "v_exp_f32 v233, v233\n\t" \
  "v_smfmac_f32_32x32x32_bf16 v[240:255], v[136:139], v[224:231], %[idx]\n\t" "v_exp_f32 v234, v234\n\t" "v_exp_f32 v235, v235\n\t" "v_exp_f32 v236, v236\n\t" \
  "v_exp_f32 v237, v237\n\t" "v_exp_f32 v238, v238\n\t" "v_exp_f32 v239, v239\n\t" "v_exp_f32 v208, v208\n\t" "v_exp_f32 v209, v209\n\t" \
  "v_smfmac_f32_32x32x32_bf16 v[240:255], v[140:143], v[232:239], %[idx]\n\t" "v_exp_f32 v210, v210\n\t" "v_exp_f32 v211, v211\n\t" "v_exp_f32 v212, v212\n\t" \
  "s_waitcnt vmcnt(9)\n\t" "v_mfma_f32_32x32x16_bf16 v[224:239], v[108:111], %[q], v[192:207]\n\t" "v_permlane16_swap_b32 v144, v148\n\t" \
  "v_permlane16_swap_b32 v145, v149\n\t" "v_permlane16_swap_b32 v146, v150\n\t" "v_permlane16_swap_b32 v147, v151\n\t" "v_exp_f32 v213, v213\n\t" \
  "v_exp_f32 v214, v214\n\t" "v_exp_f32 v215, v215\n\t" "v_exp_f32 v216, v216\n\t" "v_exp_f32 v217, v217\n\t" \
  "v_smfmac_f32_32x32x32_bf16 v[240:255], v[144:147], v[208:215], %[idx]\n\t" "v_exp_f32 v218, v218\n\t" "v_exp_f32 v219, v219\n\t" "v_exp_f32 v220, v220\n\t" \
  "v_exp_f32 v221, v221\n\t" "v_exp_f32 v222, v222\n\t" "v_exp_f32 v223, v223\n\t" "v_exp_f32 v224, v224\n\t" "v_exp_f32 v225, v225\n\t" \
  "v_smfmac_f32_32x32x32_bf16 v[240:255], v[148:151], v[216:223], %[idx]\n\t" "v_exp_f32 v226, v226\n\t" "v_exp_f32 v227, v227\n\t" "v_exp_f32 v228, v228\n\t" \
  "s_waitcnt vmcnt(7)\n\t" "v_mfma_f32_32x32x16_bf16 v[208:223], v[112:115], %[q], v[192:207]\n\t" "v_permlane16_swap_b32 v152, v156\n\t" \
  "v_permlane16_swap_b32 v153, v157\n\t" "v_permlane16_swap_b32 v154, v158\n\t" "v_permlane16_swap_b32 v155, v159\n\t" "v_exp_f32 v229, v229\n\t" \
  "v_exp_f32 v230, v230\n\t" "v_exp_f32 v231, v231\n\t" "v_exp_f32 v232, v232\n\t" "v_exp_f32 v233, v233\n\t" \
  "v_smfmac_f32_32x32x32_bf16 v[240:255], v[152:155], v[224:231], %[idx]\n\t" "v_exp_f32 v234, v234\n\t" "v_exp_f32 v235, v235\n\t" "v_exp_f32 v236, v236\n\t" \
  "v_exp_f32 v237, v237\n\t" "v_exp_f32 v238, v238\n\t" "v_exp_f32 v239, v239\n\t" "v_exp_f32 v208, v208\n\t" "v_exp_f32 v209, v209\n\t" \
  "v_smfmac_f32_32x32x32_bf16 v[240:255], v[156:159], v[232:239], %[idx]\n\t" "v_exp_f32 v210, v210\n\t" "v_exp_f32 v211, v211\n\t" "v_exp_f32 v212, v212\n\t" \
  "s_waitcnt vmcnt(5)\n\t" "v_mfma_f32_32x32x16_bf16 v[224:239], v[116:119], %[q], v[192:207]\n\t" "v_permlane16_swap_b32 v160, v164\n\t" \
  "v_permlane16_swap_b32 v161, v165\n\t" "v_permlane16_swap_b32 v162, v166\n\t" "v_permlane16_swap_b32 v163, v167\n\t" "v_exp_f32 v213, v213\n\t" \
  "v_exp_f32 v214, v214\n\t" "v_exp_f32 v215, v215\n\t" "v_exp_f32 v216, v216\n\t" "v_exp_f32 v217, v217\n\t" \
  "v_smfmac_f32_32x32x32_bf16 v[240:255], v[160:163], v[208:215], %[idx]\n\t" "v_exp_f32 v218, v218\n\t" "v_exp_f32 v219, v219\n\t" "v_exp_f32 v220, v220\n\t" \
  "v_exp_f32 v221, v221\n\t" "v_exp_f32 v222, v222\n\t" "v_exp_f32 v223, v223\n\t" "v_exp_f32 v224, v224\n\t" "v_exp_f32 v225, v225\n\t" \
  "v_smfmac_f32_32x32x32_bf16 v[240:255], v[164:167], v[216:223], %[idx]\n\t" "v_exp_f32 v226, v226\n\t" "v_exp_f32 v227, v227\n\t" "v_exp_f32 v228, v228\n\t" \
  "s_waitcnt vmcnt(3)\n\t" "v_mfma_f32_32x32x16_bf16 v[208:223], v[120:123], %[q], v[192:207]\n\t" "v_permlane16_swap_b32 v168, v172\n\t" \
  "v_permlane16_swap_b32 v169, v173\n\t" "v_permlane16_swap_b32 v170, v174\n\t" "v_permlane16_swap_b32 v171, v175\n\t" "v_exp_f32 v229, v229\n\t" \
  "v_exp_f32 v230, v230\n\t" "v_exp_f32 v231, v231\n\t" "v_exp_f32 v232, v232\n\t" "v_exp_f32 v233, v233\n\t" \
  "v_smfmac_f32_32x32x32_bf16 v[240:255], v[168:171], v[224:231], %[idx]\n\t" "v_exp_f32 v234, v234\n\t" "v_exp_f32 v235, v235\n\t" "v_exp_f32 v236, v236\n\t" \
  "v_exp_f32 v237, v237\n\t" "v_exp_f32 v238, v238\n\t" "v_exp_f32 v239, v239\n\t" "v_exp_f32 v208, v208\n\t" "v_exp_f32 v209, v209\n\t" \
  "v_smfmac_f32_32x32x32_bf16 v[240:255], v[172:175], v[232:239], %[idx]\n\t" "v_exp_f32 v210, v210\n\t" "v_exp_f32 v211, v211\n\t" "v_exp_f32 v212, v212\n\t" \
  "s_waitcnt vmcnt(1)\n\t" "v_mfma_f32_32x32x16_bf16 v[224:239], v[124:127], %[q], v[192:207]\n\t" "ds_read_b128 %[q], %[qa]\n\t" "v_permlane16_swap_b32 v176, v180\n\t" \
  "v_permlane16_swap_b32 v177, v181\n\t" "v_permlane16_swap_b32 v178, v182\n\t" "v_permlane16_swap_b32 v179, v183\n\t" "v_exp_f32 v213, v213\n\t" \
  "v_exp_f32 v214, v214\n\t" "v_exp_f32 v215, v215\n\t" "v_exp_f32 v216, v216\n\t" "v_exp_f32 v217, v217\n\t" \
  "v_smfmac_f32_32x32x32_bf16 v[240:255], v[176:179], v[208:215], %[idx]\n\t" "v_exp_f32 v218, v218\n\t" "v_exp_f32 v219, v219\n\t" "v_exp_f32 v220, v220\n\t" \
  "v_exp_f32 v221, v221\n\t" "v_exp_f32 v222, v222\n\t" "v_exp_f32 v223, v223\n\t" "v_exp_f32 v224, v224\n\t" "v_exp_f32 v225, v225\n\t" \
  "v_smfmac_f32_32x32x32_bf16 v[240:255], v[180:183], v[216:223], %[idx]\n\t" "v_exp_f32 v226, v226\n\t" "v_exp_f32 v227, v227\n\t" "v_exp_f32 v228, v228\n\t" \
  "s_waitcnt vmcnt(0) lgkmcnt(0)\n\t" "v_mfma_f32_32x32x16_bf16 v[208:223], v[96:99], %[q], v[192:207]\n\t" "v_permlane16_swap_b32 v184, v188\n\t" \
  "v_permlane16_swap_b32 v185, v189\n\t" "v_permlane16_swap_b32 v186, v190\n\t" "v_permlane16_swap_b32 v187, v191\n\t" "v_exp_f32 v229, v229\n\t" \
  "v_exp_f32 v230, v230\n\t" "v_exp_f32 v231, v231\n\t" "v_exp_f32 v232, v232\n\t" "v_exp_f32 v233, v233\n\t" \
  "v_smfmac_f32_32x32x32_bf16 v[240:255], v[184:187], v[224:231], %[idx]\n\t" "v_exp_f32 v234, v234\n\t" "v_exp_f32 v235, v235\n\t" "v_exp_f32 v236, v236\n\t" \
  "v_exp_f32 v237, v237\n\t" "v_exp_f32 v238, v238\n\t" "v_exp_f32 v239, v239\n\t" "v_exp_f32 v208, v208\n\t" "v_exp_f32 v209, v209\n\t" \
  "v_smfmac_f32_32x32x32_bf16 v[240:255], v[188:191], v[232:239], %[idx]\n\t" "v_exp_f32 v210, v210\n\t" "v_exp_f32 v211, v211\n\t" "v_exp_f32 v212, v212\n\t" \
  "v_mfma_f32_32x32x16_bf16 v[224:239], v[100:103], %[q], v[192:207]\n\t" "v_exp_f32 v213, v213\n\t" "v_exp_f32 v214, v214\n\t" "v_exp_f32 v215, v215\n\t" \
  "v_exp_f32 v216, v216\n\t" "v_exp_f32 v217, v217\n\t" "v_exp_f32 v218, v218\n\t" "v_exp_f32 v219, v219\n\t" "v_exp_f32 v220, v220\n\t" ""
#define PASSN_ASM \
  "v_mfma_f32_32x32x16_bf16 v[240:255], %[z], %[z], 0\n\t" "v_exp_f32 v221, v221\n\t" "v_exp_f32 v222, v222\n\t" "v_exp_f32 v223, v223\n\t" \
  "v_smfmac_f32_32x32x32_bf16 v[240:255], v[128:131], v[208:215], %[idx]\n\t" "v_exp_f32 v224, v224\n\t" "v_exp_f32 v225, v225\n\t" \
  "v_smfmac_f32_32x32x32_bf16 v[240:255], v[132:135], v[216:223], %[idx]\n\t" "v_exp_f32 v226, v226\n\t" "v_exp_f32 v227, v227\n\t" "v_exp_f32 v228, v228\n\t" \
  "v_mfma_f32_32x32x16_bf16 v[208:223], v[104:107], %[q], v[192:207]\n\t" "v_exp_f32 v229, v229\n\t" "v_exp_f32 v230, v230\n\t" "v_exp_f32 v231, v231\n\t" \
  "v_exp_f32 v232, v232\n\t" "v_exp_f32 v233, v233\n\t" "v_smfmac_f32_32x32x32_bf16 v[240:255], v[136:139], v[224:231], %[idx]\n\t" "v_exp_f32 v234, v234\n\t" \
  "v_exp_f32 v235, v235\n\t" "v_exp_f32 v236, v236\n\t" "v_exp_f32 v237, v237\n\t" "v_exp_f32 v238, v238\n\t" "v_exp_f32 v239, v239\n\t" "v_exp_f32 v208, v208\n\t" \
  "v_exp_f32 v209, v209\n\t" "v_smfmac_f32_32x32x32_bf16 v[240:255], v[140:143], v[232:239], %[idx]\n\t" "v_exp_f32 v210, v210\n\t" "v_exp_f32 v211, v211\n\t" \
  "v_exp_f32 v212, v212\n\t" "v_mfma_f32_32x32x16_bf16 v[224:239], v[108:111], %[q], v[192:207]\n\t" "v_exp_f32 v213, v213\n\t" "v_exp_f32 v214, v214\n\t" \
  "v_exp_f32 v215, v215\n\t" "v_exp_f32 v216, v216\n\t" "v_exp_f32 v217, v217\n\t" "v_smfmac_f32_32x32x32_bf16 v[240:255], v[144:147], v[208:215], %[idx]\n\t" \
  "v_exp_f32 v218, v218\n\t" "v_exp_f32 v219, v219\n\t" "v_exp_f32 v220, v220\n\t" "v_exp_f32 v221, v221\n\t" "v_exp_f32 v222, v222\n\t" "v_exp_f32 v223, v223\n\t" \
  "v_exp_f32 v224, v224\n\t" "v_exp_f32 v225, v225\n\t" "v_smfmac_f32_32x32x32_bf16 v[240:255], v[148:151], v[216:223], %[idx]\n\t" "v_exp_f32 v226, v226\n\t" \
  "v_exp_f32 v227, v227\n\t" "v_exp_f32 v228, v228\n\t" "v_mfma_f32_32x32x16_bf16 v[208:223], v[112:115], %[q], v[192:207]\n\t" "v_exp_f32 v229, v229\n\t" \
  "v_exp_f32 v230, v230\n\t" "v_exp_f32 v231, v231\n\t" "v_exp_f32 v232, v232\n\t" "v_exp_f32 v233, v233\n\t" \
  "v_smfmac_f32_32x32x32_bf16 v[240:255], v[152:155], v[224:231], %[idx]\n\t" "v_exp_f32 v234, v234\n\t" "v_exp_f32 v235, v235\n\t" "v_exp_f32 v236, v236\n\t" \
  "v_exp_f32 v237, v237\n\t" "v_exp_f32 v238, v238\n\t" "v_exp_f32 v239, v239\n\t" "v_exp_f32 v208, v208\n\t" "v_exp_f32 v209, v209\n\t" \
  "v_smfmac_f32_32x32x32_bf16 v[240:255], v[156:159], v[232:239], %[idx]\n\t" "v_exp_f32 v210, v210\n\t" "v_exp_f32 v211, v211\n\t" "v_exp_f32 v212, v212\n\t" \
  "v_mfma_f32_32x32x16_bf16 v[224:239], v[116:119], %[q], v[192:207]\n\t" "v_exp_f32 v213, v213\n\t" "v_exp_f32 v214, v214\n\t" "v_exp_f32 v215, v215\n\t" \
  "v_exp_f32 v216, v216\n\t" "v_exp_f32 v217, v217\n\t" "v_smfmac_f32_32x32x32_bf16 v[240:255], v[160:163], v[208:215], %[idx]\n\t" "v_exp_f32 v218, v218\n\t" \
  "v_exp_f32 v219, v219\n\t" "v_exp_f32 v220, v220\n\t" "v_exp_f32 v221, v221\n\t" "v_exp_f32 v222, v222\n\t" "v_exp_f32 v223, v223\n\t" "v_exp_f32 v224, v224\n\t" \
  "v_exp_f32 v225, v225\n\t" "v_smfmac_f32_32x32x32_bf16 v[240:255], v[164:167], v[216:223], %[idx]\n\t" "v_exp_f32 v226, v226\n\t" "v_exp_f32 v227, v227\n\t" \
  "v_exp_f32 v228, v228\n\t" "v_mfma_f32_32x32x16_bf16 v[208:223], v[120:123], %[q], v[192:207]\n\t" "v_exp_f32 v229, v229\n\t" "v_exp_f32 v230, v230\n\t" \
  "v_exp_f32 v231, v231\n\t" "v_exp_f32 v232, v232\n\t" "v_exp_f32 v233, v233\n\t" "v_smfmac_f32_32x32x32_bf16 v[240:255], v[168:171], v[224:231], %[idx]\n\t" \
  "v_exp_f32 v234, v234\n\t" "v_exp_f32 v235, v235\n\t" "v_exp_f32 v236, v236\n\t" "v_exp_f32 v237, v237\n\t" "v_exp_f32 v238, v238\n\t" "v_exp_f32 v239, v239\n\t" \
  "v_exp_f32 v208, v208\n\t" "v_exp_f32 v209, v209\n\t" "v_smfmac_f32_32x32x32_bf16 v[240:255], v[172:175], v[232:239], %[idx]\n\t" "v_exp_f32 v210, v210\n\t" \
  "v_exp_f32 v211, v211\n\t" "v_exp_f32 v212, v212\n\t" "v_mfma_f32_32x32x16_bf16 v[224:239], v[124:127], %[q], v[192:207]\n\t" "ds_read_b128 %[q], %[qa]\n\t" \
  "v_exp_f32 v213, v213\n\t" "v_exp_f32 v214, v214\n\t" "v_exp_f32 v215, v215\n\t" "v_exp_f32 v216, v216\n\t" "v_exp_f32 v217, v217\n\t" \
  "v_smfmac_f32_32x32x32_bf16 v[240:255], v[176:179], v[208:215], %[idx]\n\t" "v_exp_f32 v218, v218\n\t" "v_exp_f32 v219, v219\n\t" "v_exp_f32 v220, v220\n\t" \
  "v_exp_f32 v221, v221\n\t" "v_exp_f32 v222, v222\n\t" "v_exp_f32 v223, v223\n\t" "v_exp_f32 v224, v224\n\t" "v_exp_f32 v225, v225\n\t" \
  "v_smfmac_f32_32x32x32_bf16 v[240:255], v[180:183], v[216:223], %[idx]\n\t" "v_exp_f32 v226, v226\n\t" "v_exp_f32 v227, v227\n\t" "v_exp_f32 v228, v228\n\t" \
  "s_waitcnt lgkmcnt(0)\n\t" "v_mfma_f32_32x32x16_bf16 v[208:223], v[96:99], %[q], v[192:207]\n\t" "v_exp_f32 v229, v229\n\t" "v_exp_f32 v230, v230\n\t" \
  "v_exp_f32 v231, v231\n\t" "v_exp_f32 v232, v232\n\t" "v_exp_f32 v233, v233\n\t" "v_smfmac_f32_32x32x32_bf16 v[240:255], v[184:187], v[224:231], %[idx]\n\t" \
  "v_exp_f32 v234, v234\n\t" "v_exp_f32 v235, v235\n\t" "v_exp_f32 v236, v236\n\t" "v_exp_f32 v237, v237\n\t" "v_exp_f32 v238, v238\n\t" "v_exp_f32 v239, v239\n\t" \
  "v_exp_f32 v208, v208\n\t" "v_exp_f32 v209, v209\n\t" "v_smfmac_f32_32x32x32_bf16 v[240:255], v[188:191], v[232:239], %[idx]\n\t" "v_exp_f32 v210, v210\n\t" \
  "v_exp_f32 v211, v211\n\t" "v_exp_f32 v212, v212\n\t" "v_mfma_f32_32x32x16_bf16 v[224:239], v[100:103], %[q], v[192:207]\n\t" "v_exp_f32 v213, v213\n\t" \
  "v_exp_f32 v214, v214\n\t" "v_exp_f32 v215, v215\n\t" "v_exp_f32 v216, v216\n\t" "v_exp_f32 v217, v217\n\t" "v_exp_f32 v218, v218\n\t" "v_exp_f32 v219, v219\n\t" \
  "v_exp_f32 v220, v220\n\t" ""
#define ROWMAX_ASM \
  "s_nop 1\n\t" "v_mfma_f32_32x32x16_bf16 v[224:239], v[96:99], %[q], 0\n\t" "s_nop 11\n\t" "v_max3_f32 %[m], %[m], v224, v225\n\t" \
  "v_max3_f32 %[m], %[m], v226, v227\n\t" "v_max3_f32 %[m], %[m], v228, v229\n\t" "v_max3_f32 %[m], %[m], v230, v231\n\t" "v_max3_f32 %[m], %[m], v232, v233\n\t" \
  "v_max3_f32 %[m], %[m], v234, v235\n\t" "v_max3_f32 %[m], %[m], v236, v237\n\t" "v_max3_f32 %[m], %[m], v238, v239\n\t" \
  "v_mfma_f32_32x32x16_bf16 v[224:239], v[100:103], %[q], 0\n\t" "s_nop 11\n\t" "v_max3_f32 %[m], %[m], v224, v225\n\t" "v_max3_f32 %[m], %[m], v226, v227\n\t" \
  "v_max3_f32 %[m], %[m], v228, v229\n\t" "v_max3_f32 %[m], %[m], v230, v231\n\t" "v_max3_f32 %[m], %[m], v232, v233\n\t" "v_max3_f32 %[m], %[m], v234, v235\n\t" \
  "v_max3_f32 %[m], %[m], v236, v237\n\t" "v_max3_f32 %[m], %[m], v238, v239\n\t" "v_mfma_f32_32x32x16_bf16 v[224:239], v[104:107], %[q], 0\n\t" "s_nop 11\n\t" \
  "v_max3_f32 %[m], %[m], v224, v225\n\t" "v_max3_f32 %[m], %[m], v226, v227\n\t" "v_max3_f32 %[m], %[m], v228, v229\n\t" "v_max3_f32 %[m], %[m], v230, v231\n\t" \
  "v_max3_f32 %[m], %[m], v232, v233\n\t" "v_max3_f32 %[m], %[m], v234, v235\n\t" "v_max3_f32 %[m], %[m], v236, v237\n\t" "v_max3_f32 %[m], %[m], v238, v239\n\t" \
  "v_mfma_f32_32x32x16_bf16 v[224:239], v[108:111], %[q], 0\n\t" "s_nop 11\n\t" "v_max3_f32 %[m], %[m], v224, v225\n\t" "v_max3_f32 %[m], %[m], v226, v227\n\t" \
  "v_max3_f32 %[m], %[m], v228, v229\n\t" "v_max3_f32 %[m], %[m], v230, v231\n\t" "v_max3_f32 %[m], %[m], v232, v233\n\t" "v_max3_f32 %[m], %[m], v234, v235\n\t" \
  "v_max3_f32 %[m], %[m], v236, v237\n\t" "v_max3_f32 %[m], %[m], v238, v239\n\t" "v_mfma_f32_32x32x16_bf16 v[224:239], v[112:115], %[q], 0\n\t" "s_nop 11\n\t" \
  "v_max3_f32 %[m], %[m], v224, v225\n\t" "v_max3_f32 %[m], %[m], v226, v227\n\t" "v_max3_f32 %[m], %[m], v228, v229\n\t" "v_max3_f32 %[m], %[m], v230, v231\n\t" \
  "v_max3_f32 %[m], %[m], v232, v233\n\t" "v_max3_f32 %[m], %[m], v234, v235\n\t" "v_max3_f32 %[m], %[m], v236, v237\n\t" "v_max3_f32 %[m], %[m], v238, v239\n\t" \
  "v_mfma_f32_32x32x16_bf16 v[224:239], v[116:119], %[q], 0\n\t" "s_nop 11\n\t" "v_max3_f32 %[m], %[m], v224, v225\n\t" "v_max3_f32 %[m], %[m], v226, v227\n\t" \
  "v_max3_f32 %[m], %[m], v228, v229\n\t" "v_max3_f32 %[m], %[m], v230, v231\n\t" "v_max3_f32 %[m], %[m], v232, v233\n\t" "v_max3_f32 %[m], %[m], v234, v235\n\t" \
  "v_max3_f32 %[m], %[m], v236, v237\n\t" "v_max3_f32 %[m], %[m], v238, v239\n\t" "v_mfma_f32_32x32x16_bf16 v[224:239], v[120:123], %[q], 0\n\t" "s_nop 11\n\t" \
  "v_max3_f32 %[m], %[m], v224, v225\n\t" "v_max3_f32 %[m], %[m], v226, v227\n\t" "v_max3_f32 %[m], %[m], v228, v229\n\t" "v_max3_f32 %[m], %[m], v230, v231\n\t" \
  "v_max3_f32 %[m], %[m], v232, v233\n\t" "v_max3_f32 %[m], %[m], v234, v235\n\t" "v_max3_f32 %[m], %[m], v236, v237\n\t" "v_max3_f32 %[m], %[m], v238, v239\n\t" \
  "v_mfma_f32_32x32x16_bf16 v[224:239], v[124:127], %[q], 0\n\t" "s_nop 11\n\t" "v_max3_f32 %[m], %[m], v224, v225\n\t" "v_max3_f32 %[m], %[m], v226, v227\n\t" \
  "v_max3_f32 %[m], %[m], v228, v229\n\t" "v_max3_f32 %[m], %[m], v230, v231\n\t" "v_max3_f32 %[m], %[m], v232, v233\n\t" "v_max3_f32 %[m], %[m], v234, v235\n\t" \
  "v_max3_f32 %[m], %[m], v236, v237\n\t" "v_max3_f32 %[m], %[m], v238, v239\n\t" ""
#define POST_ASM \
  "s_add_u32 %[i], %[i], 1\n\t" "s_cmp_eq_u32 %[i], 8\n\t" "s_cbranch_scc1 .Lfb_out%=\n\t" "v_cndmask_b32 %[t1], %[sh], v248, %[lo]\n\t" \
  "v_add_u32 %[t2], %[so], %[l16]\n\t" "ds_write_b128 %[t2], v[240:243]\n\t" "ds_write_b128 %[t2], v[244:247] offset:1024\n\t" "v_add_u32 %[t2], %[so], %[l4]\n\t" \
  "ds_write_b32 %[t2], %[t1] offset:2048\n\t" "s_add_u32 %[so], %[so], 14080\n\t" "s_and_b32 %[so], %[so], 0x1ffff\n\t" "s_add_u32 %[qo], %[qo], 1024\n\t" \
  "s_and_b32 %[qo], %[qo], 0x1fff\n\t" "v_add_u32 %[qa], %[qo], %[qb]\n\t" ""
#define WOLOAD_ASM \
  "s_cmp_lg_u32 %[i], %[iw]\n\t" "s_cbranch_scc1 .Lfb_skip%=\n\t" "s_mov_b64 %[wb2], %[wb]\n\t" "global_load_dwordx4 v[64:67], %[ro], %[wb2]\n\t" \
  "v_add_u32 %[ro], 0x2000, %[ro]\n\t" "global_load_dwordx4 v[68:71], %[ro], %[wb2]\n\t" "v_add_u32 %[ro], 0x2000, %[ro]\n\t" \
  "global_load_dwordx4 v[72:75], %[ro], %[wb2]\n\t" "v_add_u32 %[ro], 0x2000, %[ro]\n\t" "global_load_dwordx4 v[76:79], %[ro], %[wb2]\n\t" \
  "v_add_u32 %[ro], 0x2000, %[ro]\n\t" "global_load_dwordx4 v[80:83], %[ro], %[wb2]\n\t" "v_add_u32 %[ro], 0x2000, %[ro]\n\t" \
  "global_load_dwordx4 v[84:87], %[ro], %[wb2]\n\t" "v_add_u32 %[ro], 0x2000, %[ro]\n\t" "global_load_dwordx4 v[88:91], %[ro], %[wb2]\n\t" \
  "v_add_u32 %[ro], 0x2000, %[ro]\n\t" "global_load_dwordx4 v[92:95], %[ro], %[wb2]\n\t" ""

#define KV_OUT "=&{v[96:127]}"(kv0), "=&{v[128:159]}"(kv1), "=&{v[160:191]}"(kv2)
#define KV_IO "+{v[96:127]}"(kv0), "+{v[128:159]}"(kv1), "+{v[160:191]}"(kv2)
#define KV_IN "{v[96:127]}"(kv0), "{v[128:159]}"(kv1), "{v[160:191]}"(kv2)
#define KV_ADDR [ka0] "v"(kb), [ka1] "v"(kb + 4096), [va0] "v"(vp), [va1] "v"(vp + 4096), [rs] "s"(rs)

__global__ __launch_bounds__(512, 2) void fused_block(const u16* __restrict__ x, const u16* __restrict__ wq, const u16* __restrict__ wk,
                                                      const u16* __restrict__ wv, const u16* __restrict__ wo, const u16* __restrict__ wr,
                                                      const u16* __restrict__ wg, const u16* __restrict__ wu, const u16* __restrict__ wd,
                                                      char* ws, u16* __restrict__ out) {
  __shared__ __attribute__((aligned(16))) char smem[L_TOTAL];
  unsigned pf1, pf2;
  {
    unsigned long long pc; unsigned t;
    asm volatile("s_getpc_b64 %[pc]\n.Lfb_begin:\n\tv_readfirstlane_b32 %[t], %[tid]\n\ts_cmp_lg_u32 %[t], 0\n\ts_cbranch_scc1 .Lfb_nopf\n\t"
                 "v_and_b32 %[a], 15, %[tid]\n\tv_lshlrev_b32 %[b], 7, %[a]\n\tglobal_load_dword %[b], %[b], %[pc]\n\t"
                 "s_lshr_b32 %[t], %[bx], 3\n\ts_mul_i32 %[t], %[t], 1152\n\ts_add_u32 %[t], %[t], 2048\n\t"
                 "v_min_u32 %[a], 8, %[a]\n\tv_lshl_add_u32 %[a], %[a], 7, %[t]\n\tv_min_u32 %[a], .Lfb_end-.Lfb_begin, %[a]\n\t"
                 "global_load_dword %[a], %[a], %[pc]\n.Lfb_nopf:"
                 : [pc] "=&s"(pc), [t] "=&s"(t), [a] "=&v"(pf1), [b] "=&v"(pf2) : [tid] "v"(threadIdx.x), [bx] "s"(blockIdx.x) : "scc", "memory");
  }
  char* const XI = smem + L_XI;
  char* const WI = smem + L_WI;
  char* const KI = smem + L_KI;
  char* const CTL = smem + L_CTL;
  const int tid = threadIdx.x, lane = tid & 63;
  const int w = __builtin_amdgcn_readfirstlane(tid >> 6);
  const int b = blockIdx.x, g8 = b & 7, tb = b & 31, hg = b >> 5;
  const int rr = lane >> 3, c = lane & 7;
  const int h = lane >> 5, l31 = lane & 31;
  const int isV = hg & 1, lr0 = (hg >> 1) * 16;
  const int pid = tb * 8 + hg;
  const int u = (b & 7) * 28 + (b >> 3);
  const rsrc_t rs = __builtin_amdgcn_make_buffer_rsrc(ws, 0, (int)WS_END, 0x00020000);
  STAMP(0, 0);

  unsigned tag;
  asm volatile("s_load_dword %0, %1, 0x0" : "=s"(tag) : "s"(ws) : "memory");
  const bool kvw = (w == (hg & 6));
  const bool ldw = (w == (hg | 1));
  u32x4 xo0, xo1, wr0, wr1, xe, xv[8];
  f32x32 wov;
  unsigned pf0 = 0;
  {
    const u16* p = x + (size_t)(tb * 64 + lr0 + 2 * w) * D_;
    GLD128(xo0, h * 1024 + l31 * 16, p);
    GLD128(xo1, h * 1024 + l31 * 16 + 512, p);
  }
#pragma unroll
  for (int i = 0; i < 2; ++i) glds16((isV ? wv : wk) + (w * 2 + i) * 512, lane * 16, KI + (w * 2 + i) * 1024);
  const unsigned row_off = rr * 1024 + c * 16;
  const unsigned wq_off = rr * 1024 + ((c ^ (((rr >> 1) & 1) << 2)) << 4);
  const u16* xrows = x + (size_t)(tb * 64 + w * 8) * D_;
  if (kvw) {
    __builtin_amdgcn_s_setprio(3);
  {
    unsigned long long ta = (unsigned long long)ws + (lane == 0 ? WS_YP + b * 5376 : (lane == 1 ? WS_K : WS_KVF));
    if (lane == 3) ta = (unsigned long long)(out + (size_t)tb * 64 * D_);
    asm volatile("global_load_dword %0, %1, off" : "=v"(pf0) : "v"(ta) : "memory");
  }
#pragma unroll
    for (int j = 0; j < 8; ++j)
      st128(rs, WS_O + (unsigned)((tb * 2 + (j >> 2)) * 32 + hg * 4 + (j & 3)) * 1024 + lane * 16, u32x4{0xffffffffu, 0xffffffffu, 0xffffffffu, 0xffffffffu});
  } else if (ldw) {
#pragma unroll
    for (int pc = 0; pc < 8; ++pc) glds16(wq + pc * 8 * D_ + hg * 64, wq_off, WI + pc * 1024);
  } else {
#pragma unroll
    for (int i = 0; i < 8; ++i) asm volatile("global_load_dwordx4 %0, %1, %2 offset:%3" : "=v"(xv[i]) : "v"(row_off), "s"(xrows), "i"(i * 128) : "memory");
  }
  VMWAIT(10);
  asm volatile("" : "+v"(xo0), "+v"(xo1), "+v"(pf1), "+v"(pf2));
  STAMP(0, 1);
  {
    float mean, var;
    stats32(xo0, xo1, mean, var);
    asm volatile("" : "+v"(xo0), "+v"(xo1));
    const float mb = rbf(mean), sd = ln_sd(var), inv = __builtin_amdgcn_rcpf(sd);
#pragma unroll
    for (int j = 0; j < 4; ++j) { xo0[j] = ln_pair(xo0[j], mb, inv); xo1[j] = ln_pair(xo1[j], mb, inv); }
    const int lrow = lr0 + 2 * w + h;
    char* xr = XI + lrow * 1024 + ((l31 ^ (lrow & 15)) << 4);
    *(u32x4*)xr = xo0; *(u32x4*)(xr + 512) = xo1;
    if (l31 == 0) *(f32x2*)(CTL + C_STAT + lrow * 8) = f32x2{mb, sd};
    if (tid < 2) *(unsigned*)(CTL + tid * 4) = 0u;
    if (kvw) VMWAIT(9); else VMWAIT(8);
    asm volatile("s_waitcnt lgkmcnt(0)" : "+s"(tag) :: "memory");
    __builtin_amdgcn_s_barrier();
    CBAR();
  }
  tag += 1u;
  STAMP(0, 2);
  if ((w >> 2) == isV) { GLD128(wr0, l31 * 16, wr); GLD128(wr1, l31 * 16 + 512, wr); }
  if (kvw) {
    const int g = lane >> 4, q = (lane & 15) >> 2, p = lane & 3;
    const char* xb = XI + (lr0 + (lane & 15)) * 1024;
    const char* wb = opq(KI + (8 * g + q) * 32 + p * 8);
    f32x4 acc = {}, act = {};
    bf16x8 fx[2][4], fw[2][4];
#define KV_LD(bf, k0) _Pragma("unroll") for (int i = 0; i < 4; ++i) { const int ks = (k0) + i; \
      fx[bf][i] = *(const bf16x8*)(xb + ((((4 * i + g) ^ (lane & 15)) << 4) + (ks >> 2) * 256)); \
      fw[bf][i] = tr_frag(wb + ks * 1024, wb + ks * 1024 + 128); }
    KV_LD(0, 0)
#pragma unroll
    for (int kb = 0; kb < 4; ++kb) {
      if (kb < 3) { KV_LD((kb + 1) & 1, 4 * kb + 4) }
      SB();
#pragma unroll
      for (int i = 0; i < 4; ++i) {
        acc = __builtin_amdgcn_mfma_f32_16x16x32_bf16(fw[kb & 1][i], fx[kb & 1][i], acc, 0, 0, 0);
        act = __builtin_amdgcn_mfma_f32_16x16x32_bf16(fx[kb & 1][i], fw[kb & 1][i], act, 0, 0, 0);
      }
      SB();
    }
    if (isV) acc = act;
    const u32x2 pk = {pack_bf(acc[0], acc[1]), pack_bf(acc[2], acc[3])};
    float n = 0.f;
    if (!isV) {
      st64(rs, WS_K + (unsigned)(tb * 64 + lr0 + (lane & 15)) * 32 + (((g & 1) << 1) | (g >> 1)) * 8, pk);
      n = wave_maxpos(add32(add16(acc[0] * acc[0] + acc[1] * acc[1] + acc[2] * acc[2] + acc[3] * acc[3])));
    } else {
      st64(rs, WS_V + (unsigned)(pid - 1 + (g >> 1)) * VHALF + (lane & 15) * 16 + (g & 1) * 8, pk);
    }
    DRAIN();
    if (lane < 8) st64(rs, WS_KVF + (unsigned)(lane * 256 + pid) * 8, u32x2{__float_as_uint(n), tag});
    __builtin_amdgcn_s_setprio(0);
    STAMP(0, 6);
  } else if (ldw) {
#pragma unroll 4
    for (int pc = 8; pc < 28; ++pc) glds16(wq + pc * 8 * D_ + hg * 64, wq_off, WI + pc * 1024);
  } else {
    const int w6 = w - (w > hg ? 2 : 0);
#pragma unroll
    for (int i = 0; i < 6; ++i) { const int pc = 28 + i * 6 + w6; glds16(wq + pc * 8 * D_ + hg * 64, wq_off, WI + pc * 1024); }
    asm volatile("s_waitcnt vmcnt(6)" : "+v"(xv[0]), "+v"(xv[1]), "+v"(xv[2]), "+v"(xv[3]), "+v"(xv[4]), "+v"(xv[5]), "+v"(xv[6]), "+v"(xv[7]) :: "memory");
    float s = 0.f, q = 0.f;
    unsigned ones = 0x3f803f80u; asm volatile("" : "+v"(ones));
#pragma unroll
    for (int i = 0; i < 8; ++i)
#pragma unroll
      for (int j = 0; j < 4; ++j) { s = dot2(xv[i][j], ones, s); q = dot2(xv[i][j], xv[i][j], q); }
    const float mean = sum8(s) * (1.0f / 512.0f);
    float var = fmaf(-mean, mean, sum8(q) * (1.0f / 512.0f));
    if (__builtin_expect(__any(!(var >= mean * mean * 0.015625f)), 0)) {
      float v = 0.f;
#pragma unroll
      for (int i = 0; i < 8; ++i)
#pragma unroll
        for (int j = 0; j < 4; ++j) { const float a = bf_lo(xv[i][j]) - mean, bb = bf_hi(xv[i][j]) - mean; v = fmaf(a, a, v); v = fmaf(bb, bb, v); }
      var = sum8(v) * (1.0f / 512.0f);
    }
    asm volatile("" : "+v"(xv[0]), "+v"(xv[1]), "+v"(xv[2]), "+v"(xv[3]), "+v"(xv[4]), "+v"(xv[5]), "+v"(xv[6]), "+v"(xv[7]));
    const float st_m = rbf(mean), st_s = ln_sd(var);
    const float inv = __builtin_amdgcn_rcpf(st_s), nb = -st_m * inv;
    const int lrow = w * 8 + rr;
#pragma unroll
    for (int i = 0; i < 8; ++i) {
      u32x4 o;
#pragma unroll
      for (int j = 0; j < 4; ++j) o[j] = pack_bf(fmaf(bf_lo(xv[i][j]), inv, nb), fmaf(bf_hi(xv[i][j]), inv, nb));
      *(u32x4*)(XI + lrow * 1024 + (((i * 8 + c) ^ (lrow & 15)) << 4)) = o;
    }
    if (c == 0) *(f32x2*)(CTL + C_STAT + lrow * 8) = f32x2{st_m, st_s};
    STAMP(0, 3);
  }
  VMWAIT(20);
  asm volatile("" : "+v"(pf0), "+v"(wr0), "+v"(wr1));
  if ((w >> 2) == isV) {
    float r = 0.f;
#pragma unroll
    for (int j = 0; j < 4; ++j) { r = dot2(xo0[j], wr0[j], r); r = dot2(xo1[j], wr1[j], r); }
    const unsigned rkey = make_key(rbf(sum32(r)), pid * 8 + 2 * (w & 3) + h);
    if (l31 == 0) *(unsigned*)(CTL + C_R + (2 * (w & 3) + h) * 4) = rkey;
  }
  if (!kvw) DRAIN();
  RAW_BARRIER();
  STAMP(0, 4);
  if (w < 4) __builtin_amdgcn_s_setprio(2); else __builtin_amdgcn_s_setprio(1);
  if (w == 4) {
    const unsigned key = lane < 8 ? *(const unsigned*)(CTL + C_R + lane * 4) : 0u;
    const unsigned k1 = wave_maxu(key), k2 = wave_maxu(key == k1 ? 0u : key);
    if (lane < 8) st128(rs, WS_CAND + (unsigned)(lane * 256 + pid) * 16, u32x4{k1, tag, k2, tag});
  }
  const int blk = (lane >> 4) & 1, q4 = (lane & 15) >> 2, p4 = lane & 3;
  if (w < 4) {
    const int nt = w & 1, tt = w >> 1;
    const int tok = tt * 32 + l31;
    const char* xb = XI + tok * 1024;
    const char* wb = WI + (8 * h + q4) * 128 + (((4 * nt + 2 * blk + (p4 >> 1)) ^ (((q4 >> 1) & 1) << 2)) << 4) + 8 * (p4 & 1);
    f32x16 acc = {};
    bf16x8 fa[2][4], fb[2][4];
#define Q_LD(bf, k0) _Pragma("unroll") for (int i = 0; i < 4; ++i) { const int ks = (k0) + i; \
      fa[bf][i] = tr_frag(wb + ks * 2048, wb + ks * 2048 + 512); \
      fb[bf][i] = *(const bf16x8*)(xb + ((((2 * (ks & 7) + h) ^ (tok & 15)) << 4) + (ks >> 3) * 256)); }
    Q_LD(0, 0)
    SB();
#pragma unroll
    for (int kb = 0; kb < 8; ++kb) {
      if (kb < 7) { Q_LD((kb + 1) & 1, 4 * kb + 4) }
#pragma unroll
      for (int i = 0; i < 4; ++i) acc = __builtin_amdgcn_mfma_f32_32x32x16_bf16(fa[kb & 1][i], fb[kb & 1][i], acc, 0, 0, 0);
#pragma unroll
      for (int i = 0; i < 4; ++i) { SGB(0x8, 1); SGB(0x100, 3); }
      SB();
    }
    const float cs = 0.25f * 1.4426950408889634f;
    float n0 = 0.f, n1 = 0.f;
#pragma unroll
    for (int r = 0; r < 8; ++r) { acc[r] *= cs; acc[r + 8] *= cs; n0 = fmaf(acc[r], acc[r], n0); n1 = fmaf(acc[r + 8], acc[r + 8], n1); }
    char* qi = smem + L_QI + (tt * 4 + nt * 2) * 1024 + lane * 16;
    *(u32x4*)qi = u32x4{pack_bf(acc[0], acc[1]), pack_bf(acc[2], acc[3]), pack_bf(acc[4], acc[5]), pack_bf(acc[6], acc[7])};
    *(u32x4*)(qi + 1024) = u32x4{pack_bf(acc[8], acc[9]), pack_bf(acc[10], acc[11]), pack_bf(acc[12], acc[13]), pack_bf(acc[14], acc[15])};
    const float nm = wave_maxpos(fmaxf(add32(n0), add32(n1)));
    if (lane == 0) {
      *(float*)(CTL + C_QMAX + w * 4) = nm;
      asm volatile("s_waitcnt lgkmcnt(0)\n\tds_add_u32 %0, %1" :: "v"((unsigned)(uintptr_t)(LAS char*)(CTL + C_CNT)), "v"(1u) : "memory");
    }
  }
  STAMP(0, 5);

  xe = *(const u32x4*)((const char*)(xrows + hg * 64) + row_off);
  f32x32 kv0, kv1, kv2;
  asm volatile(VCONST_ASM : KV_OUT : [cst] "v"((lane & 15) ? 0u : 0x3f803f80u));
  float kmax;
  {
    u32x2 f;
    SPIN {
      f = ld64(rs, WS_KVF + (unsigned)(g8 * 256 + w * 32 + l31) * 8);
      if (__all(f[1] == tag)) break;
      SLEEP(1);
    }
    CBAR();
    kmax = __uint_as_float(f[0]);
  }
  STAMP(1, 0);
  {
    const unsigned kb = WS_K + (unsigned)(w * 256 + l31) * 32 + h * 16;
    const unsigned vp = WS_V + (unsigned)(w * 32 + 2 * blk + h) * VHALF + (lane & 15) * 16;
    asm volatile(LOADS_ASM : KV_IO : KV_ADDR : "memory");
  }
  kmax = wave_maxpos(kmax);
  u32x4 gatep;
  {
    const f32x2 st = *(const f32x2*)(CTL + C_STAT + (w * 8 + rr) * 8);
    const float inv = __builtin_amdgcn_rcpf(st[1]);
#pragma unroll
    for (int j = 0; j < 4; ++j) gatep[j] = silu_pair(ln_pair(xe[j], st[0], inv));
    asm volatile("" : "+v"(gatep));
  }
  lds_wait_ge(CTL + C_CNT, 4u);
  STAMP(1, 1);
  const f32x4 qn = *(const f32x4*)(CTL + C_QMAX);
  const float qmax = fmaxf(fmaxf(qn[0], qn[1]), fmaxf(qn[2], qn[3]));
  const float bound = __builtin_amdgcn_sqrtf(qmax * kmax) * 1.02f + 0.01f;
  int fasti = __builtin_amdgcn_readfirstlane(bound <= 48.0f ? 1 : 0);
  asm volatile("" : "+s"(fasti));
  const bool fast = fasti != 0;
  const int idx = (int)0xDDDDDDDDu;
  bf16x8 zf = {};
  asm volatile("" : "+v"(zf));
  const f32x16 z16 = {};
  const char* qbase = smem + L_QI + lane * 16;
  f32x16 o;
  float shift = bound;
  bf16x8 qf = *(const bf16x8*)(qbase + ((w + 1) & 7) * 1024);
  const unsigned lds0 = (unsigned)(uintptr_t)(LAS char*)smem;
  unsigned qa = lds0 + L_QI + lane * 16 + ((w + 2) & 7) * 1024;
  f32x16 cc = splat16(-bound);
  f32x32 sab;
  int i = 0;
  unsigned so = ((w + 1) & 7) * PREG + 6 * PSLOT, qo = ((w + 2) & 7) * 1024; float t1_; unsigned t2_;
#define RUN_OUT "=&{v[240:255]}"(o), "+{v[192:207]}"(cc), [q] "+v"(qf), [qa] "+v"(qa), [i] "+s"(i), [so] "+s"(so), [qo] "+s"(qo), [t1] "=&v"(t1_), [t2] "=&v"(t2_)
#define RUN_IN [z] "v"(zf), [idx] "v"(idx), [sh] "v"(shift), [lo] "s"(0xffffffffull), [l16] "v"(lds0 + lane * 16), [l4] "v"(lds0 + lane * 4), \
               [qb] "v"(lds0 + L_QI + lane * 16)
  STAMP(3, 0);
  asm volatile("s_cmp_eq_u32 %[fast], 0\n\ts_cbranch_scc1 .Lfb_slow0\n\t" PROLOG0_ASM PASS0_ASM POST_ASM
               "s_branch .Lfb_out%=\n.Lfb_slow0:\n\ts_waitcnt vmcnt(0)\n\t" SWAPS_ASM "\n.Lfb_out%=:"
               : "=&{v[208:239]}"(sab), RUN_OUT, KV_IO : RUN_IN, [fast] "s"(fasti) : "scc", "memory");

  if (w == 2) {
        int lane = tid & 63; asm volatile("" : "+v"(lane));
        unsigned m1 = 0, m2 = 0;
        SPIN {
          bool ok = true; m1 = 0; m2 = 0;
#pragma unroll
          for (int j = 0; j < 4; ++j) {
            const u32x4 cd = ld128(rs, WS_CAND + (unsigned)(g8 * 256 + j * 64 + lane) * 16);
            ok = ok && cd[1] == tag && cd[3] == tag;
            m2 = max(max(m2, cd[2]), min(m1, cd[0])); m1 = max(m1, cd[0]);
          }
          if (__all(ok)) break;
          SLEEP(1);
        }
        CBAR();
        const unsigned k1 = __builtin_amdgcn_readfirstlane(wave_maxu(m1));
        const unsigned k2 = __builtin_amdgcn_readfirstlane(wave_maxu(m1 == k1 ? m2 : m1));
        if (lane == 0) *(u32x2*)(CTL + C_K1) = u32x2{k1, k2};
        if (b < NU) {
          const int h = lane >> 5, l31 = lane & 31;
          const u16* xr = x + key_tok(h ? k2 : k1) * D_ + l31 * 8;
        {
          const u32x4 xs0 = *(const u32x4*)xr, xs1 = *(const u32x4*)(xr + 256);
          float mean, var;
          stats32(xs0, xs1, mean, var);
          const float mb = rbf(mean), inv = __builtin_amdgcn_rcpf(ln_sd(var));
          u32x4 o0;
#pragma unroll
          for (int j = 0; j < 4; ++j) o0[j] = ln_pair((u & 4) ? xs1[j] : xs0[j], mb, inv);
          *(u32x4*)(smem + L_XS + h * 1024 + l31 * 16) = o0;
          asm volatile("s_waitcnt lgkmcnt(0)" ::: "memory");
          if (lane == 0) *(volatile LAS unsigned*)(CTL + C_SEL) = 1u;
        }
        }
  }
  if (w == 0 && b < NU) {
    int ln = tid; asm volatile("" : "+v"(ln));
    const unsigned go = (ln >> 3) * (F_ * 2) + (((ln & 7) ^ (((ln >> 4) & 1) << 2)) << 4);
#pragma unroll
    for (int j = 0; j < 16; ++j) glds16((j < 8 ? wg : wu) + ((u & 7) * 64 + (j & 7) * 8) * F_ + (u >> 3) * 64, go, smem + L_GU + j * 1024);
  }
  if (w == 1 && b < NU) {
#pragma unroll
    for (int f = 0; f < 8; ++f) glds16(wd + (u * 8 + f) * D_, lane * 16, smem + (f < 6 ? L_DN + f * 1088 : L_DN2 + (f - 6) * 1088));
  }
  asm volatile("" : "=&{v[64:95]}"(wov));
  const int iw = (0x33222453 >> (4 * w)) & 15;
  unsigned ro = row_off;
  unsigned long long wb2_;

#pragma nounroll
  while (i < 8) {
    if (b < NU && ((w == 0 && i == 2) || (w == 1 && i == 4))) {
      int lane = tid & 63, u = (b & 7) * 28 + (b >> 3);
      asm volatile("" : "+v"(lane), "+s"(u));
      const int h = lane >> 5, l31 = lane & 31, blk = (lane >> 4) & 1, q4 = (lane & 15) >> 2, p4 = lane & 3;
      if (i == 2) {
        DRAIN();
        lds_wait_ge(CTL + C_SEL, 1u);
        const char* xb = opq(smem + L_XS + (lane & 1) * 1024 + (u & 3) * 128 + h * 16);
        bf16x8 fx[4];
#pragma unroll
        for (int ks = 0; ks < 4; ++ks) fx[ks] = *(const bf16x8*)(xb + ks * 32);
#pragma unroll
        for (int nt = 0; nt < 2; ++nt) {
          const char* wb = opq(smem + L_GU + (8 * h + q4) * 128 + (((4 * nt + 2 * blk + (p4 >> 1)) ^ (((q4 >> 1) & 1) << 2)) << 4) + 8 * (p4 & 1));
#pragma unroll
          for (int mat = 0; mat < 2; ++mat) {
            f32x16 z = z16;
#pragma unroll
            for (int ks = 0; ks < 4; ++ks) z = QK(fx[ks], tr_frag(wb + mat * 8192 + ks * 2048, wb + mat * 8192 + ks * 2048 + 512), z);
            if (lane < 32) st128(rs, WS_P1 + (unsigned)u * 2048 + (mat * 64 + nt * 32 + lane) * 16, u32x4{__float_as_uint(z[0]), tag, __float_as_uint(z[1]), tag});
            SB();
          }
        }
      } else {
        const unsigned po = WS_P1 + (unsigned)((u & ~7) + (lane >> 3)) * 2048 + ((u & 7) * 8 + (lane & 7)) * 16;
        u32x4 pg, pu;
        SPIN {
          pg = ld128(rs, po); pu = ld128(rs, po + 1024);
          if (__all(pg[1] == tag && pg[3] == tag && pu[1] == tag && pu[3] == tag)) break;
          SLEEP(2);
        }
        DRAIN();
        float t[4] = {__uint_as_float(pg[0]), __uint_as_float(pg[2]), __uint_as_float(pu[0]), __uint_as_float(pu[2])};
#pragma unroll
        for (int j = 0; j < 4; ++j) { t[j] += dpp_f<0x128>(t[j]); t[j] = add32(add16(t[j])); }
        const unsigned hp = pack_bf(rbf(silu_f(rbf(t[0]))) * rbf(t[2]), rbf(silu_f(rbf(t[1]))) * rbf(t[3]));
        if (lane < 8) { *(u16*)(smem + L_HS + lane * 2) = (u16)hp; *(u16*)(smem + L_HS + 16 + lane * 2) = (u16)(hp >> 16); }
        u32x4 ha = *(const u32x4*)(smem + L_HS + (lane & 1) * 16);
        if (lane >= 2) ha = u32x4{0u, 0u, 0u, 0u};
        const bf16x8 A = __builtin_bit_cast(bf16x8, ha);
        const int co = (2 * blk + (p4 >> 1)) * 16 + 8 * (p4 & 1);
        const char* db0 = opq(smem + L_DN + q4 * 1088 + co);
        const char* db1 = opq(smem + (q4 < 2 ? L_DN + (4 + q4) * 1088 : L_DN2 + (q4 - 2) * 1088) + co);
#pragma unroll 2
        for (int ct = 0; ct < 16; ++ct) {
          const bf16x8 bw = tr_frag(db0 + ct * 64, db1 + ct * 64);
          const f32x16 z = QK(A, bw, z16);
          auto sw = __builtin_amdgcn_permlane32_swap(__float_as_uint(z[0]), __float_as_uint(z[1]), false, false);
          st32(rs, WS_YP + (unsigned)(h * 64 + ct * 4 + (l31 >> 3)) * (NU * 32) + u * 32 + (l31 & 7) * 4, sw[0]);
        }
        DRAIN();
        if (lane < 8) st32(rs, WS_YF + lane * 1024 + u * 4, tag);
      }
    }
  if (w < 4 && i == 7 && b < NU) __builtin_amdgcn_s_setprio(0);
    STAMP(3, i);
    int stop = 8;
    if (b < NU) {
      if (w < 4 && i < 7) stop = 7;
      if (w == 0 && i < 2) stop = 2;
      if (w == 1 && i < 4) stop = 4;
    }
    if (!fast) {
      stop = i + 1;
      float m = -3.0e38f;
      asm volatile(ROWMAX_ASM : [m] "+v"(m), "=&{v[208:239]}"(sab) : [q] "v"(qf), KV_IN);
      shift = max32(m);
      cc = splat16(-shift);
      asm volatile(PROLOG_ASM : "=&{v[208:239]}"(sab), "+{v[192:207]}"(cc) : [q] "v"(qf), KV_IN);
    }
    asm volatile(".Lfb_loop%=:\n\t" PASSN_ASM POST_ASM WOLOAD_ASM "\n.Lfb_skip%=:\n\ts_cmp_lt_u32 %[i], %[stop]\n\ts_cbranch_scc1 .Lfb_loop%=\n.Lfb_out%=:"
                 : "+{v[208:239]}"(sab), RUN_OUT, "+{v[64:95]}"(wov), [ro] "+v"(ro), [wb2] "=&s"(wb2_)
                 : RUN_IN, [stop] "s"(stop), [iw] "s"(iw), [wb] "s"(wo + (size_t)(w * 64) * D_ + hg * 64), KV_IN : "scc", "memory");
  }
  {
  int tid = threadIdx.x; asm volatile("" : "+v"(tid));
  const int lane = tid & 63, rr = lane >> 3, c = lane & 7, h = lane >> 5, l31 = lane & 31, blk = (lane >> 4) & 1, q4 = (lane & 15) >> 2, p4 = lane & 3;
  if (w < 4 && b >= NU) {
    SPIN {
      const u32x4 f = ld128(rs, WS_YF + g8 * 1024 + min(lane, 55) * 16);
      if (__all(f[0] == tag && f[1] == tag && f[2] == tag && f[3] == tag)) break;
      SLEEP(8);
    }
    CBAR();
    const unsigned rw = (unsigned)((b - NU) * 4 + w);
    const unsigned yo = WS_YP + rw * (NU * 32) + lane * 16;
    f32x4 y = {};
#pragma unroll
    for (int j = 0; j < 7; ++j) y += __builtin_bit_cast(f32x4, ld128(rs, yo + j * 1024));
#pragma unroll
    for (int j = 0; j < 4; ++j) { float t = y[j]; t += dpp_f<0x4E>(t); t += dpp_f<0x124>(t); t += dpp_f<0x128>(t); y[j] = add32(add16(t)); }
    if (lane < 2) {
      st128(rs, WS_YG + rw * 64 + lane * 32, u32x4{__float_as_uint(y[0]), tag, __float_as_uint(y[1]), tag});
      st128(rs, WS_YG + rw * 64 + lane * 32 + 16, u32x4{__float_as_uint(y[2]), tag, __float_as_uint(y[3]), tag});
    }
  }
  __builtin_amdgcn_s_setprio(0);
  DRAIN();
  const u32x4 xe = *(const u32x4*)(x + (size_t)(tb * 64 + (tid >> 3)) * D_ + hg * 64 + c * 8);
  STAMP(1, 2);
  RAW_BARRIER();
  STAMP(1, 3);
  const unsigned fr = (unsigned)((tb * 2 + (w >> 2)) * 32 + hg * 4 + (w & 3));
  {
    float a[8], pl[7], pm[7];
    auto sw = __builtin_amdgcn_permlane32_swap(__float_as_uint(o[8]), __float_as_uint(o[8]), false, false);
    float l = __uint_as_float(sw[0]);
    float mx = shift;
#pragma unroll
    for (int sidx = 0; sidx < 7; ++sidx) {
      const char* sl = smem + w * PREG + sidx * PSLOT + 2048 + l31 * 4;
      pl[sidx] = *(const float*)sl; pm[sidx] = *(const float*)(sl + 128);
      mx = fmaxf(mx, pm[sidx]);
    }
    const float f0 = __builtin_amdgcn_exp2f(shift - mx);
    l *= f0;
#pragma unroll
    for (int r = 0; r < 8; ++r) a[r] = o[r] * f0;
#pragma unroll
    for (int sidx = 0; sidx < 7; ++sidx) {
      const char* sl = smem + w * PREG + sidx * PSLOT;
      const f32x4 p0 = *(const f32x4*)(sl + lane * 16), p1 = *(const f32x4*)(sl + 1024 + lane * 16);
      const float f = __builtin_amdgcn_exp2f(pm[sidx] - mx);
#pragma unroll
      for (int r = 0; r < 4; ++r) { a[r] = fmaf(p0[r], f, a[r]); a[r + 4] = fmaf(p1[r], f, a[r + 4]); }
      l = fmaf(pl[sidx], f, l);
    }
    const float inv = __builtin_amdgcn_rcpf(l);
    const u32x4 of = {min(pack_bf(a[0] * inv, a[1] * inv), 0xfffffffeu), min(pack_bf(a[2] * inv, a[3] * inv), 0xfffffffeu),
                      min(pack_bf(a[4] * inv, a[5] * inv), 0xfffffffeu), min(pack_bf(a[6] * inv, a[7] * inv), 0xfffffffeu)};
    st128(rs, WS_O + fr * 1024 + lane * 16, of);
    *(u32x4*)(smem + L_QI + w * 1024 + lane * 16) = of;
  }
  asm volatile("s_waitcnt lgkmcnt(0)\n\tds_write_b128 %0, v[64:67]\n\tds_write_b128 %0, v[68:71] offset:1024\n\tds_write_b128 %0, v[72:75] offset:2048\n\t"
               "ds_write_b128 %0, v[76:79] offset:3072\n\tds_write_b128 %0, v[80:83] offset:4096\n\tds_write_b128 %0, v[84:87] offset:5120\n\t"
               "ds_write_b128 %0, v[88:91] offset:6144\n\tds_write_b128 %0, v[92:95] offset:7168"
               :: "v"((unsigned)(uintptr_t)(LAS char*)smem + w * PREG + rr * 128 + ((c ^ (((rr >> 1) & 1) << 2)) << 4)), "{v[64:95]}"(wov) : "memory");
  if (b == 0 && tid == 0) st32(rs, WS_EPOCH, tag);
  STAMP(1, 4);
  RAW_BARRIER();
  const int etok = tid >> 3, gtok = tb * 64 + etok;
  {
    const int tt = w >> 2, kq = w & 3;
    bf16x8 fa[2][8];
#pragma unroll
    for (int nt = 0; nt < 2; ++nt) {
      const char* wb = opq(smem + 2 * kq * PREG + (4 * h + q4) * 128 + (((4 * nt + 2 * blk + (p4 >> 1)) ^ (((q4 >> 1) & 1) << 2)) << 4) + 8 * (p4 & 1));
#pragma unroll
      for (int i = 0; i < 8; ++i) fa[nt][i] = tr_frag(wb + (i >> 2) * PREG + (i & 3) * 2048, wb + (i >> 2) * PREG + (i & 3) * 2048 + 1024);
    }
    SB();
    const unsigned fo = WS_O + (unsigned)((tb * 2 + tt) * 32 + kq * 8) * 1024 + lane * 16;
    u32x4 fb[8];
    if (hg != 2 * kq) {
#pragma unroll
      for (int i = 0; i < 4; ++i) fb[i] = ld128(rs, fo + i * 1024);
    } else {
#pragma unroll
      for (int i = 0; i < 4; ++i) fb[i] = *(const u32x4*)(smem + L_QI + (tt * 4 + i) * 1024 + lane * 16);
    }
    if (hg != 2 * kq + 1) {
#pragma unroll
      for (int i = 4; i < 8; ++i) fb[i] = ld128(rs, fo + i * 1024);
    } else {
#pragma unroll
      for (int i = 0; i < 4; ++i) fb[4 + i] = *(const u32x4*)(smem + L_QI + (tt * 4 + i) * 1024 + lane * 16);
    }
    SB();
    STAMP(2, 0);
    {
      const unsigned k1 = *(const unsigned*)(CTL + C_K1), k2 = *(const unsigned*)(CTL + C_K2);
      const int sel = (gtok == key_tok(k1)) ? 0 : ((gtok == key_tok(k2)) ? 1 : -1);
      if (__builtin_expect(__any(sel >= 0), 0)) {
        const f32x2 st = *(const f32x2*)(CTL + C_STAT + etok * 8);
        const float inv = __builtin_amdgcn_rcpf(st[1]);
        const float rsg = rbf(sigm_f(key_val(sel > 0 ? k2 : k1)));
        const unsigned go = WS_YG + (unsigned)((sel > 0 ? 512 : 0) + hg * 64 + c * 8) * 8;
        u32x4 gq[4];
        SPIN {
          bool ok = true;
#pragma unroll
          for (int j = 0; j < 4; ++j) { gq[j] = ld128(rs, go + j * 16); ok = ok && gq[j][1] == tag && gq[j][3] == tag; }
          if (__all(ok)) break;
          SLEEP(1);
        }
#pragma unroll
        for (int j = 0; j < 4; ++j) {
          const unsigned xn = ln_pair(xe[j], st[0], inv);
          const float u0 = rbf(rbf(__uint_as_float(gq[j][0])) * rsg), u1 = rbf(rbf(__uint_as_float(gq[j][2])) * rsg);
          if (sel >= 0) gatep[j] = silu_pair(pack_bf(bf_lo(xn) + u0, bf_hi(xn) + u1));
        }
      }
    }
    SB();
    STAMP(2, 1);
    f32x16 acc0 = {}, acc1 = {};
#define FB_MAX(i) max(max(fb[i][0], fb[i][1]), max(fb[i][2], fb[i][3]))
#pragma unroll
    for (int hf = 0; hf < 2; ++hf) {
      if (__builtin_expect(__any(max(max(FB_MAX(4 * hf), FB_MAX(4 * hf + 1)), max(FB_MAX(4 * hf + 2), FB_MAX(4 * hf + 3))) == 0xffffffffu), 0)) {
        _Pragma("nounroll") for (int tries = 0; tries < 64; ++tries) {
          unsigned bad = 0;
#pragma unroll
          for (int i = 4 * hf; i < 8; ++i) bad |= __any(FB_MAX(i) == 0xffffffffu) ? 1u << i : 0u;
          const unsigned need = bad & (15u << (4 * hf));
          if (!need) break;
          SPIN {
            const unsigned pv = ld32(rs, fo - lane * 16 + (lane & 7) * 1024 + 1020);
            if (!__any(pv == 0xffffffffu && ((need >> (lane & 7)) & 1u))) break;
            SLEEP(1);
          }
#pragma unroll
          for (int i = 4 * hf; i < 8; ++i) if ((bad >> i) & 1u) fb[i] = ld128(rs, fo + i * 1024);
        }
      }
#pragma unroll
      for (int i = 4 * hf; i < 4 * hf + 4; ++i) {
        acc0 = QK(fa[0][i], __builtin_bit_cast(bf16x8, fb[i]), acc0);
        acc1 = QK(fa[1][i], __builtin_bit_cast(bf16x8, fb[i]), acc1);
      }
      SB();
    }
    STAMP(2, 2);
    char* pr = smem + w * PREG + 8192 + l31 * 256;
#pragma unroll
    for (int g = 0; g < 4; ++g) {
      *(f32x4*)(pr + (((2 * g + h) ^ (l31 & 7)) << 4)) = f32x4{acc0[4 * g], acc0[4 * g + 1], acc0[4 * g + 2], acc0[4 * g + 3]};
      *(f32x4*)(pr + 128 + (((2 * g + h) ^ (l31 & 7)) << 4)) = f32x4{acc1[4 * g], acc1[4 * g + 1], acc1[4 * g + 2], acc1[4 * g + 3]};
    }
  }
  RAW_BARRIER();
  {
    f32x4 s0v = {}, s1v = {};
#pragma unroll
    for (int kq = 0; kq < 4; ++kq) {
      const char* pr = smem + ((w >> 2) * 4 + kq) * PREG + 8192 + (etok & 31) * 256 + (c >> 2) * 128;
      s0v += *(const f32x4*)(pr + ((((2 * c) & 7) ^ rr) << 4)); s1v += *(const f32x4*)(pr + ((((2 * c + 1) & 7) ^ rr) << 4));
    }
    u32x4 ov;
#pragma unroll
    for (int j = 0; j < 4; ++j) {
      const unsigned sv = j < 2 ? pack_bf(s0v[2 * j], s0v[2 * j + 1]) : pack_bf(s1v[2 * j - 4], s1v[2 * j - 3]);
      const unsigned pr2 = pack_bf(bf_lo(sv) * bf_lo(gatep[j]), bf_hi(sv) * bf_hi(gatep[j]));
      ov[j] = pack_bf(bf_lo(xe[j]) + bf_lo(pr2), bf_hi(xe[j]) + bf_hi(pr2));
    }
    *(u32x4*)(out + (size_t)gtok * D_ + hg * 64 + c * 8) = ov;
  }
  }
  asm volatile(".Lfb_end:");
  STAMP(2, 3);
}

extern "C" void kernel_launch(void* const* d_in, const int* in_sizes, int n_in, void* d_out, int out_size, void* d_ws, size_t ws_size,
                              hipStream_t stream) {
  fused_block<<<256, 512, 0, stream>>>((const u16*)d_in[0], (const u16*)d_in[1], (const u16*)d_in[2], (const u16*)d_in[3], (const u16*)d_in[4],
                                       (const u16*)d_in[6], (const u16*)d_in[7], (const u16*)d_in[8], (const u16*)d_in[9], (char*)d_ws, (u16*)d_out);
}
```

```cpp
#include <hip/hip_runtime.h>
#include <hip/hip_bf16.h>
#include <stdint.h>

typedef __attribute__((ext_vector_type(2))) __bf16 bf16x2;
typedef __attribute__((ext_vector_type(8))) __bf16 bf16x8;
typedef __attribute__((ext_vector_type(4))) short s16x4;
typedef __attribute__((ext_vector_type(8))) short s16x8;
typedef __attribute__((ext_vector_type(2))) float f32x2;
typedef __attribute__((ext_vector_type(4))) float f32x4;
typedef __attribute__((ext_vector_type(16))) float f32x16;
typedef __attribute__((ext_vector_type(32))) float f32x32;
typedef __attribute__((ext_vector_type(2))) unsigned u32x2;
typedef __attribute__((ext_vector_type(4))) unsigned u32x4;
typedef unsigned short u16;
typedef __amdgpu_buffer_rsrc_t rsrc_t;
#define LAS __attribute__((address_space(3)))
#define DI __device__ __forceinline__

constexpr int S_ = 2048, D_ = 512, F_ = 1792;
constexpr int NU = 224;
constexpr int VHALF = 256;
constexpr int SC1 = 16;
constexpr unsigned SPIN_MAX = 1u << 20;

constexpr unsigned WS_EPOCH = 0;
constexpr unsigned WS_KVF = 1024;
constexpr unsigned WS_CAND = WS_KVF + 8 * 256 * 8;
constexpr unsigned WS_YF = WS_CAND + 8 * 512 * 8;
constexpr unsigned WS_YG = WS_YF + 8 * 1024;
constexpr unsigned WS_K = WS_YG + 1024 * 8;
constexpr unsigned WS_V = WS_K + S_ * 32;
constexpr unsigned WS_YP = WS_V + 256 * VHALF;
constexpr unsigned WS_P1 = WS_YP + NU * 2 * D_ * 4;
constexpr unsigned WS_O = WS_P1 + NU * 2048;
constexpr unsigned WS_END = WS_O + S_ * D_ * 2;

constexpr int L_WI = 0;
constexpr int L_XI = 65536;
constexpr int L_KI = 131072;
constexpr int PSLOT = 2304;
constexpr int PREG = 16384;
constexpr int L_XS = 7 * PREG;
constexpr int L_GU = 131072;
constexpr int L_QI = 147456;
constexpr int L_CTL = L_QI + 8192;
constexpr int L_DN = L_CTL + 1024;
constexpr int L_DN2 = 7 * PREG + PSLOT + 128;
constexpr int L_HS = 0;
constexpr int L_TOTAL = L_DN + 6 * 1088;
constexpr int C_CNT = 0;
constexpr int C_SEL = 4;
constexpr int C_K1 = 8, C_K2 = 12;
constexpr int C_QMAX = 16;
constexpr int C_R = 32;
constexpr int C_STAT = 64;

#define STAMP(kid, i)

DI float bf_lo(unsigned u) { return __uint_as_float(u << 16); }
DI float bf_hi(unsigned u) { return __uint_as_float(u & 0xffff0000u); }
DI unsigned pack_bf(float a, float b) { f32x2 f = {a, b}; return __builtin_bit_cast(unsigned, __builtin_convertvector(f, bf16x2)); }
DI float rbf(float a) { return (float)(__bf16)a; }
DI float dot2(unsigned a, unsigned b, float c) { return __builtin_amdgcn_fdot2_f32_bf16(__builtin_bit_cast(bf16x2, a), __builtin_bit_cast(bf16x2, b), c, false); }
DI void glds16(const void* sbase, unsigned voff, void* lds_wave_base) {
  unsigned keep; const unsigned dst = (unsigned)(uintptr_t)(LAS char*)lds_wave_base;
  asm volatile("s_mov_b32 %0, m0\n\ts_mov_b32 m0, %2\n\ts_nop 0\n\tglobal_load_lds_dwordx4 %1, %3\n\ts_mov_b32 m0, %0"
               : "=&s"(keep) : "v"(voff), "s"(dst), "s"(sbase) : "memory");
}
#define GLD128(dst, voff, sbase) asm volatile("global_load_dwordx4 %0, %1, %2" : "=v"(dst) : "v"(voff), "s"(sbase) : "memory")
DI const char* opq(const char* p) { unsigned a = (unsigned)(uintptr_t)(LAS char*)p; asm volatile("" : "+v"(a)); return (const char*)(LAS char*)(uintptr_t)a; }
DI s16x4 tr_read(const char* p) { return __builtin_amdgcn_ds_read_tr16_b64_v4i16((LAS s16x4*)p); }
DI bf16x8 tr_frag(const char* p0, const char* p1) {
  s16x4 a = tr_read(p0), b = tr_read(p1);
  s16x8 v = __builtin_shufflevector(a, b, 0, 1, 2, 3, 4, 5, 6, 7);
  return __builtin_bit_cast(bf16x8, v);
}
template <int CTRL> DI float dpp_f(float v) { return __int_as_float(__builtin_amdgcn_update_dpp(0, __float_as_int(v), CTRL, 0xf, 0xf, true)); }
template <int CTRL> DI unsigned dpp_u(unsigned v) { return (unsigned)__builtin_amdgcn_update_dpp(0, (int)v, CTRL, 0xf, 0xf, true); }
DI float sum8(float v) {
  v += dpp_f<0xB1>(v); v += dpp_f<0x4E>(v); v += dpp_f<0x141>(v);
  return v;
}
DI float add16(float v) { auto r = __builtin_amdgcn_permlane16_swap(__float_as_uint(v), __float_as_uint(v), false, false); return __uint_as_float(r[0]) + __uint_as_float(r[1]); }
DI float add32(float v) { auto r = __builtin_amdgcn_permlane32_swap(__float_as_uint(v), __float_as_uint(v), false, false); return __uint_as_float(r[0]) + __uint_as_float(r[1]); }
DI float max16(float v) { auto r = __builtin_amdgcn_permlane16_swap(__float_as_uint(v), __float_as_uint(v), false, false); return fmaxf(__uint_as_float(r[0]), __uint_as_float(r[1])); }
DI float max32(float v) { auto r = __builtin_amdgcn_permlane32_swap(__float_as_uint(v), __float_as_uint(v), false, false); return fmaxf(__uint_as_float(r[0]), __uint_as_float(r[1])); }
DI float sum32(float v) { v = sum8(v); v += dpp_f<0x140>(v); return add16(v); }
DI float wave_sum(float v) { return add32(sum32(v)); }
DI float wave_maxf(float v) {
  v = fmaxf(v, dpp_f<0xB1>(v)); v = fmaxf(v, dpp_f<0x4E>(v)); v = fmaxf(v, dpp_f<0x141>(v)); v = fmaxf(v, dpp_f<0x140>(v));
  return max32(max16(v));
}
DI unsigned wave_maxu(unsigned v) {
  v = max(v, dpp_u<0xB1>(v)); v = max(v, dpp_u<0x4E>(v)); v = max(v, dpp_u<0x141>(v)); v = max(v, dpp_u<0x140>(v));
  auto r = __builtin_amdgcn_permlane16_swap(v, v, false, false); v = max(r[0], r[1]);
  auto q = __builtin_amdgcn_permlane32_swap(v, v, false, false); return max(q[0], q[1]);
}
DI float wave_maxpos(float v) { return __uint_as_float(wave_maxu(__float_as_uint(v))); }
DI float silu_f(float v) { return v * __builtin_amdgcn_rcpf(1.0f + __builtin_amdgcn_exp2f(-1.4426950408889634f * v)); }
DI float sigm_f(float v) { return __builtin_amdgcn_rcpf(1.0f + __builtin_amdgcn_exp2f(-1.4426950408889634f * v)); }
DI float ln_sd(float var) { return rbf(__builtin_amdgcn_sqrtf(rbf(rbf(var) + 1e-5f))); }
DI unsigned ln_pair(unsigned xu, float mb, float inv) {
  const unsigned d = pack_bf(bf_lo(xu) - mb, bf_hi(xu) - mb);
  return pack_bf(bf_lo(d) * inv, bf_hi(d) * inv);
}
DI unsigned silu_pair(unsigned v) { return pack_bf(silu_f(bf_lo(v)), silu_f(bf_hi(v))); }

DI void stats32(const u32x4& a, const u32x4& b2, float& mean, float& var) {
  float s = 0.f, q = 0.f;
#pragma unroll
  for (int j = 0; j < 4; ++j) { s = dot2(a[j], 0x3f803f80u, s); s = dot2(b2[j], 0x3f803f80u, s); q = dot2(a[j], a[j], q); q = dot2(b2[j], b2[j], q); }
  mean = sum32(s) * (1.0f / 512.0f);
  var = fmaf(-mean, mean, sum32(q) * (1.0f / 512.0f));
  if (__builtin_expect(__any(!(var >= mean * mean * 0.015625f)), 0)) {
    float v = 0.f;
#pragma unroll
    for (int j = 0; j < 4; ++j) {
      const float a0 = bf_lo(a[j]) - mean, a1 = bf_hi(a[j]) - mean, a2 = bf_lo(b2[j]) - mean, a3 = bf_hi(b2[j]) - mean;
      v = fmaf(a0, a0, v); v = fmaf(a1, a1, v); v = fmaf(a2, a2, v); v = fmaf(a3, a3, v);
    }
    var = sum32(v) * (1.0f / 512.0f);
  }
}

DI unsigned make_key(float r_bf, int token) {
  unsigned b = __float_as_uint(r_bf) >> 16;
  b = (b & 0x8000u) ? (~b & 0xffffu) : (b | 0x8000u);
  return (b << 16) | (unsigned)(2047 - token);
}
DI float key_val(unsigned key) {
  unsigned b = key >> 16;
  b = (b & 0x8000u) ? (b & 0x7fffu) : (~b & 0xffffu);
  return __uint_as_float(b << 16);
}
DI int key_tok(unsigned key) { return 2047 - (int)(key & 0x7ffu); }

DI u32x4 ld128(rsrc_t rs, unsigned off) { return __builtin_amdgcn_raw_buffer_load_b128(rs, (int)off, 0, SC1); }
DI u32x2 ld64(rsrc_t rs, unsigned off) { return __builtin_amdgcn_raw_buffer_load_b64(rs, (int)off, 0, SC1); }
DI unsigned ld32(rsrc_t rs, unsigned off) { return __builtin_amdgcn_raw_buffer_load_b32(rs, (int)off, 0, SC1); }
DI void st128(rsrc_t rs, unsigned off, u32x4 v) { __builtin_amdgcn_raw_buffer_store_b128(v, rs, (int)off, 0, SC1); }
DI void st64(rsrc_t rs, unsigned off, u32x2 v) { __builtin_amdgcn_raw_buffer_store_b64(v, rs, (int)off, 0, SC1); }
DI void st32(rsrc_t rs, unsigned off, unsigned v) { __builtin_amdgcn_raw_buffer_store_b32(v, rs, (int)off, 0, SC1); }
#define DRAIN() asm volatile("s_waitcnt vmcnt(0)" ::: "memory")
#define VMWAIT(n) asm volatile("s_waitcnt vmcnt(" #n ")" ::: "memory")
#define CBAR() asm volatile("" ::: "memory")
#define RAW_BARRIER() do { asm volatile("s_waitcnt lgkmcnt(0)" ::: "memory"); __builtin_amdgcn_s_barrier(); asm volatile("" ::: "memory"); } while (0)
#define SLEEP(n) do { __builtin_amdgcn_s_sleep(n); asm volatile("" ::: "memory"); } while (0)
#define SPIN _Pragma("nounroll") for (unsigned s_ = 0; s_ < SPIN_MAX; ++s_)
DI unsigned lds_ld(const char* p) { return __hip_atomic_load((LAS unsigned*)p, __ATOMIC_RELAXED, __HIP_MEMORY_SCOPE_WORKGROUP); }
DI void lds_wait_ge(const char* p, unsigned want) {
  SPIN {
    if ((unsigned)__builtin_amdgcn_readfirstlane(lds_ld(p)) >= want) break;
    SLEEP(1);
  }
  CBAR();
}
#define QK(KF, QF, C) __builtin_amdgcn_mfma_f32_32x32x16_bf16((KF), (QF), (C), 0, 0, 0)
DI f32x16 splat16(float v) { f32x16 r;
#pragma unroll
  for (int i = 0; i < 16; ++i) r[i] = v;
  return r; }
#define SB() __builtin_amdgcn_sched_barrier(0)
#define SGB(mask, n) __builtin_amdgcn_sched_group_barrier(mask, n, 0)

#define VCONST_ASM \
  "v_mov_b32 v132, %[cst]\n\t" "v_mov_b32 v133, %[cst]\n\t" "v_mov_b32 v134, %[cst]\n\t" "v_mov_b32 v135, %[cst]\n\t" "v_mov_b32 v140, %[cst]\n\t" \
  "v_mov_b32 v141, %[cst]\n\t" "v_mov_b32 v142, %[cst]\n\t" "v_mov_b32 v143, %[cst]\n\t" "v_mov_b32 v148, %[cst]\n\t" "v_mov_b32 v149, %[cst]\n\t" \
  "v_mov_b32 v150, %[cst]\n\t" "v_mov_b32 v151, %[cst]\n\t" "v_mov_b32 v156, %[cst]\n\t" "v_mov_b32 v157, %[cst]\n\t" "v_mov_b32 v158, %[cst]\n\t" \
  "v_mov_b32 v159, %[cst]\n\t" "v_mov_b32 v164, %[cst]\n\t" "v_mov_b32 v165, %[cst]\n\t" "v_mov_b32 v166, %[cst]\n\t" "v_mov_b32 v167, %[cst]\n\t" \
  "v_mov_b32 v172, %[cst]\n\t" "v_mov_b32 v173, %[cst]\n\t" "v_mov_b32 v174, %[cst]\n\t" "v_mov_b32 v175, %[cst]\n\t" "v_mov_b32 v180, %[cst]\n\t" \
  "v_mov_b32 v181, %[cst]\n\t" "v_mov_b32 v182, %[cst]\n\t" "v_mov_b32 v183, %[cst]\n\t" "v_mov_b32 v188, %[cst]\n\t" "v_mov_b32 v189, %[cst]\n\t" \
  "v_mov_b32 v190, %[cst]\n\t" "v_mov_b32 v191, %[cst]\n\t" ""
#define LOADS_ASM \
  "buffer_load_dwordx4 v[96:99], %[ka0], %[rs], 0 offen offset:0 sc1\n\t" "buffer_load_dwordx4 v[128:131], %[va0], %[rs], 0 offen offset:0 sc1\n\t" \
  "buffer_load_dwordx4 v[100:103], %[ka0], %[rs], 0 offen offset:1024 sc1\n\t" "buffer_load_dwordx4 v[136:139], %[va0], %[rs], 0 offen offset:1024 sc1\n\t" \
  "buffer_load_dwordx4 v[104:107], %[ka0], %[rs], 0 offen offset:2048 sc1\n\t" "buffer_load_dwordx4 v[144:147], %[va0], %[rs], 0 offen offset:2048 sc1\n\t" \
  "buffer_load_dwordx4 v[108:111], %[ka0], %[rs], 0 offen offset:3072 sc1\n\t" "buffer_load_dwordx4 v[152:155], %[va0], %[rs], 0 offen offset:3072 sc1\n\t" \
  "buffer_load_dwordx4 v[112:115], %[ka1], %[rs], 0 offen offset:0 sc1\n\t" "buffer_load_dwordx4 v[160:163], %[va1], %[rs], 0 offen offset:0 sc1\n\t" \
  "buffer_load_dwordx4 v[116:119], %[ka1], %[rs], 0 offen offset:1024 sc1\n\t" "buffer_load_dwordx4 v[168:171], %[va1], %[rs], 0 offen offset:1024 sc1\n\t" \
  "buffer_load_dwordx4 v[120:123], %[ka1], %[rs], 0 offen offset:2048 sc1\n\t" "buffer_load_dwordx4 v[176:179], %[va1], %[rs], 0 offen offset:2048 sc1\n\t" \
  "buffer_load_dwordx4 v[124:127], %[ka1], %[rs], 0 offen offset:3072 sc1\n\t" "buffer_load_dwordx4 v[184:187], %[va1], %[rs], 0 offen offset:3072 sc1\n\t" ""
#define SWAPS_ASM \
  "v_permlane16_swap_b32 v128, v132\n\t" "v_permlane16_swap_b32 v129, v133\n\t" "v_permlane16_swap_b32 v130, v134\n\t" "v_permlane16_swap_b32 v131, v135\n\t" \
  "v_permlane16_swap_b32 v136, v140\n\t" "v_permlane16_swap_b32 v137, v141\n\t" "v_permlane16_swap_b32 v138, v142\n\t" "v_permlane16_swap_b32 v139, v143\n\t" \
  "v_permlane16_swap_b32 v144, v148\n\t" "v_permlane16_swap_b32 v145, v149\n\t" "v_permlane16_swap_b32 v146, v150\n\t" "v_permlane16_swap_b32 v147, v151\n\t" \
  "v_permlane16_swap_b32 v152, v156\n\t" "v_permlane16_swap_b32 v153, v157\n\t" "v_permlane16_swap_b32 v154, v158\n\t" "v_permlane16_swap_b32 v155, v159\n\t" \
  "v_permlane16_swap_b32 v160, v164\n\t" "v_permlane16_swap_b32 v161, v165\n\t" "v_permlane16_swap_b32 v162, v166\n\t" "v_permlane16_swap_b32 v163, v167\n\t" \
  "v_permlane16_swap_b32 v168, v172\n\t" "v_permlane16_swap_b32 v169, v173\n\t" "v_permlane16_swap_b32 v170, v174\n\t" "v_permlane16_swap_b32 v171, v175\n\t" \
  "v_permlane16_swap_b32 v176, v180\n\t" "v_permlane16_swap_b32 v177, v181\n\t" "v_permlane16_swap_b32 v178, v182\n\t" "v_permlane16_swap_b32 v179, v183\n\t" \
  "v_permlane16_swap_b32 v184, v188\n\t" "v_permlane16_swap_b32 v185, v189\n\t" "v_permlane16_swap_b32 v186, v190\n\t" "v_permlane16_swap_b32 v187, v191\n\t" ""
#define PROLOG0_ASM \
  "s_waitcnt vmcnt(15)\n\t" "s_nop 1\n\t" "v_mfma_f32_32x32x16_bf16 v[208:223], v[96:99], %[q], 0\n\t" "s_waitcnt vmcnt(13)\n\t" \
  "v_mfma_f32_32x32x16_bf16 v[224:239], v[100:103], %[q], 0\n\t" "v_permlane16_swap_b32 v128, v132\n\t" "v_permlane16_swap_b32 v129, v133\n\t" \
  "v_permlane16_swap_b32 v130, v134\n\t" "v_permlane16_swap_b32 v131, v135\n\t" "s_nop 6\n\t" "v_exp_f32 v208, v208\n\t" "v_exp_f32 v209, v209\n\t" \
  "v_exp_f32 v210, v210\n\t" "v_exp_f32 v211, v211\n\t" "v_exp_f32 v212, v212\n\t" "v_exp_f32 v213, v213\n\t" "v_exp_f32 v214, v214\n\t" "v_exp_f32 v215, v215\n\t" \
  "v_exp_f32 v216, v216\n\t" "v_exp_f32 v217, v217\n\t" "v_exp_f32 v218, v218\n\t" "v_exp_f32 v219, v219\n\t" "v_exp_f32 v220, v220\n\t" ""
#define PROLOG_ASM \
  "s_nop 1\n\t" "v_mfma_f32_32x32x16_bf16 v[208:223], v[96:99], %[q], 0\n\t" "v_mfma_f32_32x32x16_bf16 v[224:239], v[100:103], %[q], 0\n\t" \
  "s_nop 10\n\t" "v_exp_f32 v208, v208\n\t" "v_exp_f32 v209, v209\n\t" "v_exp_f32 v210, v210\n\t" "v_exp_f32 v211, v211\n\t" "v_exp_f32 v212, v212\n\t" \
  "v_exp_f32 v213, v213\n\t" "v_exp_f32 v214, v214\n\t" "v_exp_f32 v215, v215\n\t" "v_exp_f32 v216, v216\n\t" "v_exp_f32 v217, v217\n\t" "v_exp_f32 v218, v218\n\t" \
  "v_exp_f32 v219, v219\n\t" "v_exp_f32 v220, v220\n\t" ""
#define PASS0_ASM \
  "v_mfma_f32_32x32x16_bf16 v[240:255], %[z], %[z], 0\n\t" "v_exp_f32 v221, v221\n\t" "v_exp_f32 v222, v222\n\t" "v_exp_f32 v223, v223\n\t" \
  "v_smfmac_f32_32x32x32_bf16 v[240:255], v[128:131], v[208:215], %[idx]\n\t" "v_exp_f32 v224, v224\n\t" "v_exp_f32 v225, v225\n\t" \
  "v_smfmac_f32_32x32x32_bf16 v[240:255], v[132:135], v[216:223], %[idx]\n\t" "v_exp_f32 v226, v226\n\t" "v_exp_f32 v227, v227\n\t" "v_exp_f32 v228, v228\n\t" \
  "s_waitcnt vmcnt(11)\n\t" "v_mfma_f32_32x32x16_bf16 v[208:223], v[104:107], %[q], 0\n\t" "v_permlane16_swap_b32 v136, v140\n\t" \
  "v_permlane16_swap_b32 v137, v141\n\t" "v_permlane16_swap_b32 v138, v142\n\t" "v_permlane16_swap_b32 v139, v143\n\t" "v_exp_f32 v229, v229\n\t" \
  "v_exp_f32 v230, v230\n\t" "v_exp_f32 v231, v231\n\t" "v_exp_f32 v232, v232\n\t" "v_exp_f32 v233, v233\n\t" \
  "v_smfmac_f32_32x32x32_bf16 v[240:255], v[136:139], v[224:231], %[idx]\n\t" "v_exp_f32 v234, v234\n\t" "v_exp_f32 v235, v235\n\t" "v_exp_f32 v236, v236\n\t" \
  "v_exp_f32 v237, v237\n\t" "v_exp_f32 v238, v238\n\t" "v_exp_f32 v239, v239\n\t" "v_exp_f32 v208, v208\n\t" "v_exp_f32 v209, v209\n\t" \
  "v_smfmac_f32_32x32x32_bf16 v[240:255], v[140:143], v[232:239], %[idx]\n\t" "v_exp_f32 v210, v210\n\t" "v_exp_f32 v211, v211\n\t" "v_exp_f32 v212, v212\n\t" \
  "s_waitcnt vmcnt(9)\n\t" "v_mfma_f32_32x32x16_bf16 v[224:239], v[108:111], %[q], 0\n\t" "v_permlane16_swap_b32 v144, v148\n\t" \
  "v_permlane16_swap_b32 v145, v149\n\t" "v_permlane16_swap_b32 v146, v150\n\t" "v_permlane16_swap_b32 v147, v151\n\t" "v_exp_f32 v213, v213\n\t" \
  "v_exp_f32 v214, v214\n\t" "v_exp_f32 v215, v215\n\t" "v_exp_f32 v216, v216\n\t" "v_exp_f32 v217, v217\n\t" \
  "v_smfmac_f32_32x32x32_bf16 v[240:255], v[144:147], v[208:215], %[idx]\n\t" "v_exp_f32 v218, v218\n\t" "v_exp_f32 v219, v219\n\t" "v_exp_f32 v220, v220\n\t" \
  "v_exp_f32 v221, v221\n\t" "v_exp_f32 v222, v222\n\t" "v_exp_f32 v223, v223\n\t" "v_exp_f32 v224, v224\n\t" "v_exp_f32 v225, v225\n\t" \
  "v_smfmac_f32_32x32x32_bf16 v[240:255], v[148:151], v[216:223], %[idx]\n\t" "v_exp_f32 v226, v226\n\t" "v_exp_f32 v227, v227\n\t" "v_exp_f32 v228, v228\n\t" \
  "s_waitcnt vmcnt(7)\n\t" "v_mfma_f32_32x32x16_bf16 v[208:223], v[112:115], %[q], 0\n\t" "v_permlane16_swap_b32 v152, v156\n\t" \
  "v_permlane16_swap_b32 v153, v157\n\t" "v_permlane16_swap_b32 v154, v158\n\t" "v_permlane16_swap_b32 v155, v159\n\t" "v_exp_f32 v229, v229\n\t" \
  "v_exp_f32 v230, v230\n\t" "v_exp_f32 v231, v231\n\t" "v_exp_f32 v232, v232\n\t" "v_exp_f32 v233, v233\n\t" \
  "v_smfmac_f32_32x32x32_bf16 v[240:255], v[152:155], v[224:231], %[idx]\n\t" "v_exp_f32 v234, v234\n\t" "v_exp_f32 v235, v235\n\t" "v_exp_f32 v236, v236\n\t" \
  "v_exp_f32 v237, v237\n\t" "v_exp_f32 v238, v238\n\t" "v_exp_f32 v239, v239\n\t" "v_exp_f32 v208, v208\n\t" "v_exp_f32 v209, v209\n\t" \
  "v_smfmac_f32_32x32x32_bf16 v[240:255], v[156:159], v[232:239], %[idx]\n\t" "v_exp_f32 v210, v210\n\t" "v_exp_f32 v211, v211\n\t" "v_exp_f32 v212, v212\n\t" \
  "s_waitcnt vmcnt(5)\n\t" "v_mfma_f32_32x32x16_bf16 v[224:239], v[116:119], %[q], 0\n\t" "v_permlane16_swap_b32 v160, v164\n\t" \
  "v_permlane16_swap_b32 v161, v165\n\t" "v_permlane16_swap_b32 v162, v166\n\t" "v_permlane16_swap_b32 v163, v167\n\t" "v_exp_f32 v213, v213\n\t" \
  "v_exp_f32 v214, v214\n\t" "v_exp_f32 v215, v215\n\t" "v_exp_f32 v216, v216\n\t" "v_exp_f32 v217, v217\n\t" \
  "v_smfmac_f32_32x32x32_bf16 v[240:255], v[160:163], v[208:215], %[idx]\n\t" "v_exp_f32 v218, v218\n\t" "v_exp_f32 v219, v219\n\t" "v_exp_f32 v220, v220\n\t" \
  "v_exp_f32 v221, v221\n\t" "v_exp_f32 v222, v222\n\t" "v_exp_f32 v223, v223\n\t" "v_exp_f32 v224, v224\n\t" "v_exp_f32 v225, v225\n\t" \
  "v_smfmac_f32_32x32x32_bf16 v[240:255], v[164:167], v[216:223], %[idx]\n\t" "v_exp_f32 v226, v226\n\t" "v_exp_f32 v227, v227\n\t" "v_exp_f32 v228, v228\n\t" \
  "s_waitcnt vmcnt(3)\n\t" "v_mfma_f32_32x32x16_bf16 v[208:223], v[120:123], %[q], 0\n\t" "v_permlane16_swap_b32 v168, v172\n\t" \
  "v_permlane16_swap_b32 v169, v173\n\t" "v_permlane16_swap_b32 v170, v174\n\t" "v_permlane16_swap_b32 v171, v175\n\t" "v_exp_f32 v229, v229\n\t" \
  "v_exp_f32 v230, v230\n\t" "v_exp_f32 v231, v231\n\t" "v_exp_f32 v232, v232\n\t" "v_exp_f32 v233, v233\n\t" \
  "v_smfmac_f32_32x32x32_bf16 v[240:255], v[168:171], v[224:231], %[idx]\n\t" "v_exp_f32 v234, v234\n\t" "v_exp_f32 v235, v235\n\t" "v_exp_f32 v236, v236\n\t" \
  "v_exp_f32 v237, v237\n\t" "v_exp_f32 v238, v238\n\t" "v_exp_f32 v239, v239\n\t" "v_exp_f32 v208, v208\n\t" "v_exp_f32 v209, v209\n\t" \
  "v_smfmac_f32_32x32x32_bf16 v[240:255], v[172:175], v[232:239], %[idx]\n\t" "v_exp_f32 v210, v210\n\t" "v_exp_f32 v211, v211\n\t" "v_exp_f32 v212, v212\n\t" \
  "s_waitcnt vmcnt(1)\n\t" "v_mfma_f32_32x32x16_bf16 v[224:239], v[124:127], %[q], 0\n\t" "ds_read_b128 %[q], %[qa]\n\t" "v_permlane16_swap_b32 v176, v180\n\t" \
  "v_permlane16_swap_b32 v177, v181\n\t" "v_permlane16_swap_b32 v178, v182\n\t" "v_permlane16_swap_b32 v179, v183\n\t" "v_exp_f32 v213, v213\n\t" \
  "v_exp_f32 v214, v214\n\t" "v_exp_f32 v215, v215\n\t" "v_exp_f32 v216, v216\n\t" "v_exp_f32 v217, v217\n\t" \
  "v_smfmac_f32_32x32x32_bf16 v[240:255], v[176:179], v[208:215], %[idx]\n\t" "v_exp_f32 v218, v218\n\t" "v_exp_f32 v219, v219\n\t" "v_exp_f32 v220, v220\n\t" \
  "v_exp_f32 v221, v221\n\t" "v_exp_f32 v222, v222\n\t" "v_exp_f32 v223, v223\n\t" "v_exp_f32 v224, v224\n\t" "v_exp_f32 v225, v225\n\t" \
  "v_smfmac_f32_32x32x32_bf16 v[240:255], v[180:183], v[216:223], %[idx]\n\t" "v_exp_f32 v226, v226\n\t" "v_exp_f32 v227, v227\n\t" "v_exp_f32 v228, v228\n\t" \
  "s_waitcnt vmcnt(0) lgkmcnt(0)\n\t" "v_mfma_f32_32x32x16_bf16 v[208:223], v[96:99], %[q], 0\n\t" "v_permlane16_swap_b32 v184, v188\n\t" \
  "v_permlane16_swap_b32 v185, v189\n\t" "v_permlane16_swap_b32 v186, v190\n\t" "v_permlane16_swap_b32 v187, v191\n\t" "v_exp_f32 v229, v229\n\t" \
  "v_exp_f32 v230, v230\n\t" "v_exp_f32 v231, v231\n\t" "v_exp_f32 v232, v232\n\t" "v_exp_f32 v233, v233\n\t" \
  "v_smfmac_f32_32x32x32_bf16 v[240:255], v[184:187], v[224:231], %[idx]\n\t" "v_exp_f32 v234, v234\n\t" "v_exp_f32 v235, v235\n\t" "v_exp_f32 v236, v236\n\t" \
  "v_exp_f32 v237, v237\n\t" "v_exp_f32 v238, v238\n\t" "v_exp_f32 v239, v239\n\t" "v_exp_f32 v208, v208\n\t" "v_exp_f32 v209, v209\n\t" \
  "v_smfmac_f32_32x32x32_bf16 v[240:255], v[188:191], v[232:239], %[idx]\n\t" "v_exp_f32 v210, v210\n\t" "v_exp_f32 v211, v211\n\t" "v_exp_f32 v212, v212\n\t" \
  "v_mfma_f32_32x32x16_bf16 v[224:239], v[100:103], %[q], 0\n\t" "v_exp_f32 v213, v213\n\t" "v_exp_f32 v214, v214\n\t" "v_exp_f32 v215, v215\n\t" \
  "v_exp_f32 v216, v216\n\t" "v_exp_f32 v217, v217\n\t" "v_exp_f32 v218, v218\n\t" "v_exp_f32 v219, v219\n\t" "v_exp_f32 v220, v220\n\t" ""
#define PASSN_ASM \
  "v_mfma_f32_32x32x16_bf16 v[240:255], %[z], %[z], 0\n\t" "v_exp_f32 v221, v221\n\t" "v_exp_f32 v222, v222\n\t" "v_exp_f32 v223, v223\n\t" \
  "v_smfmac_f32_32x32x32_bf16 v[240:255], v[128:131], v[208:215], %[idx]\n\t" "v_exp_f32 v224, v224\n\t" "v_exp_f32 v225, v225\n\t" \
  "v_smfmac_f32_32x32x32_bf16 v[240:255], v[132:135], v[216:223], %[idx]\n\t" "v_exp_f32 v226, v226\n\t" "v_exp_f32 v227, v227\n\t" "v_exp_f32 v228, v228\n\t" \
  "v_mfma_f32_32x32x16_bf16 v[208:223], v[104:107], %[q], 0\n\t" "v_exp_f32 v229, v229\n\t" "v_exp_f32 v230, v230\n\t" "v_exp_f32 v231, v231\n\t" \
  "v_exp_f32 v232, v232\n\t" "v_exp_f32 v233, v233\n\t" "v_smfmac_f32_32x32x32_bf16 v[240:255], v[136:139], v[224:231], %[idx]\n\t" "v_exp_f32 v234, v234\n\t" \
  "v_exp_f32 v235, v235\n\t" "v_exp_f32 v236, v236\n\t" "v_exp_f32 v237, v237\n\t" "v_exp_f32 v238, v238\n\t" "v_exp_f32 v239, v239\n\t" "v_exp_f32 v208, v208\n\t" \
  "v_exp_f32 v209, v209\n\t" "v_smfmac_f32_32x32x32_bf16 v[240:255], v[140:143], v[232:239], %[idx]\n\t" "v_exp_f32 v210, v210\n\t" "v_exp_f32 v211, v211\n\t" \
  "v_exp_f32 v212, v212\n\t" "v_mfma_f32_32x32x16_bf16 v[224:239], v[108:111], %[q], 0\n\t" "v_exp_f32 v213, v213\n\t" "v_exp_f32 v214, v214\n\t" \
  "v_exp_f32 v215, v215\n\t" "v_exp_f32 v216, v216\n\t" "v_exp_f32 v217, v217\n\t" "v_smfmac_f32_32x32x32_bf16 v[240:255], v[144:147], v[208:215], %[idx]\n\t" \
  "v_exp_f32 v218, v218\n\t" "v_exp_f32 v219, v219\n\t" "v_exp_f32 v220, v220\n\t" "v_exp_f32 v221, v221\n\t" "v_exp_f32 v222, v222\n\t" "v_exp_f32 v223, v223\n\t" \
  "v_exp_f32 v224, v224\n\t" "v_exp_f32 v225, v225\n\t" "v_smfmac_f32_32x32x32_bf16 v[240:255], v[148:151], v[216:223], %[idx]\n\t" "v_exp_f32 v226, v226\n\t" \
  "v_exp_f32 v227, v227\n\t" "v_exp_f32 v228, v228\n\t" "v_mfma_f32_32x32x16_bf16 v[208:223], v[112:115], %[q], 0\n\t" "v_exp_f32 v229, v229\n\t" \
  "v_exp_f32 v230, v230\n\t" "v_exp_f32 v231, v231\n\t" "v_exp_f32 v232, v232\n\t" "v_exp_f32 v233, v233\n\t" \
  "v_smfmac_f32_32x32x32_bf16 v[240:255], v[152:155], v[224:231], %[idx]\n\t" "v_exp_f32 v234, v234\n\t" "v_exp_f32 v235, v235\n\t" "v_exp_f32 v236, v236\n\t" \
  "v_exp_f32 v237, v237\n\t" "v_exp_f32 v238, v238\n\t" "v_exp_f32 v239, v239\n\t" "v_exp_f32 v208, v208\n\t" "v_exp_f32 v209, v209\n\t" \
  "v_smfmac_f32_32x32x32_bf16 v[240:255], v[156:159], v[232:239], %[idx]\n\t" "v_exp_f32 v210, v210\n\t" "v_exp_f32 v211, v211\n\t" "v_exp_f32 v212, v212\n\t" \
  "v_mfma_f32_32x32x16_bf16 v[224:239], v[116:119], %[q], 0\n\t" "v_exp_f32 v213, v213\n\t" "v_exp_f32 v214, v214\n\t" "v_exp_f32 v215, v215\n\t" \
  "v_exp_f32 v216, v216\n\t" "v_exp_f32 v217, v217\n\t" "v_smfmac_f32_32x32x32_bf16 v[240:255], v[160:163], v[208:215], %[idx]\n\t" "v_exp_f32 v218, v218\n\t" \
  "v_exp_f32 v219, v219\n\t" "v_exp_f32 v220, v220\n\t" "v_exp_f32 v221, v221\n\t" "v_exp_f32 v222, v222\n\t" "v_exp_f32 v223, v223\n\t" "v_exp_f32 v224, v224\n\t" \
  "v_exp_f32 v225, v225\n\t" "v_smfmac_f32_32x32x32_bf16 v[240:255], v[164:167], v[216:223], %[idx]\n\t" "v_exp_f32 v226, v226\n\t" "v_exp_f32 v227, v227\n\t" \
  "v_exp_f32 v228, v228\n\t" "v_mfma_f32_32x32x16_bf16 v[208:223], v[120:123], %[q], 0\n\t" "v_exp_f32 v229, v229\n\t" "v_exp_f32 v230, v230\n\t" \
  "v_exp_f32 v231, v231\n\t" "v_exp_f32 v232, v232\n\t" "v_exp_f32 v233, v233\n\t" "v_smfmac_f32_32x32x32_bf16 v[240:255], v[168:171], v[224:231], %[idx]\n\t" \
  "v_exp_f32 v234, v234\n\t" "v_exp_f32 v235, v235\n\t" "v_exp_f32 v236, v236\n\t" "v_exp_f32 v237, v237\n\t" "v_exp_f32 v238, v238\n\t" "v_exp_f32 v239, v239\n\t" \
  "v_exp_f32 v208, v208\n\t" "v_exp_f32 v209, v209\n\t" "v_smfmac_f32_32x32x32_bf16 v[240:255], v[172:175], v[232:239], %[idx]\n\t" "v_exp_f32 v210, v210\n\t" \
  "v_exp_f32 v211, v211\n\t" "v_exp_f32 v212, v212\n\t" "v_mfma_f32_32x32x16_bf16 v[224:239], v[124:127], %[q], 0\n\t" "ds_read_b128 %[q], %[qa]\n\t" \
  "v_exp_f32 v213, v213\n\t" "v_exp_f32 v214, v214\n\t" "v_exp_f32 v215, v215\n\t" "v_exp_f32 v216, v216\n\t" "v_exp_f32 v217, v217\n\t" \
  "v_smfmac_f32_32x32x32_bf16 v[240:255], v[176:179], v[208:215], %[idx]\n\t" "v_exp_f32 v218, v218\n\t" "v_exp_f32 v219, v219\n\t" "v_exp_f32 v220, v220\n\t" \
  "v_exp_f32 v221, v221\n\t" "v_exp_f32 v222, v222\n\t" "v_exp_f32 v223, v223\n\t" "v_exp_f32 v224, v224\n\t" "v_exp_f32 v225, v225\n\t" \
  "v_smfmac_f32_32x32x32_bf16 v[240:255], v[180:183], v[216:223], %[idx]\n\t" "v_exp_f32 v226, v226\n\t" "v_exp_f32 v227, v227\n\t" "v_exp_f32 v228, v228\n\t" \
  "s_waitcnt lgkmcnt(0)\n\t" "v_mfma_f32_32x32x16_bf16 v[208:223], v[96:99], %[q], 0\n\t" "v_exp_f32 v229, v229\n\t" "v_exp_f32 v230, v230\n\t" \
  "v_exp_f32 v231, v231\n\t" "v_exp_f32 v232, v232\n\t" "v_exp_f32 v233, v233\n\t" "v_smfmac_f32_32x32x32_bf16 v[240:255], v[184:187], v[224:231], %[idx]\n\t" \
  "v_exp_f32 v234, v234\n\t" "v_exp_f32 v235, v235\n\t" "v_exp_f32 v236, v236\n\t" "v_exp_f32 v237, v237\n\t" "v_exp_f32 v238, v238\n\t" "v_exp_f32 v239, v239\n\t" \
  "v_exp_f32 v208, v208\n\t" "v_exp_f32 v209, v209\n\t" "v_smfmac_f32_32x32x32_bf16 v[240:255], v[188:191], v[232:239], %[idx]\n\t" "v_exp_f32 v210, v210\n\t" \
  "v_exp_f32 v211, v211\n\t" "v_exp_f32 v212, v212\n\t" "v_mfma_f32_32x32x16_bf16 v[224:239], v[100:103], %[q], 0\n\t" "v_exp_f32 v213, v213\n\t" \
  "v_exp_f32 v214, v214\n\t" "v_exp_f32 v215, v215\n\t" "v_exp_f32 v216, v216\n\t" "v_exp_f32 v217, v217\n\t" "v_exp_f32 v218, v218\n\t" "v_exp_f32 v219, v219\n\t" \
  "v_exp_f32 v220, v220\n\t" ""
#define ROWMAX_ASM \
  "s_nop 1\n\t" "v_mfma_f32_32x32x16_bf16 v[224:239], v[96:99], %[q], 0\n\t" "s_nop 11\n\t" "v_max3_f32 %[m], %[m], v224, v225\n\t" \
  "v_max3_f32 %[m], %[m], v226, v227\n\t" "v_max3_f32 %[m], %[m], v228, v229\n\t" "v_max3_f32 %[m], %[m], v230, v231\n\t" "v_max3_f32 %[m], %[m], v232, v233\n\t" \
  "v_max3_f32 %[m], %[m], v234, v235\n\t" "v_max3_f32 %[m], %[m], v236, v237\n\t" "v_max3_f32 %[m], %[m], v238, v239\n\t" \
  "v_mfma_f32_32x32x16_bf16 v[224:239], v[100:103], %[q], 0\n\t" "s_nop 11\n\t" "v_max3_f32 %[m], %[m], v224, v225\n\t" "v_max3_f32 %[m], %[m], v226, v227\n\t" \
  "v_max3_f32 %[m], %[m], v228, v229\n\t" "v_max3_f32 %[m], %[m], v230, v231\n\t" "v_max3_f32 %[m], %[m], v232, v233\n\t" "v_max3_f32 %[m], %[m], v234, v235\n\t" \
  "v_max3_f32 %[m], %[m], v236, v237\n\t" "v_max3_f32 %[m], %[m], v238, v239\n\t" "v_mfma_f32_32x32x16_bf16 v[224:239], v[104:107], %[q], 0\n\t" "s_nop 11\n\t" \
  "v_max3_f32 %[m], %[m], v224, v225\n\t" "v_max3_f32 %[m], %[m], v226, v227\n\t" "v_max3_f32 %[m], %[m], v228, v229\n\t" "v_max3_f32 %[m], %[m], v230, v231\n\t" \
  "v_max3_f32 %[m], %[m], v232, v233\n\t" "v_max3_f32 %[m], %[m], v234, v235\n\t" "v_max3_f32 %[m], %[m], v236, v237\n\t" "v_max3_f32 %[m], %[m], v238, v239\n\t" \
  "v_mfma_f32_32x32x16_bf16 v[224:239], v[108:111], %[q], 0\n\t" "s_nop 11\n\t" "v_max3_f32 %[m], %[m], v224, v225\n\t" "v_max3_f32 %[m], %[m], v226, v227\n\t" \
  "v_max3_f32 %[m], %[m], v228, v229\n\t" "v_max3_f32 %[m], %[m], v230, v231\n\t" "v_max3_f32 %[m], %[m], v232, v233\n\t" "v_max3_f32 %[m], %[m], v234, v235\n\t" \
  "v_max3_f32 %[m], %[m], v236, v237\n\t" "v_max3_f32 %[m], %[m], v238, v239\n\t" "v_mfma_f32_32x32x16_bf16 v[224:239], v[112:115], %[q], 0\n\t" "s_nop 11\n\t" \
  "v_max3_f32 %[m], %[m], v224, v225\n\t" "v_max3_f32 %[m], %[m], v226, v227\n\t" "v_max3_f32 %[m], %[m], v228, v229\n\t" "v_max3_f32 %[m], %[m], v230, v231\n\t" \
  "v_max3_f32 %[m], %[m], v232, v233\n\t" "v_max3_f32 %[m], %[m], v234, v235\n\t" "v_max3_f32 %[m], %[m], v236, v237\n\t" "v_max3_f32 %[m], %[m], v238, v239\n\t" \
  "v_mfma_f32_32x32x16_bf16 v[224:239], v[116:119], %[q], 0\n\t" "s_nop 11\n\t" "v_max3_f32 %[m], %[m], v224, v225\n\t" "v_max3_f32 %[m], %[m], v226, v227\n\t" \
  "v_max3_f32 %[m], %[m], v228, v229\n\t" "v_max3_f32 %[m], %[m], v230, v231\n\t" "v_max3_f32 %[m], %[m], v232, v233\n\t" "v_max3_f32 %[m], %[m], v234, v235\n\t" \
  "v_max3_f32 %[m], %[m], v236, v237\n\t" "v_max3_f32 %[m], %[m], v238, v239\n\t" "v_mfma_f32_32x32x16_bf16 v[224:239], v[120:123], %[q], 0\n\t" "s_nop 11\n\t" \
  "v_max3_f32 %[m], %[m], v224, v225\n\t" "v_max3_f32 %[m], %[m], v226, v227\n\t" "v_max3_f32 %[m], %[m], v228, v229\n\t" "v_max3_f32 %[m], %[m], v230, v231\n\t" \
  "v_max3_f32 %[m], %[m], v232, v233\n\t" "v_max3_f32 %[m], %[m], v234, v235\n\t" "v_max3_f32 %[m], %[m], v236, v237\n\t" "v_max3_f32 %[m], %[m], v238, v239\n\t" \
  "v_mfma_f32_32x32x16_bf16 v[224:239], v[124:127], %[q], 0\n\t" "s_nop 11\n\t" "v_max3_f32 %[m], %[m], v224, v225\n\t" "v_max3_f32 %[m], %[m], v226, v227\n\t" \
  "v_max3_f32 %[m], %[m], v228, v229\n\t" "v_max3_f32 %[m], %[m], v230, v231\n\t" "v_max3_f32 %[m], %[m], v232, v233\n\t" "v_max3_f32 %[m], %[m], v234, v235\n\t" \
  "v_max3_f32 %[m], %[m], v236, v237\n\t" "v_max3_f32 %[m], %[m], v238, v239\n\t" ""
#define POST_ASM \
  "s_add_u32 %[i], %[i], 1\n\t" "s_cmp_eq_u32 %[i], 8\n\t" "s_cbranch_scc1 .Lfb_out%=\n\t" "v_cndmask_b32 %[t1], %[sh], v248, %[lo]\n\t" \
  "v_add_u32 %[t2], %[so], %[l16]\n\t" "ds_write_b128 %[t2], v[240:243]\n\t" "ds_write_b128 %[t2], v[244:247] offset:1024\n\t" "v_add_u32 %[t2], %[so], %[l4]\n\t" \
  "ds_write_b32 %[t2], %[t1] offset:2048\n\t" "s_add_u32 %[so], %[so], 14080\n\t" "s_and_b32 %[so], %[so], 0x1ffff\n\t" "s_add_u32 %[qo], %[qo], 1024\n\t" \
  "s_and_b32 %[qo], %[qo], 0x1fff\n\t" "v_add_u32 %[qa], %[qo], %[qb]\n\t" ""
#define WOLOAD_ASM \
  "s_cmp_lg_u32 %[i], %[iw]\n\t" "s_cbranch_scc1 .Lfb_skip%=\n\t" "s_mov_b64 %[wb2], %[wb]\n\t" "global_load_dwordx4 v[64:67], %[ro], %[wb2]\n\t" \
  "v_add_u32 %[ro], 0x2000, %[ro]\n\t" "global_load_dwordx4 v[68:71], %[ro], %[wb2]\n\t" "v_add_u32 %[ro], 0x2000, %[ro]\n\t" \
  "global_load_dwordx4 v[72:75], %[ro], %[wb2]\n\t" "v_add_u32 %[ro], 0x2000, %[ro]\n\t" "global_load_dwordx4 v[76:79], %[ro], %[wb2]\n\t" \
  "v_add_u32 %[ro], 0x2000, %[ro]\n\t" "global_load_dwordx4 v[80:83], %[ro], %[wb2]\n\t" "v_add_u32 %[ro], 0x2000, %[ro]\n\t" \
  "global_load_dwordx4 v[84:87], %[ro], %[wb2]\n\t" "v_add_u32 %[ro], 0x2000, %[ro]\n\t" "global_load_dwordx4 v[88:91], %[ro], %[wb2]\n\t" \
  "v_add_u32 %[ro], 0x2000, %[ro]\n\t" "global_load_dwordx4 v[92:95], %[ro], %[wb2]\n\t" ""

#define KV_OUT "=&{v[96:127]}"(kv0), "=&{v[128:159]}"(kv1), "=&{v[160:191]}"(kv2)
#define KV_IO "+{v[96:127]}"(kv0), "+{v[128:159]}"(kv1), "+{v[160:191]}"(kv2)
#define KV_IN "{v[96:127]}"(kv0), "{v[128:159]}"(kv1), "{v[160:191]}"(kv2)
#define KV_ADDR [ka0] "v"(kb), [ka1] "v"(kb + 4096), [va0] "v"(vp), [va1] "v"(vp + 4096), [rs] "s"(rs)

__global__ __launch_bounds__(512, 2) void fused_block(const u16* __restrict__ x, const u16* __restrict__ wq, const u16* __restrict__ wk,
                                                      const u16* __restrict__ wv, const u16* __restrict__ wo, const u16* __restrict__ wr,
                                                      const u16* __restrict__ wg, const u16* __restrict__ wu, const u16* __restrict__ wd,
                                                      char* ws, u16* __restrict__ out) {
  __shared__ __attribute__((aligned(16))) char smem[L_TOTAL];
  unsigned pf1, pf2;
  {
    unsigned long long pc; unsigned t;
    asm volatile("s_getpc_b64 %[pc]\n.Lfb_begin:\n\tv_readfirstlane_b32 %[t], %[tid]\n\ts_cmp_lg_u32 %[t], 0\n\ts_cbranch_scc1 .Lfb_nopf\n\t"
                 "v_and_b32 %[a], 15, %[tid]\n\tv_lshlrev_b32 %[b], 7, %[a]\n\tglobal_load_dword %[b], %[b], %[pc]\n\t"
                 "s_lshr_b32 %[t], %[bx], 3\n\ts_mul_i32 %[t], %[t], 1152\n\ts_add_u32 %[t], %[t], 2048\n\t"
                 "v_min_u32 %[a], 8, %[a]\n\tv_lshl_add_u32 %[a], %[a], 7, %[t]\n\tv_min_u32 %[a], .Lfb_end-.Lfb_begin, %[a]\n\t"
                 "global_load_dword %[a], %[a], %[pc]\n.Lfb_nopf:"
                 : [pc] "=&s"(pc), [t] "=&s"(t), [a] "=&v"(pf1), [b] "=&v"(pf2) : [tid] "v"(threadIdx.x), [bx] "s"(blockIdx.x) : "scc", "memory");
  }
  char* const XI = smem + L_XI;
  char* const WI = smem + L_WI;
  char* const KI = smem + L_KI;
  char* const CTL = smem + L_CTL;
  const int tid = threadIdx.x, lane = tid & 63;
  const int w = __builtin_amdgcn_readfirstlane(tid >> 6);
  const int b = blockIdx.x, g8 = b & 7, tb = b & 31, hg = b >> 5;
  const int rr = lane >> 3, c = lane & 7;
  const int h = lane >> 5, l31 = lane & 31;
  const int isV = hg & 1, lr0 = (hg >> 1) * 16;
  const int pid = tb * 8 + hg;
  const int u = (b & 7) * 28 + (b >> 3);
  const rsrc_t rs = __builtin_amdgcn_make_buffer_rsrc(ws, 0, (int)WS_END, 0x00020000);
  STAMP(0, 0);

  unsigned tag;
  asm volatile("s_load_dword %0, %1, 0x0" : "=s"(tag) : "s"(ws) : "memory");
  const bool kvw = (w == (hg & 6));
  const bool ldw = (w == (hg | 1));
  u32x4 xo0, xo1, wr0, wr1, xe, xv[8];
  f32x32 wov;
  unsigned pf0 = 0;
  {
    const u16* p = x + (size_t)(tb * 64 + lr0 + 2 * w) * D_;
    GLD128(xo0, h * 1024 + l31 * 16, p);
    GLD128(xo1, h * 1024 + l31 * 16 + 512, p);
  }
#pragma unroll
  for (int i = 0; i < 2; ++i) glds16((isV ? wv : wk) + (w * 2 + i) * 512, lane * 16, KI + (w * 2 + i) * 1024);
  const unsigned row_off = rr * 1024 + c * 16;
  const unsigned wq_off = rr * 1024 + ((c ^ (((rr >> 1) & 1) << 2)) << 4);
  const u16* xrows = x + (size_t)(tb * 64 + w * 8) * D_;
  if (kvw) {
    __builtin_amdgcn_s_setprio(3);
  {
    unsigned long long ta = (unsigned long long)ws + (lane == 0 ? WS_YP + b * 5376 : (lane == 1 ? WS_K : WS_KVF));
    if (lane == 3) ta = (unsigned long long)(out + (size_t)tb * 64 * D_);
    asm volatile("global_load_dword %0, %1, off" : "=v"(pf0) : "v"(ta) : "memory");
  }
#pragma unroll
    for (int j = 0; j < 8; ++j)
      st128(rs, WS_O + (unsigned)((tb * 2 + (j >> 2)) * 32 + hg * 4 + (j & 3)) * 1024 + lane * 16, u32x4{0xffffffffu, 0xffffffffu, 0xffffffffu, 0xffffffffu});
  } else if (ldw) {
#pragma unroll
    for (int pc = 0; pc < 8; ++pc) glds16(wq + pc * 8 * D_ + hg * 64, wq_off, WI + pc * 1024);
  } else {
#pragma unroll
    for (int i = 0; i < 8; ++i) asm volatile("global_load_dwordx4 %0, %1, %2 offset:%3" : "=v"(xv[i]) : "v"(row_off), "s"(xrows), "i"(i * 128) : "memory");
  }
  VMWAIT(10);
  asm volatile("" : "+v"(xo0), "+v"(xo1), "+v"(pf1), "+v"(pf2));
  STAMP(0, 1);
  {
    float mean, var;
    stats32(xo0, xo1, mean, var);
    asm volatile("" : "+v"(xo0), "+v"(xo1));
    const float mb = rbf(mean), sd = ln_sd(var), inv = __builtin_amdgcn_rcpf(sd);
#pragma unroll
    for (int j = 0; j < 4; ++j) { xo0[j] = ln_pair(xo0[j], mb, inv); xo1[j] = ln_pair(xo1[j], mb, inv); }
    const int lrow = lr0 + 2 * w + h;
    char* xr = XI + lrow * 1024 + ((l31 ^ (lrow & 15)) << 4);
    *(u32x4*)xr = xo0; *(u32x4*)(xr + 512) = xo1;
    if (l31 == 0) *(f32x2*)(CTL + C_STAT + lrow * 8) = f32x2{mb, sd};
    if (tid < 2) *(unsigned*)(CTL + tid * 4) = 0u;
    if (kvw) VMWAIT(9); else VMWAIT(8);
    asm volatile("s_waitcnt lgkmcnt(0)" : "+s"(tag) :: "memory");
    __builtin_amdgcn_s_barrier();
    CBAR();
  }
  tag += 1u;
  STAMP(0, 2);
  if ((w >> 2) == isV) { GLD128(wr0, l31 * 16, wr); GLD128(wr1, l31 * 16 + 512, wr); }
  if (kvw) {
    const int g = lane >> 4, q = (lane & 15) >> 2, p = lane & 3;
    const char* xb = XI + (lr0 + (lane & 15)) * 1024;
    const char* wb = opq(KI + (8 * g + q) * 32 + p * 8);
    f32x4 acc = {}, act = {};
    bf16x8 fx[2][4], fw[2][4];
#define KV_LD(bf, k0) _Pragma("unroll") for (int i = 0; i < 4; ++i) { const int ks = (k0) + i; \
      fx[bf][i] = *(const bf16x8*)(xb + ((((4 * i + g) ^ (lane & 15)) << 4) + (ks >> 2) * 256)); \
      fw[bf][i] = tr_frag(wb + ks * 1024, wb + ks * 1024 + 128); }
    KV_LD(0, 0)
#pragma unroll
    for (int kb = 0; kb < 4; ++kb) {
      if (kb < 3) { KV_LD((kb + 1) & 1, 4 * kb + 4) }
      SB();
#pragma unroll
      for (int i = 0; i < 4; ++i) {
        acc = __builtin_amdgcn_mfma_f32_16x16x32_bf16(fw[kb & 1][i], fx[kb & 1][i], acc, 0, 0, 0);
        act = __builtin_amdgcn_mfma_f32_16x16x32_bf16(fx[kb & 1][i], fw[kb & 1][i], act, 0, 0, 0);
      }
      SB();
    }
    if (isV) acc = act;
    const u32x2 pk = {pack_bf(acc[0], acc[1]), pack_bf(acc[2], acc[3])};
    float n = 0.f;
    if (!isV) {
      st64(rs, WS_K + (unsigned)(tb * 64 + lr0 + (lane & 15)) * 32 + (((g & 1) << 1) | (g >> 1)) * 8, pk);
      n = wave_maxpos(add32(add16(acc[0] * acc[0] + acc[1] * acc[1] + acc[2] * acc[2] + acc[3] * acc[3])));
    } else {
      st64(rs, WS_V + (unsigned)(pid - 1 + (g >> 1)) * VHALF + (lane & 15) * 16 + (g & 1) * 8, pk);
    }
    DRAIN();
    if (lane < 8) st64(rs, WS_KVF + (unsigned)(lane * 256 + pid) * 8, u32x2{__float_as_uint(n), tag});
    __builtin_amdgcn_s_setprio(0);
    STAMP(0, 6);
  } else if (ldw) {
#pragma unroll 4
    for (int pc = 8; pc < 28; ++pc) glds16(wq + pc * 8 * D_ + hg * 64, wq_off, WI + pc * 1024);
  } else {
    const int w6 = w - (w > hg ? 2 : 0);
#pragma unroll
    for (int i = 0; i < 6; ++i) { const int pc = 28 + i * 6 + w6; glds16(wq + pc * 8 * D_ + hg * 64, wq_off, WI + pc * 1024); }
    asm volatile("s_waitcnt vmcnt(6)" : "+v"(xv[0]), "+v"(xv[1]), "+v"(xv[2]), "+v"(xv[3]), "+v"(xv[4]), "+v"(xv[5]), "+v"(xv[6]), "+v"(xv[7]) :: "memory");
    float s = 0.f, q = 0.f;
    unsigned ones = 0x3f803f80u; asm volatile("" : "+v"(ones));
#pragma unroll
    for (int i = 0; i < 8; ++i)
#pragma unroll
      for (int j = 0; j < 4; ++j) { s = dot2(xv[i][j], ones, s); q = dot2(xv[i][j], xv[i][j], q); }
    const float mean = sum8(s) * (1.0f / 512.0f);
    float var = fmaf(-mean, mean, sum8(q) * (1.0f / 512.0f));
    if (__builtin_expect(__any(!(var >= mean * mean * 0.015625f)), 0)) {
      float v = 0.f;
#pragma unroll
      for (int i = 0; i < 8; ++i)
#pragma unroll
        for (int j = 0; j < 4; ++j) { const float a = bf_lo(xv[i][j]) - mean, bb = bf_hi(xv[i][j]) - mean; v = fmaf(a, a, v); v = fmaf(bb, bb, v); }
      var = sum8(v) * (1.0f / 512.0f);
    }
    asm volatile("" : "+v"(xv[0]), "+v"(xv[1]), "+v"(xv[2]), "+v"(xv[3]), "+v"(xv[4]), "+v"(xv[5]), "+v"(xv[6]), "+v"(xv[7]));
    const float st_m = rbf(mean), st_s = ln_sd(var);
    const float inv = __builtin_amdgcn_rcpf(st_s), nb = -st_m * inv;
    const int lrow = w * 8 + rr;
#pragma unroll
    for (int i = 0; i < 8; ++i) {
      u32x4 o;
#pragma unroll
      for (int j = 0; j < 4; ++j) o[j] = pack_bf(fmaf(bf_lo(xv[i][j]), inv, nb), fmaf(bf_hi(xv[i][j]), inv, nb));
      *(u32x4*)(XI + lrow * 1024 + (((i * 8 + c) ^ (lrow & 15)) << 4)) = o;
    }
    if (c == 0) *(f32x2*)(CTL + C_STAT + lrow * 8) = f32x2{st_m, st_s};
    STAMP(0, 3);
  }
  VMWAIT(20);
  asm volatile("" : "+v"(pf0), "+v"(wr0), "+v"(wr1));
  if ((w >> 2) == isV) {
    float r = 0.f;
#pragma unroll
    for (int j = 0; j < 4; ++j) { r = dot2(xo0[j], wr0[j], r); r = dot2(xo1[j], wr1[j], r); }
    const unsigned rkey = make_key(rbf(sum32(r)), pid * 8 + 2 * (w & 3) + h);
    if (l31 == 0) *(unsigned*)(CTL + C_R + (2 * (w & 3) + h) * 4) = rkey;
  }
  if (!kvw) DRAIN();
  RAW_BARRIER();
  STAMP(0, 4);
  if (w < 4) __builtin_amdgcn_s_setprio(2); else __builtin_amdgcn_s_setprio(1);
  if (w == 4) {
    const unsigned key = lane < 8 ? *(const unsigned*)(CTL + C_R + lane * 4) : 0u;
    const unsigned k1 = wave_maxu(key), k2 = wave_maxu(key == k1 ? 0u : key);
    if (lane < 8) st128(rs, WS_CAND + (unsigned)(lane * 256 + pid) * 16, u32x4{k1, tag, k2, tag});
  }
  const int blk = (lane >> 4) & 1, q4 = (lane & 15) >> 2, p4 = lane & 3;
  if (w < 4) {
    const int nt = w & 1, tt = w >> 1;
    const int tok = tt * 32 + l31;
    const char* xb = XI + tok * 1024;
    const char* wb = WI + (8 * h + q4) * 128 + (((4 * nt + 2 * blk + (p4 >> 1)) ^ (((q4 >> 1) & 1) << 2)) << 4) + 8 * (p4 & 1);
    f32x16 acc = {};
    bf16x8 fa[2][4], fb[2][4];
#define Q_LD(bf, k0) _Pragma("unroll") for (int i = 0; i < 4; ++i) { const int ks = (k0) + i; \
      fa[bf][i] = tr_frag(wb + ks * 2048, wb + ks * 2048 + 512); \
      fb[bf][i] = *(const bf16x8*)(xb + ((((2 * (ks & 7) + h) ^ (tok & 15)) << 4) + (ks >> 3) * 256)); }
    Q_LD(0, 0)
    SB();
#pragma unroll
    for (int kb = 0; kb < 8; ++kb) {
      if (kb < 7) { Q_LD((kb + 1) & 1, 4 * kb + 4) }
#pragma unroll
      for (int i = 0; i < 4; ++i) acc = __builtin_amdgcn_mfma_f32_32x32x16_bf16(fa[kb & 1][i], fb[kb & 1][i], acc, 0, 0, 0);
#pragma unroll
      for (int i = 0; i < 4; ++i) { SGB(0x8, 1); SGB(0x100, 3); }
      SB();
    }
    const float cs = 0.25f * 1.4426950408889634f;
    float n0 = 0.f, n1 = 0.f;
#pragma unroll
    for (int r = 0; r < 8; ++r) { acc[r] *= cs; acc[r + 8] *= cs; n0 = fmaf(acc[r], acc[r], n0); n1 = fmaf(acc[r + 8], acc[r + 8], n1); }
    char* qi = smem + L_QI + (tt * 4 + nt * 2) * 1024 + lane * 16;
    *(u32x4*)qi = u32x4{pack_bf(acc[0], acc[1]), pack_bf(acc[2], acc[3]), pack_bf(acc[4], acc[5]), pack_bf(acc[6], acc[7])};
    *(u32x4*)(qi + 1024) = u32x4{pack_bf(acc[8], acc[9]), pack_bf(acc[10], acc[11]), pack_bf(acc[12], acc[13]), pack_bf(acc[14], acc[15])};
    const float nm = wave_maxpos(fmaxf(add32(n0), add32(n1)));
    if (lane == 0) {
      *(float*)(CTL + C_QMAX + w * 4) = nm;
      asm volatile("s_waitcnt lgkmcnt(0)\n\tds_add_u32 %0, %1" :: "v"((unsigned)(uintptr_t)(LAS char*)(CTL + C_CNT)), "v"(1u) : "memory");
    }
  }
  STAMP(0, 5);

  xe = *(const u32x4*)((const char*)(xrows + hg * 64) + row_off);
  f32x32 kv0, kv1, kv2;
  asm volatile(VCONST_ASM : KV_OUT : [cst] "v"((lane & 15) ? 0u : 0x3f803f80u));
  float kmax;
  {
    u32x2 f;
    SPIN {
      f = ld64(rs, WS_KVF + (unsigned)(g8 * 256 + w * 32 + l31) * 8);
      if (__all(f[1] == tag)) break;
      SLEEP(1);
    }
    CBAR();
    kmax = __uint_as_float(f[0]);
  }
  STAMP(1, 0);
  {
    const unsigned kb = WS_K + (unsigned)(w * 256 + l31) * 32 + h * 16;
    const unsigned vp = WS_V + (unsigned)(w * 32 + 2 * blk + h) * VHALF + (lane & 15) * 16;
    asm volatile(LOADS_ASM : KV_IO : KV_ADDR : "memory");
  }
  kmax = wave_maxpos(kmax);
  u32x4 gatep;
  {
    const f32x2 st = *(const f32x2*)(CTL + C_STAT + (w * 8 + rr) * 8);
    const float inv = __builtin_amdgcn_rcpf(st[1]);
#pragma unroll
    for (int j = 0; j < 4; ++j) gatep[j] = silu_pair(ln_pair(xe[j], st[0], inv));
    asm volatile("" : "+v"(gatep));
  }
  lds_wait_ge(CTL + C_CNT, 4u);
  STAMP(1, 1);
  const f32x4 qn = *(const f32x4*)(CTL + C_QMAX);
  const float qmax = fmaxf(fmaxf(qn[0], qn[1]), fmaxf(qn[2], qn[3]));
  const float bound = __builtin_amdgcn_sqrtf(qmax * kmax) * 1.02f + 0.01f;
  int fasti = __builtin_amdgcn_readfirstlane(bound <= 48.0f ? 1 : 0);
  asm volatile("" : "+s"(fasti));
  const bool fast = fasti != 0;
  const int idx = (int)0xDDDDDDDDu;
  bf16x8 zf = {};
  asm volatile("" : "+v"(zf));
  const f32x16 z16 = {};
  const char* qbase = smem + L_QI + lane * 16;
  f32x16 o;
  float shift = 0.f;
  bf16x8 qf = *(const bf16x8*)(qbase + ((w + 1) & 7) * 1024);
  const unsigned lds0 = (unsigned)(uintptr_t)(LAS char*)smem;
  unsigned qa = lds0 + L_QI + lane * 16 + ((w + 2) & 7) * 1024;
  f32x16 cc = splat16(-bound);
  f32x32 sab;
  int i = 0;
  unsigned so = ((w + 1) & 7) * PREG + 6 * PSLOT, qo = ((w + 2) & 7) * 1024; float t1_; unsigned t2_;
#define RUN_OUT "=&{v[240:255]}"(o), "+{v[192:207]}"(cc), [q] "+v"(qf), [qa] "+v"(qa), [i] "+s"(i), [so] "+s"(so), [qo] "+s"(qo), [t1] "=&v"(t1_), [t2] "=&v"(t2_)
#define RUN_IN [z] "v"(zf), [idx] "v"(idx), [sh] "v"(shift), [lo] "s"(0xffffffffull), [l16] "v"(lds0 + lane * 16), [l4] "v"(lds0 + lane * 4), \
               [qb] "v"(lds0 + L_QI + lane * 16)
  STAMP(3, 0);
  asm volatile("s_cmp_eq_u32 %[fast], 0\n\ts_cbranch_scc1 .Lfb_slow0\n\t" PROLOG0_ASM PASS0_ASM POST_ASM
               "s_branch .Lfb_out%=\n.Lfb_slow0:\n\ts_waitcnt vmcnt(0)\n\t" SWAPS_ASM "\n.Lfb_out%=:"
               : "=&{v[208:239]}"(sab), RUN_OUT, KV_IO : RUN_IN, [fast] "s"(fasti) : "scc", "memory");

  if (w == 2) {
        int lane = tid & 63; asm volatile("" : "+v"(lane));
        unsigned m1 = 0, m2 = 0;
        SPIN {
          bool ok = true; m1 = 0; m2 = 0;
#pragma unroll
          for (int j = 0; j < 4; ++j) {
            const u32x4 cd = ld128(rs, WS_CAND + (unsigned)(g8 * 256 + j * 64 + lane) * 16);
            ok = ok && cd[1] == tag && cd[3] == tag;
            m2 = max(max(m2, cd[2]), min(m1, cd[0])); m1 = max(m1, cd[0]);
          }
          if (__all(ok)) break;
          SLEEP(1);
        }
        CBAR();
        const unsigned k1 = __builtin_amdgcn_readfirstlane(wave_maxu(m1));
        const unsigned k2 = __builtin_amdgcn_readfirstlane(wave_maxu(m1 == k1 ? m2 : m1));
        if (lane == 0) *(u32x2*)(CTL + C_K1) = u32x2{k1, k2};
        if (b < NU) {
          const int h = lane >> 5, l31 = lane & 31;
          const u16* xr = x + key_tok(h ? k2 : k1) * D_ + l31 * 8;
        {
          const u32x4 xs0 = *(const u32x4*)xr, xs1 = *(const u32x4*)(xr + 256);
          float mean, var;
          stats32(xs0, xs1, mean, var);
          const float mb = rbf(mean), inv = __builtin_amdgcn_rcpf(ln_sd(var));
          u32x4 o0;
#pragma unroll
          for (int j = 0; j < 4; ++j) o0[j] = ln_pair((u & 4) ? xs1[j] : xs0[j], mb, inv);
          *(u32x4*)(smem + L_XS + h * 1024 + l31 * 16) = o0;
          asm volatile("s_waitcnt lgkmcnt(0)" ::: "memory");
          if (lane == 0) *(volatile LAS unsigned*)(CTL + C_SEL) = 1u;
        }
        }
  }
  if (w == 0 && b < NU) {
    int ln = tid; asm volatile("" : "+v"(ln));
    const unsigned go = (ln >> 3) * (F_ * 2) + (((ln & 7) ^ (((ln >> 4) & 1) << 2)) << 4);
#pragma unroll
    for (int j = 0; j < 16; ++j) glds16((j < 8 ? wg : wu) + ((u & 7) * 64 + (j & 7) * 8) * F_ + (u >> 3) * 64, go, smem + L_GU + j * 1024);
  }
  if (w == 1 && b < NU) {
#pragma unroll
    for (int f = 0; f < 8; ++f) glds16(wd + (u * 8 + f) * D_, lane * 16, smem + (f < 6 ? L_DN + f * 1088 : L_DN2 + (f - 6) * 1088));
  }
  asm volatile("" : "=&{v[64:95]}"(wov));
  const int iw = (0x33222453 >> (4 * w)) & 15;
  unsigned ro = row_off;
  unsigned long long wb2_;

#pragma nounroll
  while (i < 8) {
    if (b < NU && ((w == 0 && i == 2) || (w == 1 && i == 4))) {
      int lane = tid & 63, u = (b & 7) * 28 + (b >> 3);
      asm volatile("" : "+v"(lane), "+s"(u));
      const int h = lane >> 5, l31 = lane & 31, blk = (lane >> 4) & 1, q4 = (lane & 15) >> 2, p4 = lane & 3;
      if (i == 2) {
        DRAIN();
        lds_wait_ge(CTL + C_SEL, 1u);
        const char* xb = opq(smem + L_XS + (lane & 1) * 1024 + (u & 3) * 128 + h * 16);
        bf16x8 fx[4];
#pragma unroll
        for (int ks = 0; ks < 4; ++ks) fx[ks] = *(const bf16x8*)(xb + ks * 32);
#pragma unroll
        for (int nt = 0; nt < 2; ++nt) {
          const char* wb = opq(smem + L_GU + (8 * h + q4) * 128 + (((4 * nt + 2 * blk + (p4 >> 1)) ^ (((q4 >> 1) & 1) << 2)) << 4) + 8 * (p4 & 1));
#pragma unroll
          for (int mat = 0; mat < 2; ++mat) {
            f32x16 z = z16;
#pragma unroll
            for (int ks = 0; ks < 4; ++ks) z = QK(fx[ks], tr_frag(wb + mat * 8192 + ks * 2048, wb + mat * 8192 + ks * 2048 + 512), z);
            if (lane < 32) st128(rs, WS_P1 + (unsigned)u * 2048 + (mat * 64 + nt * 32 + lane) * 16, u32x4{__float_as_uint(z[0]), tag, __float_as_uint(z[1]), tag});
            SB();
          }
        }
      } else {
        const unsigned po = WS_P1 + (unsigned)((u & ~7) + (lane >> 3)) * 2048 + ((u & 7) * 8 + (lane & 7)) * 16;
        u32x4 pg, pu;
        SPIN {
          pg = ld128(rs, po); pu = ld128(rs, po + 1024);
          if (__all(pg[1] == tag && pg[3] == tag && pu[1] == tag && pu[3] == tag)) break;
          SLEEP(2);
        }
        DRAIN();
        float t[4] = {__uint_as_float(pg[0]), __uint_as_float(pg[2]), __uint_as_float(pu[0]), __uint_as_float(pu[2])};
#pragma unroll
        for (int j = 0; j < 4; ++j) { t[j] += dpp_f<0x128>(t[j]); t[j] = add32(add16(t[j])); }
        const unsigned hp = pack_bf(rbf(silu_f(rbf(t[0]))) * rbf(t[2]), rbf(silu_f(rbf(t[1]))) * rbf(t[3]));
        if (lane < 8) { *(u16*)(smem + L_HS + lane * 2) = (u16)hp; *(u16*)(smem + L_HS + 16 + lane * 2) = (u16)(hp >> 16); }
        u32x4 ha = *(const u32x4*)(smem + L_HS + (lane & 1) * 16);
        if (lane >= 2) ha = u32x4{0u, 0u, 0u, 0u};
        const bf16x8 A = __builtin_bit_cast(bf16x8, ha);
        const int co = (2 * blk + (p4 >> 1)) * 16 + 8 * (p4 & 1);
        const char* db0 = opq(smem + L_DN + q4 * 1088 + co);
        const char* db1 = opq(smem + (q4 < 2 ? L_DN + (4 + q4) * 1088 : L_DN2 + (q4 - 2) * 1088) + co);
#pragma unroll 2
        for (int ct = 0; ct < 16; ++ct) {
          const bf16x8 bw = tr_frag(db0 + ct * 64, db1 + ct * 64);
          const f32x16 z = QK(A, bw, z16);
          auto sw = __builtin_amdgcn_permlane32_swap(__float_as_uint(z[0]), __float_as_uint(z[1]), false, false);
          st32(rs, WS_YP + (unsigned)(h * 64 + ct * 4 + (l31 >> 3)) * (NU * 32) + u * 32 + (l31 & 7) * 4, sw[0]);
        }
        DRAIN();
        if (lane < 8) st32(rs, WS_YF + lane * 1024 + u * 4, tag);
      }
    }
  if (w < 4 && i == 7 && b < NU) __builtin_amdgcn_s_setprio(0);
    STAMP(3, i);
    int stop = 8;
    if (b < NU) {
      if (w < 4 && i < 7) stop = 7;
      if (w == 0 && i < 2) stop = 2;
      if (w == 1 && i < 4) stop = 4;
    }
    if (!fast) {
      stop = i + 1;
      float m = -3.0e38f;
      asm volatile(ROWMAX_ASM : [m] "+v"(m), "=&{v[208:239]}"(sab) : [q] "v"(qf), KV_IN);
      shift = max32(m);
      cc = splat16(-shift);
      asm volatile(PROLOG_ASM : "=&{v[208:239]}"(sab), "+{v[192:207]}"(cc) : [q] "v"(qf), KV_IN);
    }
    asm volatile(".Lfb_loop%=:\n\t" PASSN_ASM POST_ASM WOLOAD_ASM "\n.Lfb_skip%=:\n\ts_cmp_lt_u32 %[i], %[stop]\n\ts_cbranch_scc1 .Lfb_loop%=\n.Lfb_out%=:"
                 : "+{v[208:239]}"(sab), RUN_OUT, "+{v[64:95]}"(wov), [ro] "+v"(ro), [wb2] "=&s"(wb2_)
                 : RUN_IN, [stop] "s"(stop), [iw] "s"(iw), [wb] "s"(wo + (size_t)(w * 64) * D_ + hg * 64), KV_IN : "scc", "memory");
  }
  {
  int tid = threadIdx.x; asm volatile("" : "+v"(tid));
  const int lane = tid & 63, rr = lane >> 3, c = lane & 7, h = lane >> 5, l31 = lane & 31, blk = (lane >> 4) & 1, q4 = (lane & 15) >> 2, p4 = lane & 3;
  if (w < 4 && b >= NU) {
    SPIN {
      const u32x4 f = ld128(rs, WS_YF + g8 * 1024 + min(lane, 55) * 16);
      if (__all(f[0] == tag && f[1] == tag && f[2] == tag && f[3] == tag)) break;
      SLEEP(8);
    }
    CBAR();
    const unsigned rw = (unsigned)((b - NU) * 4 + w);
    const unsigned yo = WS_YP + rw * (NU * 32) + lane * 16;
    f32x4 y = {};
#pragma unroll
    for (int j = 0; j < 7; ++j) y += __builtin_bit_cast(f32x4, ld128(rs, yo + j * 1024));
#pragma unroll
    for (int j = 0; j < 4; ++j) { float t = y[j]; t += dpp_f<0x4E>(t); t += dpp_f<0x124>(t); t += dpp_f<0x128>(t); y[j] = add32(add16(t)); }
    if (lane < 2) {
      st128(rs, WS_YG + rw * 64 + lane * 32, u32x4{__float_as_uint(y[0]), tag, __float_as_uint(y[1]), tag});
      st128(rs, WS_YG + rw * 64 + lane * 32 + 16, u32x4{__float_as_uint(y[2]), tag, __float_as_uint(y[3]), tag});
    }
  }
  __builtin_amdgcn_s_setprio(0);
  DRAIN();
  const u32x4 xe = *(const u32x4*)(x + (size_t)(tb * 64 + (tid >> 3)) * D_ + hg * 64 + c * 8);
  STAMP(1, 2);
  RAW_BARRIER();
  STAMP(1, 3);
  const unsigned fr = (unsigned)((tb * 2 + (w >> 2)) * 32 + hg * 4 + (w & 3));
  {
    float a[8], pl[7], pm[7];
    auto sw = __builtin_amdgcn_permlane32_swap(__float_as_uint(o[8]), __float_as_uint(o[8]), false, false);
    float l = __uint_as_float(sw[0]);
    float mx = shift;
#pragma unroll
    for (int sidx = 0; sidx < 7; ++sidx) {
      const char* sl = smem + w * PREG + sidx * PSLOT + 2048 + l31 * 4;
      pl[sidx] = *(const float*)sl; pm[sidx] = *(const float*)(sl + 128);
      mx = fmaxf(mx, pm[sidx]);
    }
    const float f0 = __builtin_amdgcn_exp2f(shift - mx);
    l *= f0;
#pragma unroll
    for (int r = 0; r < 8; ++r) a[r] = o[r] * f0;
#pragma unroll
    for (int sidx = 0; sidx < 7; ++sidx) {
      const char* sl = smem + w * PREG + sidx * PSLOT;
      const f32x4 p0 = *(const f32x4*)(sl + lane * 16), p1 = *(const f32x4*)(sl + 1024 + lane * 16);
      const float f = __builtin_amdgcn_exp2f(pm[sidx] - mx);
#pragma unroll
      for (int r = 0; r < 4; ++r) { a[r] = fmaf(p0[r], f, a[r]); a[r + 4] = fmaf(p1[r], f, a[r + 4]); }
      l = fmaf(pl[sidx], f, l);
    }
    const float inv = __builtin_amdgcn_rcpf(l);
    const u32x4 of = {min(pack_bf(a[0] * inv, a[1] * inv), 0xfffffffeu), min(pack_bf(a[2] * inv, a[3] * inv), 0xfffffffeu),
                      min(pack_bf(a[4] * inv, a[5] * inv), 0xfffffffeu), min(pack_bf(a[6] * inv, a[7] * inv), 0xfffffffeu)};
    st128(rs, WS_O + fr * 1024 + lane * 16, of);
    *(u32x4*)(smem + L_QI + w * 1024 + lane * 16) = of;
  }
  asm volatile("s_waitcnt lgkmcnt(0)\n\tds_write_b128 %0, v[64:67]\n\tds_write_b128 %0, v[68:71] offset:1024\n\tds_write_b128 %0, v[72:75] offset:2048\n\t"
               "ds_write_b128 %0, v[76:79] offset:3072\n\tds_write_b128 %0, v[80:83] offset:4096\n\tds_write_b128 %0, v[84:87] offset:5120\n\t"
               "ds_write_b128 %0, v[88:91] offset:6144\n\tds_write_b128 %0, v[92:95] offset:7168"
               :: "v"((unsigned)(uintptr_t)(LAS char*)smem + w * PREG + rr * 128 + ((c ^ (((rr >> 1) & 1) << 2)) << 4)), "{v[64:95]}"(wov) : "memory");
  if (b == 0 && tid == 0) st32(rs, WS_EPOCH, tag);
  STAMP(1, 4);
  RAW_BARRIER();
  const int etok = tid >> 3, gtok = tb * 64 + etok;
  {
    const int tt = w >> 2, kq = w & 3;
    bf16x8 fa[2][8];
#pragma unroll
    for (int nt = 0; nt < 2; ++nt) {
      const char* wb = opq(smem + 2 * kq * PREG + (4 * h + q4) * 128 + (((4 * nt + 2 * blk + (p4 >> 1)) ^ (((q4 >> 1) & 1) << 2)) << 4) + 8 * (p4 & 1));
#pragma unroll
      for (int i = 0; i < 8; ++i) fa[nt][i] = tr_frag(wb + (i >> 2) * PREG + (i & 3) * 2048, wb + (i >> 2) * PREG + (i & 3) * 2048 + 1024);
    }
    SB();
    const unsigned fo = WS_O + (unsigned)((tb * 2 + tt) * 32 + kq * 8) * 1024 + lane * 16;
    u32x4 fb[8];
    if (hg != 2 * kq) {
#pragma unroll
      for (int i = 0; i < 4; ++i) fb[i] = ld128(rs, fo + i * 1024);
    } else {
#pragma unroll
      for (int i = 0; i < 4; ++i) fb[i] = *(const u32x4*)(smem + L_QI + (tt * 4 + i) * 1024 + lane * 16);
    }
    if (hg != 2 * kq + 1) {
#pragma unroll
      for (int i = 4; i < 8; ++i) fb[i] = ld128(rs, fo + i * 1024);
    } else {
#pragma unroll
      for (int i = 0; i < 4; ++i) fb[4 + i] = *(const u32x4*)(smem + L_QI + (tt * 4 + i) * 1024 + lane * 16);
    }
    SB();
    STAMP(2, 0);
    {
      const unsigned k1 = *(const unsigned*)(CTL + C_K1), k2 = *(const unsigned*)(CTL + C_K2);
      const int sel = (gtok == key_tok(k1)) ? 0 : ((gtok == key_tok(k2)) ? 1 : -1);
      if (__builtin_expect(__any(sel >= 0), 0)) {
        const f32x2 st = *(const f32x2*)(CTL + C_STAT + etok * 8);
        const float inv = __builtin_amdgcn_rcpf(st[1]);
        const float rsg = rbf(sigm_f(key_val(sel > 0 ? k2 : k1)));
        const unsigned go = WS_YG + (unsigned)((sel > 0 ? 512 : 0) + hg * 64 + c * 8) * 8;
        u32x4 gq[4];
        SPIN {
          bool ok = true;
#pragma unroll
          for (int j = 0; j < 4; ++j) { gq[j] = ld128(rs, go + j * 16); ok = ok && gq[j][1] == tag && gq[j][3] == tag; }
          if (__all(ok)) break;
          SLEEP(1);
        }
#pragma unroll
        for (int j = 0; j < 4; ++j) {
          const unsigned xn = ln_pair(xe[j], st[0], inv);
          const float u0 = rbf(rbf(__uint_as_float(gq[j][0])) * rsg), u1 = rbf(rbf(__uint_as_float(gq[j][2])) * rsg);
          if (sel >= 0) gatep[j] = silu_pair(pack_bf(bf_lo(xn) + u0, bf_hi(xn) + u1));
        }
      }
    }
    SB();
    STAMP(2, 1);
    f32x16 acc0 = {}, acc1 = {};
#define FB_MAX(i) max(max(fb[i][0], fb[i][1]), max(fb[i][2], fb[i][3]))
#pragma unroll
    for (int hf = 0; hf < 2; ++hf) {
      if (__builtin_expect(__any(max(max(FB_MAX(4 * hf), FB_MAX(4 * hf + 1)), max(FB_MAX(4 * hf + 2), FB_MAX(4 * hf + 3))) == 0xffffffffu), 0)) {
        _Pragma("nounroll") for (int tries = 0; tries < 64; ++tries) {
          unsigned bad = 0;
#pragma unroll
          for (int i = 4 * hf; i < 8; ++i) bad |= __any(FB_MAX(i) == 0xffffffffu) ? 1u << i : 0u;
          const unsigned need = bad & (15u << (4 * hf));
          if (!need) break;
          SPIN {
            const unsigned pv = ld32(rs, fo - lane * 16 + (lane & 7) * 1024 + 1020);
            if (!__any(pv == 0xffffffffu && ((need >> (lane & 7)) & 1u))) break;
            SLEEP(1);
          }
#pragma unroll
          for (int i = 4 * hf; i < 8; ++i) if ((bad >> i) & 1u) fb[i] = ld128(rs, fo + i * 1024);
        }
      }
#pragma unroll
      for (int i = 4 * hf; i < 4 * hf + 4; ++i) {
        acc0 = QK(fa[0][i], __builtin_bit_cast(bf16x8, fb[i]), acc0);
        acc1 = QK(fa[1][i], __builtin_bit_cast(bf16x8, fb[i]), acc1);
      }
      SB();
    }
    STAMP(2, 2);
    char* pr = smem + w * PREG + 8192 + l31 * 256;
#pragma unroll
    for (int g = 0; g < 4; ++g) {
      *(f32x4*)(pr + (((2 * g + h) ^ (l31 & 7)) << 4)) = f32x4{acc0[4 * g], acc0[4 * g + 1], acc0[4 * g + 2], acc0[4 * g + 3]};
      *(f32x4*)(pr + 128 + (((2 * g + h) ^ (l31 & 7)) << 4)) = f32x4{acc1[4 * g], acc1[4 * g + 1], acc1[4 * g + 2], acc1[4 * g + 3]};
    }
  }
  RAW_BARRIER();
  {
    f32x4 s0v = {}, s1v = {};
#pragma unroll
    for (int kq = 0; kq < 4; ++kq) {
      const char* pr = smem + ((w >> 2) * 4 + kq) * PREG + 8192 + (etok & 31) * 256 + (c >> 2) * 128;
      s0v += *(const f32x4*)(pr + ((((2 * c) & 7) ^ rr) << 4)); s1v += *(const f32x4*)(pr + ((((2 * c + 1) & 7) ^ rr) << 4));
    }
    u32x4 ov;
#pragma unroll
    for (int j = 0; j < 4; ++j) {
      const unsigned sv = j < 2 ? pack_bf(s0v[2 * j], s0v[2 * j + 1]) : pack_bf(s1v[2 * j - 4], s1v[2 * j - 3]);
      const unsigned pr2 = pack_bf(bf_lo(sv) * bf_lo(gatep[j]), bf_hi(sv) * bf_hi(gatep[j]));
      ov[j] = pack_bf(bf_lo(xe[j]) + bf_lo(pr2), bf_hi(xe[j]) + bf_hi(pr2));
    }
    *(u32x4*)(out + (size_t)gtok * D_ + hg * 64 + c * 8) = ov;
  }
  }
  asm volatile(".Lfb_end:");
  STAMP(2, 3);
}

extern "C" void kernel_launch(void* const* d_in, const int* in_sizes, int n_in, void* d_out, int out_size, void* d_ws, size_t ws_size,
                              hipStream_t stream) {
  fused_block<<<256, 512, 0, stream>>>((const u16*)d_in[0], (const u16*)d_in[1], (const u16*)d_in[2], (const u16*)d_in[3], (const u16*)d_in[4],
                                       (const u16*)d_in[6], (const u16*)d_in[7], (const u16*)d_in[8], (const u16*)d_in[9], (char*)d_ws, (u16*)d_out);
}
```

```cpp
#include <hip/hip_runtime.h>
#include <hip/hip_bf16.h>
#include <stdint.h>

typedef __attribute__((ext_vector_type(2))) __bf16 bf16x2;
typedef __attribute__((ext_vector_type(8))) __bf16 bf16x8;
typedef __attribute__((ext_vector_type(4))) short s16x4;
typedef __attribute__((ext_vector_type(8))) short s16x8;
typedef __attribute__((ext_vector_type(2))) float f32x2;
typedef __attribute__((ext_vector_type(4))) float f32x4;
typedef __attribute__((ext_vector_type(16))) float f32x16;
typedef __attribute__((ext_vector_type(32))) float f32x32;
typedef __attribute__((ext_vector_type(2))) unsigned u32x2;
typedef __attribute__((ext_vector_type(4))) unsigned u32x4;
typedef unsigned short u16;
typedef __amdgpu_buffer_rsrc_t rsrc_t;
#define LAS __attribute__((address_space(3)))
#define DI __device__ __forceinline__

constexpr int S_ = 2048, D_ = 512, F_ = 1792;
constexpr int NU = 224;
constexpr int VHALF = 256;
constexpr int SC1 = 16;
constexpr unsigned SPIN_MAX = 1u << 20;

constexpr unsigned WS_EPOCH = 0;
constexpr unsigned WS_KVF = 1024;
constexpr unsigned WS_CAND = WS_KVF + 8 * 256 * 8;
constexpr unsigned WS_YF = WS_CAND + 8 * 512 * 8;
constexpr unsigned WS_YG = WS_YF + 8 * 1024;
constexpr unsigned WS_K = WS_YG + 1024 * 8;
constexpr unsigned WS_V = WS_K + S_ * 32;
constexpr unsigned WS_YP = WS_V + 256 * VHALF;
constexpr unsigned WS_P1 = WS_YP + NU * 2 * D_ * 4;
constexpr unsigned WS_O = WS_P1 + NU * 2048;
constexpr unsigned WS_END = WS_O + S_ * D_ * 2;

constexpr int L_WI = 0;
constexpr int L_XI = 65536;
constexpr int L_KI = 131072;
constexpr int PSLOT = 2304;
constexpr int PREG = 16384;
constexpr int L_XS = 7 * PREG;
constexpr int L_GU = 131072;
constexpr int L_QI = 147456;
constexpr int L_CTL = L_QI + 8192;
constexpr int L_DN = L_CTL + 1024;
constexpr int L_DN2 = 7 * PREG + PSLOT + 128;
constexpr int L_HS = 0;
constexpr int L_TOTAL = L_DN + 6 * 1088;
constexpr int C_CNT = 0;
constexpr int C_SEL = 4;
constexpr int C_K1 = 8, C_K2 = 12;
constexpr int C_QMAX = 16;
constexpr int C_R = 32;
constexpr int C_STAT = 64;


DI float bf_lo(unsigned u) { return __uint_as_float(u << 16); }
DI float bf_hi(unsigned u) { return __uint_as_float(u & 0xffff0000u); }
DI unsigned pack_bf(float a, float b) { f32x2 f = {a, b}; return __builtin_bit_cast(unsigned, __builtin_convertvector(f, bf16x2)); }
DI float rbf(float a) { return (float)(__bf16)a; }
DI float dot2(unsigned a, unsigned b, float c) { return __builtin_amdgcn_fdot2_f32_bf16(__builtin_bit_cast(bf16x2, a), __builtin_bit_cast(bf16x2, b), c, false); }
DI void glds16(const void* sbase, unsigned voff, void* lds_wave_base) {
  unsigned keep; const unsigned dst = (unsigned)(uintptr_t)(LAS char*)lds_wave_base;
  asm volatile("s_mov_b32 %0, m0\n\ts_mov_b32 m0, %2\n\ts_nop 0\n\tglobal_load_lds_dwordx4 %1, %3\n\ts_mov_b32 m0, %0"
               : "=&s"(keep) : "v"(voff), "s"(dst), "s"(sbase) : "memory");
}
#define GLD128(dst, voff, sbase) asm volatile("global_load_dwordx4 %0, %1, %2" : "=v"(dst) : "v"(voff), "s"(sbase) : "memory")
DI const char* opq(const char* p) { unsigned a = (unsigned)(uintptr_t)(LAS char*)p; asm volatile("" : "+v"(a)); return (const char*)(LAS char*)(uintptr_t)a; }
DI s16x4 tr_read(const char* p) { return __builtin_amdgcn_ds_read_tr16_b64_v4i16((LAS s16x4*)p); }
DI bf16x8 tr_frag(const char* p0, const char* p1) {
  s16x4 a = tr_read(p0), b = tr_read(p1);
  s16x8 v = __builtin_shufflevector(a, b, 0, 1, 2, 3, 4, 5, 6, 7);
  return __builtin_bit_cast(bf16x8, v);
}
template <int CTRL> DI float dpp_f(float v) { return __int_as_float(__builtin_amdgcn_update_dpp(0, __float_as_int(v), CTRL, 0xf, 0xf, true)); }
template <int CTRL> DI unsigned dpp_u(unsigned v) { return (unsigned)__builtin_amdgcn_update_dpp(0, (int)v, CTRL, 0xf, 0xf, true); }
DI float sum8(float v) {
  v += dpp_f<0xB1>(v); v += dpp_f<0x4E>(v); v += dpp_f<0x141>(v);
  return v;
}
DI float add16(float v) { auto r = __builtin_amdgcn_permlane16_swap(__float_as_uint(v), __float_as_uint(v), false, false); return __uint_as_float(r[0]) + __uint_as_float(r[1]); }
DI float add32(float v) { auto r = __builtin_amdgcn_permlane32_swap(__float_as_uint(v), __float_as_uint(v), false, false); return __uint_as_float(r[0]) + __uint_as_float(r[1]); }
DI float max16(float v) { auto r = __builtin_amdgcn_permlane16_swap(__float_as_uint(v), __float_as_uint(v), false, false); return fmaxf(__uint_as_float(r[0]), __uint_as_float(r[1])); }
DI float max32(float v) { auto r = __builtin_amdgcn_permlane32_swap(__float_as_uint(v), __float_as_uint(v), false, false); return fmaxf(__uint_as_float(r[0]), __uint_as_float(r[1])); }
DI float sum32(float v) { v = sum8(v); v += dpp_f<0x140>(v); return add16(v); }
DI float wave_sum(float v) { return add32(sum32(v)); }
DI float wave_maxf(float v) {
  v = fmaxf(v, dpp_f<0xB1>(v)); v = fmaxf(v, dpp_f<0x4E>(v)); v = fmaxf(v, dpp_f<0x141>(v)); v = fmaxf(v, dpp_f<0x140>(v));
  return max32(max16(v));
}
DI unsigned wave_maxu(unsigned v) {
  v = max(v, dpp_u<0xB1>(v)); v = max(v, dpp_u<0x4E>(v)); v = max(v, dpp_u<0x141>(v)); v = max(v, dpp_u<0x140>(v));
  auto r = __builtin_amdgcn_permlane16_swap(v, v, false, false); v = max(r[0], r[1]);
  auto q = __builtin_amdgcn_permlane32_swap(v, v, false, false); return max(q[0], q[1]);
}
DI float wave_maxpos(float v) { return __uint_as_float(wave_maxu(__float_as_uint(v))); }
DI float silu_f(float v) { return v * __builtin_amdgcn_rcpf(1.0f + __builtin_amdgcn_exp2f(-1.4426950408889634f * v)); }
DI float sigm_f(float v) { return __builtin_amdgcn_rcpf(1.0f + __builtin_amdgcn_exp2f(-1.4426950408889634f * v)); }
DI float ln_sd(float var) { return rbf(__builtin_amdgcn_sqrtf(rbf(rbf(var) + 1e-5f))); }
DI unsigned ln_pair(unsigned xu, float mb, float inv) {
  const unsigned d = pack_bf(bf_lo(xu) - mb, bf_hi(xu) - mb);
  return pack_bf(bf_lo(d) * inv, bf_hi(d) * inv);
}
DI unsigned silu_pair(unsigned v) { return pack_bf(silu_f(bf_lo(v)), silu_f(bf_hi(v))); }

DI void stats32(const u32x4& a, const u32x4& b2, float& mean, float& var) {
  float s = 0.f, q = 0.f;
#pragma unroll
  for (int j = 0; j < 4; ++j) { s = dot2(a[j], 0x3f803f80u, s); s = dot2(b2[j], 0x3f803f80u, s); q = dot2(a[j], a[j], q); q = dot2(b2[j], b2[j], q); }
  mean = sum32(s) * (1.0f / 512.0f);
  var = fmaf(-mean, mean, sum32(q) * (1.0f / 512.0f));
  if (__builtin_expect(__any(!(var >= mean * mean * 0.015625f)), 0)) {
    float v = 0.f;
#pragma unroll
    for (int j = 0; j < 4; ++j) {
      const float a0 = bf_lo(a[j]) - mean, a1 = bf_hi(a[j]) - mean, a2 = bf_lo(b2[j]) - mean, a3 = bf_hi(b2[j]) - mean;
      v = fmaf(a0, a0, v); v = fmaf(a1, a1, v); v = fmaf(a2, a2, v); v = fmaf(a3, a3, v);
    }
    var = sum32(v) * (1.0f / 512.0f);
  }
}

DI unsigned make_key(float r_bf, int token) {
  unsigned b = __float_as_uint(r_bf) >> 16;
  b = (b & 0x8000u) ? (~b & 0xffffu) : (b | 0x8000u);
  return (b << 16) | (unsigned)(2047 - token);
}
DI float key_val(unsigned key) {
  unsigned b = key >> 16;
  b = (b & 0x8000u) ? (b & 0x7fffu) : (~b & 0xffffu);
  return __uint_as_float(b << 16);
}
DI int key_tok(unsigned key) { return 2047 - (int)(key & 0x7ffu); }

DI u32x4 ld128(rsrc_t rs, unsigned off) { return __builtin_amdgcn_raw_buffer_load_b128(rs, (int)off, 0, SC1); }
DI u32x2 ld64(rsrc_t rs, unsigned off) { return __builtin_amdgcn_raw_buffer_load_b64(rs, (int)off, 0, SC1); }
DI unsigned ld32(rsrc_t rs, unsigned off) { return __builtin_amdgcn_raw_buffer_load_b32(rs, (int)off, 0, SC1); }
DI void st128(rsrc_t rs, unsigned off, u32x4 v) { __builtin_amdgcn_raw_buffer_store_b128(v, rs, (int)off, 0, SC1); }
DI void st64(rsrc_t rs, unsigned off, u32x2 v) { __builtin_amdgcn_raw_buffer_store_b64(v, rs, (int)off, 0, SC1); }
DI void st32(rsrc_t rs, unsigned off, unsigned v) { __builtin_amdgcn_raw_buffer_store_b32(v, rs, (int)off, 0, SC1); }
#define DRAIN() asm volatile("s_waitcnt vmcnt(0)" ::: "memory")
#define VMWAIT(n) asm volatile("s_waitcnt vmcnt(" #n ")" ::: "memory")
#define CBAR() asm volatile("" ::: "memory")
#define RAW_BARRIER() do { asm volatile("s_waitcnt lgkmcnt(0)" ::: "memory"); __builtin_amdgcn_s_barrier(); asm volatile("" ::: "memory"); } while (0)
#define SLEEP(n) do { __builtin_amdgcn_s_sleep(n); asm volatile("" ::: "memory"); } while (0)
#define SPIN _Pragma("nounroll") for (unsigned s_ = 0; s_ < SPIN_MAX; ++s_)
DI unsigned lds_ld(const char* p) { return __hip_atomic_load((LAS unsigned*)p, __ATOMIC_RELAXED, __HIP_MEMORY_SCOPE_WORKGROUP); }
DI void lds_wait_ge(const char* p, unsigned want) {
  SPIN {
    if ((unsigned)__builtin_amdgcn_readfirstlane(lds_ld(p)) >= want) break;
    SLEEP(1);
  }
  CBAR();
}
#define QK(KF, QF, C) __builtin_amdgcn_mfma_f32_32x32x16_bf16((KF), (QF), (C), 0, 0, 0)
DI f32x16 splat16(float v) { f32x16 r;
#pragma unroll
  for (int i = 0; i < 16; ++i) r[i] = v;
  return r; }
#define SB() __builtin_amdgcn_sched_barrier(0)
#define SGB(mask, n) __builtin_amdgcn_sched_group_barrier(mask, n, 0)

#define VCONST_ASM \
  "v_mov_b32 v132, %[cst]\n\t" "v_mov_b32 v133, %[cst]\n\t" "v_mov_b32 v134, %[cst]\n\t" "v_mov_b32 v135, %[cst]\n\t" "v_mov_b32 v140, %[cst]\n\t" \
  "v_mov_b32 v141, %[cst]\n\t" "v_mov_b32 v142, %[cst]\n\t" "v_mov_b32 v143, %[cst]\n\t" "v_mov_b32 v148, %[cst]\n\t" "v_mov_b32 v149, %[cst]\n\t" \
  "v_mov_b32 v150, %[cst]\n\t" "v_mov_b32 v151, %[cst]\n\t" "v_mov_b32 v156, %[cst]\n\t" "v_mov_b32 v157, %[cst]\n\t" "v_mov_b32 v158, %[cst]\n\t" \
  "v_mov_b32 v159, %[cst]\n\t" "v_mov_b32 v164, %[cst]\n\t" "v_mov_b32 v165, %[cst]\n\t" "v_mov_b32 v166, %[cst]\n\t" "v_mov_b32 v167, %[cst]\n\t" \
  "v_mov_b32 v172, %[cst]\n\t" "v_mov_b32 v173, %[cst]\n\t" "v_mov_b32 v174, %[cst]\n\t" "v_mov_b32 v175, %[cst]\n\t" "v_mov_b32 v180, %[cst]\n\t" \
  "v_mov_b32 v181, %[cst]\n\t" "v_mov_b32 v182, %[cst]\n\t" "v_mov_b32 v183, %[cst]\n\t" "v_mov_b32 v188, %[cst]\n\t" "v_mov_b32 v189, %[cst]\n\t" \
  "v_mov_b32 v190, %[cst]\n\t" "v_mov_b32 v191, %[cst]\n\t" ""
#define LOADS_ASM \
  "buffer_load_dwordx4 v[96:99], %[ka0], %[rs], 0 offen offset:0 sc1\n\t" "buffer_load_dwordx4 v[128:131], %[va0], %[rs], 0 offen offset:0 sc1\n\t" \
  "buffer_load_dwordx4 v[100:103], %[ka0], %[rs], 0 offen offset:1024 sc1\n\t" "buffer_load_dwordx4 v[136:139], %[va0], %[rs], 0 offen offset:1024 sc1\n\t" \
  "buffer_load_dwordx4 v[104:107], %[ka0], %[rs], 0 offen offset:2048 sc1\n\t" "buffer_load_dwordx4 v[144:147], %[va0], %[rs], 0 offen offset:2048 sc1\n\t" \
  "buffer_load_dwordx4 v[108:111], %[ka0], %[rs], 0 offen offset:3072 sc1\n\t" "buffer_load_dwordx4 v[152:155], %[va0], %[rs], 0 offen offset:3072 sc1\n\t" \
  "buffer_load_dwordx4 v[112:115], %[ka1], %[rs], 0 offen offset:0 sc1\n\t" "buffer_load_dwordx4 v[160:163], %[va1], %[rs], 0 offen offset:0 sc1\n\t" \
  "buffer_load_dwordx4 v[116:119], %[ka1], %[rs], 0 offen offset:1024 sc1\n\t" "buffer_load_dwordx4 v[168:171], %[va1], %[rs], 0 offen offset:1024 sc1\n\t" \
  "buffer_load_dwordx4 v[120:123], %[ka1], %[rs], 0 offen offset:2048 sc1\n\t" "buffer_load_dwordx4 v[176:179], %[va1], %[rs], 0 offen offset:2048 sc1\n\t" \
  "buffer_load_dwordx4 v[124:127], %[ka1], %[rs], 0 offen offset:3072 sc1\n\t" "buffer_load_dwordx4 v[184:187], %[va1], %[rs], 0 offen offset:3072 sc1\n\t" ""
#define SWAPS_ASM \
  "v_permlane16_swap_b32 v128, v132\n\t" "v_permlane16_swap_b32 v129, v133\n\t" "v_permlane16_swap_b32 v130, v134\n\t" "v_permlane16_swap_b32 v131, v135\n\t" \
  "v_permlane16_swap_b32 v136, v140\n\t" "v_permlane16_swap_b32 v137, v141\n\t" "v_permlane16_swap_b32 v138, v142\n\t" "v_permlane16_swap_b32 v139, v143\n\t" \
  "v_permlane16_swap_b32 v144, v148\n\t" "v_permlane16_swap_b32 v145, v149\n\t" "v_permlane16_swap_b32 v146, v150\n\t" "v_permlane16_swap_b32 v147, v151\n\t" \
  "v_permlane16_swap_b32 v152, v156\n\t" "v_permlane16_swap_b32 v153, v157\n\t" "v_permlane16_swap_b32 v154, v158\n\t" "v_permlane16_swap_b32 v155, v159\n\t" \
  "v_permlane16_swap_b32 v160, v164\n\t" "v_permlane16_swap_b32 v161, v165\n\t" "v_permlane16_swap_b32 v162, v166\n\t" "v_permlane16_swap_b32 v163, v167\n\t" \
  "v_permlane16_swap_b32 v168, v172\n\t" "v_permlane16_swap_b32 v169, v173\n\t" "v_permlane16_swap_b32 v170, v174\n\t" "v_permlane16_swap_b32 v171, v175\n\t" \
  "v_permlane16_swap_b32 v176, v180\n\t" "v_permlane16_swap_b32 v177, v181\n\t" "v_permlane16_swap_b32 v178, v182\n\t" "v_permlane16_swap_b32 v179, v183\n\t" \
  "v_permlane16_swap_b32 v184, v188\n\t" "v_permlane16_swap_b32 v185, v189\n\t" "v_permlane16_swap_b32 v186, v190\n\t" "v_permlane16_swap_b32 v187, v191\n\t" ""
#define PROLOG0_ASM \
  "s_waitcnt vmcnt(15)\n\t" "s_nop 1\n\t" "v_mfma_f32_32x32x16_bf16 v[208:223], v[96:99], %[q], v[192:207]\n\t" "s_waitcnt vmcnt(13)\n\t" \
  "v_mfma_f32_32x32x16_bf16 v[224:239], v[100:103], %[q], v[192:207]\n\t" "v_permlane16_swap_b32 v128, v132\n\t" "v_permlane16_swap_b32 v129, v133\n\t" \
  "v_permlane16_swap_b32 v130, v134\n\t" "v_permlane16_swap_b32 v131, v135\n\t" "s_nop 6\n\t" "v_exp_f32 v208, v208\n\t" "v_exp_f32 v209, v209\n\t" \
  "v_exp_f32 v210, v210\n\t" "v_exp_f32 v211, v211\n\t" "v_exp_f32 v212, v212\n\t" "v_exp_f32 v213, v213\n\t" "v_exp_f32 v214, v214\n\t" "v_exp_f32 v215, v215\n\t" \
  "v_exp_f32 v216, v216\n\t" "v_exp_f32 v217, v217\n\t" "v_exp_f32 v218, v218\n\t" "v_exp_f32 v219, v219\n\t" "v_exp_f32 v220, v220\n\t" ""
#define PROLOG_ASM \
  "s_nop 1\n\t" "v_mfma_f32_32x32x16_bf16 v[208:223], v[96:99], %[q], v[192:207]\n\t" "v_mfma_f32_32x32x16_bf16 v[224:239], v[100:103], %[q], v[192:207]\n\t" \
  "s_nop 10\n\t" "v_exp_f32 v208, v208\n\t" "v_exp_f32 v209, v209\n\t" "v_exp_f32 v210, v210\n\t" "v_exp_f32 v211, v211\n\t" "v_exp_f32 v212, v212\n\t" \
  "v_exp_f32 v213, v213\n\t" "v_exp_f32 v214, v214\n\t" "v_exp_f32 v215, v215\n\t" "v_exp_f32 v216, v216\n\t" "v_exp_f32 v217, v217\n\t" "v_exp_f32 v218, v218\n\t" \
  "v_exp_f32 v219, v219\n\t" "v_exp_f32 v220, v220\n\t" ""
#define PASS0_ASM \
  "v_mfma_f32_32x32x16_bf16 v[240:255], %[z], %[z], 0\n\t" "v_exp_f32 v221, v221\n\t" "v_exp_f32 v222, v222\n\t" "v_exp_f32 v223, v223\n\t" \
  "v_smfmac_f32_32x32x32_bf16 v[240:255], v[128:131], v[208:215], %[idx]\n\t" "v_exp_f32 v224, v224\n\t" "v_exp_f32 v225, v225\n\t" \
  "v_smfmac_f32_32x32x32_bf16 v[240:255], v[132:135], v[216:223], %[idx]\n\t" "v_exp_f32 v226, v226\n\t" "v_exp_f32 v227, v227\n\t" "v_exp_f32 v228, v228\n\t" \
  "s_waitcnt vmcnt(11)\n\t" "v_mfma_f32_32x32x16_bf16 v[208:223], v[104:107], %[q], v[192:207]\n\t" "v_permlane16_swap_b32 v136, v140\n\t" \
  "v_permlane16_swap_b32 v137, v141\n\t" "v_permlane16_swap_b32 v138, v142\n\t" "v_permlane16_swap_b32 v139, v143\n\t" "v_exp_f32 v229, v229\n\t" \
  "v_exp_f32 v230, v230\n\t" "v_exp_f32 v231, v231\n\t" "v_exp_f32 v232, v232\n\t" "v_exp_f32 v233, v233\n\t" \
  "v_smfmac_f32_32x32x32_bf16 v[240:255], v[136:139], v[224:231], %[idx]\n\t" "v_exp_f32 v234, v234\n\t" "v_exp_f32 v235, v235\n\t" "v_exp_f32 v236, v236\n\t" \
  "v_exp_f32 v237, v237\n\t" "v_exp_f32 v238, v238\n\t" "v_exp_f32 v239, v239\n\t" "v_exp_f32 v208, v208\n\t" "v_exp_f32 v209, v209\n\t" \
  "v_smfmac_f32_32x32x32_bf16 v[240:255], v[140:143], v[232:239], %[idx]\n\t" "v_exp_f32 v210, v210\n\t" "v_exp_f32 v211, v211\n\t" "v_exp_f32 v212, v212\n\t" \
  "s_waitcnt vmcnt(9)\n\t" "v_mfma_f32_32x32x16_bf16 v[224:239], v[108:111], %[q], v[192:207]\n\t" "v_permlane16_swap_b32 v144, v148\n\t" \
  "v_permlane16_swap_b32 v145, v149\n\t" "v_permlane16_swap_b32 v146, v150\n\t" "v_permlane16_swap_b32 v147, v151\n\t" "v_exp_f32 v213, v213\n\t" \
  "v_exp_f32 v214, v214\n\t" "v_exp_f32 v215, v215\n\t" "v_exp_f32 v216, v216\n\t" "v_exp_f32 v217, v217\n\t" \
  "v_smfmac_f32_32x32x32_bf16 v[240:255], v[144:147], v[208:215], %[idx]\n\t" "v_exp_f32 v218, v218\n\t" "v_exp_f32 v219, v219\n\t" "v_exp_f32 v220, v220\n\t" \
  "v_exp_f32 v221, v221\n\t" "v_exp_f32 v222, v222\n\t" "v_exp_f32 v223, v223\n\t" "v_exp_f32 v224, v224\n\t" "v_exp_f32 v225, v225\n\t" \
  "v_smfmac_f32_32x32x32_bf16 v[240:255], v[148:151], v[216:223], %[idx]\n\t" "v_exp_f32 v226, v226\n\t" "v_exp_f32 v227, v227\n\t" "v_exp_f32 v228, v228\n\t" \
  "s_waitcnt vmcnt(7)\n\t" "v_mfma_f32_32x32x16_bf16 v[208:223], v[112:115], %[q], v[192:207]\n\t" "v_permlane16_swap_b32 v152, v156\n\t" \
  "v_permlane16_swap_b32 v153, v157\n\t" "v_permlane16_swap_b32 v154, v158\n\t" "v_permlane16_swap_b32 v155, v159\n\t" "v_exp_f32 v229, v229\n\t" \
  "v_exp_f32 v230, v230\n\t" "v_exp_f32 v231, v231\n\t" "v_exp_f32 v232, v232\n\t" "v_exp_f32 v233, v233\n\t" \
  "v_smfmac_f32_32x32x32_bf16 v[240:255], v[152:155], v[224:231], %[idx]\n\t" "v_exp_f32 v234, v234\n\t" "v_exp_f32 v235, v235\n\t" "v_exp_f32 v236, v236\n\t" \
  "v_exp_f32 v237, v237\n\t" "v_exp_f32 v238, v238\n\t" "v_exp_f32 v239, v239\n\t" "v_exp_f32 v208, v208\n\t" "v_exp_f32 v209, v209\n\t" \
  "v_smfmac_f32_32x32x32_bf16 v[240:255], v[156:159], v[232:239], %[idx]\n\t" "v_exp_f32 v210, v210\n\t" "v_exp_f32 v211, v211\n\t" "v_exp_f32 v212, v212\n\t" \
  "s_waitcnt vmcnt(5)\n\t" "v_mfma_f32_32x32x16_bf16 v[224:239], v[116:119], %[q], v[192:207]\n\t" "v_permlane16_swap_b32 v160, v164\n\t" \
  "v_permlane16_swap_b32 v161, v165\n\t" "v_permlane16_swap_b32 v162, v166\n\t" "v_permlane16_swap_b32 v163, v167\n\t" "v_exp_f32 v213, v213\n\t" \
  "v_exp_f32 v214, v214\n\t" "v_exp_f32 v215, v215\n\t" "v_exp_f32 v216, v216\n\t" "v_exp_f32 v217, v217\n\t" \
  "v_smfmac_f32_32x32x32_bf16 v[240:255], v[160:163], v[208:215], %[idx]\n\t" "v_exp_f32 v218, v218\n\t" "v_exp_f32 v219, v219\n\t" "v_exp_f32 v220, v220\n\t" \
  "v_exp_f32 v221, v221\n\t" "v_exp_f32 v222, v222\n\t" "v_exp_f32 v223, v223\n\t" "v_exp_f32 v224, v224\n\t" "v_exp_f32 v225, v225\n\t" \
  "v_smfmac_f32_32x32x32_bf16 v[240:255], v[164:167], v[216:223], %[idx]\n\t" "v_exp_f32 v226, v226\n\t" "v_exp_f32 v227, v227\n\t" "v_exp_f32 v228, v228\n\t" \
  "s_waitcnt vmcnt(3)\n\t" "v_mfma_f32_32x32x16_bf16 v[208:223], v[120:123], %[q], v[192:207]\n\t" "v_permlane16_swap_b32 v168, v172\n\t" \
  "v_permlane16_swap_b32 v169, v173\n\t" "v_permlane16_swap_b32 v170, v174\n\t" "v_permlane16_swap_b32 v171, v175\n\t" "v_exp_f32 v229, v229\n\t" \
  "v_exp_f32 v230, v230\n\t" "v_exp_f32 v231, v231\n\t" "v_exp_f32 v232, v232\n\t" "v_exp_f32 v233, v233\n\t" \
  "v_smfmac_f32_32x32x32_bf16 v[240:255], v[168:171], v[224:231], %[idx]\n\t" "v_exp_f32 v234, v234\n\t" "v_exp_f32 v235, v235\n\t" "v_exp_f32 v236, v236\n\t" \
  "v_exp_f32 v237, v237\n\t" "v_exp_f32 v238, v238\n\t" "v_exp_f32 v239, v239\n\t" "v_exp_f32 v208, v208\n\t" "v_exp_f32 v209, v209\n\t" \
  "v_smfmac_f32_32x32x32_bf16 v[240:255], v[172:175], v[232:239], %[idx]\n\t" "v_exp_f32 v210, v210\n\t" "v_exp_f32 v211, v211\n\t" "v_exp_f32 v212, v212\n\t" \
  "s_waitcnt vmcnt(1)\n\t" "v_mfma_f32_32x32x16_bf16 v[224:239], v[124:127], %[q], v[192:207]\n\t" "ds_read_b128 %[q], %[qa]\n\t" "v_permlane16_swap_b32 v176, v180\n\t" \
  "v_permlane16_swap_b32 v177, v181\n\t" "v_permlane16_swap_b32 v178, v182\n\t" "v_permlane16_swap_b32 v179, v183\n\t" "v_exp_f32 v213, v213\n\t" \
  "v_exp_f32 v214, v214\n\t" "v_exp_f32 v215, v215\n\t" "v_exp_f32 v216, v216\n\t" "v_exp_f32 v217, v217\n\t" \
  "v_smfmac_f32_32x32x32_bf16 v[240:255], v[176:179], v[208:215], %[idx]\n\t" "v_exp_f32 v218, v218\n\t" "v_exp_f32 v219, v219\n\t" "v_exp_f32 v220, v220\n\t" \
  "v_exp_f32 v221, v221\n\t" "v_exp_f32 v222, v222\n\t" "v_exp_f32 v223, v223\n\t" "v_exp_f32 v224, v224\n\t" "v_exp_f32 v225, v225\n\t" \
  "v_smfmac_f32_32x32x32_bf16 v[240:255], v[180:183], v[216:223], %[idx]\n\t" "v_exp_f32 v226, v226\n\t" "v_exp_f32 v227, v227\n\t" "v_exp_f32 v228, v228\n\t" \
  "s_waitcnt vmcnt(0) lgkmcnt(0)\n\t" "v_mfma_f32_32x32x16_bf16 v[208:223], v[96:99], %[q], v[192:207]\n\t" "v_permlane16_swap_b32 v184, v188\n\t" \
  "v_permlane16_swap_b32 v185, v189\n\t" "v_permlane16_swap_b32 v186, v190\n\t" "v_permlane16_swap_b32 v187, v191\n\t" "v_exp_f32 v229, v229\n\t" \
  "v_exp_f32 v230, v230\n\t" "v_exp_f32 v231, v231\n\t" "v_exp_f32 v232, v232\n\t" "v_exp_f32 v233, v233\n\t" \
  "v_smfmac_f32_32x32x32_bf16 v[240:255], v[184:187], v[224:231], %[idx]\n\t" "v_exp_f32 v234, v234\n\t" "v_exp_f32 v235, v235\n\t" "v_exp_f32 v236, v236\n\t" \
  "v_exp_f32 v237, v237\n\t" "v_exp_f32 v238, v238\n\t" "v_exp_f32 v239, v239\n\t" "v_exp_f32 v208, v208\n\t" "v_exp_f32 v209, v209\n\t" \
  "v_smfmac_f32_32x32x32_bf16 v[240:255], v[188:191], v[232:239], %[idx]\n\t" "v_exp_f32 v210, v210\n\t" "v_exp_f32 v211, v211\n\t" "v_exp_f32 v212, v212\n\t" \
  "v_mfma_f32_32x32x16_bf16 v[224:239], v[100:103], %[q], v[192:207]\n\t" "v_exp_f32 v213, v213\n\t" "v_exp_f32 v214, v214\n\t" "v_exp_f32 v215, v215\n\t" \
  "v_exp_f32 v216, v216\n\t" "v_exp_f32 v217, v217\n\t" "v_exp_f32 v218, v218\n\t" "v_exp_f32 v219, v219\n\t" "v_exp_f32 v220, v220\n\t" ""
#define PASSN_ASM \
  "v_mfma_f32_32x32x16_bf16 v[240:255], %[z], %[z], 0\n\t" "v_exp_f32 v221, v221\n\t" "v_exp_f32 v222, v222\n\t" "v_exp_f32 v223, v223\n\t" \
  "v_smfmac_f32_32x32x32_bf16 v[240:255], v[128:131], v[208:215], %[idx]\n\t" "v_exp_f32 v224, v224\n\t" "v_exp_f32 v225, v225\n\t" \
  "v_smfmac_f32_32x32x32_bf16 v[240:255], v[132:135], v[216:223], %[idx]\n\t" "v_exp_f32 v226, v226\n\t" "v_exp_f32 v227, v227\n\t" "v_exp_f32 v228, v228\n\t" \
  "v_mfma_f32_32x32x16_bf16 v[208:223], v[104:107], %[q], v[192:207]\n\t" "v_exp_f32 v229, v229\n\t" "v_exp_f32 v230, v230\n\t" "v_exp_f32 v231, v231\n\t" \
  "v_exp_f32 v232, v232\n\t" "v_exp_f32 v233, v233\n\t" "v_smfmac_f32_32x32x32_bf16 v[240:255], v[136:139], v[224:231], %[idx]\n\t" "v_exp_f32 v234, v234\n\t" \
  "v_exp_f32 v235, v235\n\t" "v_exp_f32 v236, v236\n\t" "v_exp_f32 v237, v237\n\t" "v_exp_f32 v238, v238\n\t" "v_exp_f32 v239, v239\n\t" "v_exp_f32 v208, v208\n\t" \
  "v_exp_f32 v209, v209\n\t" "v_smfmac_f32_32x32x32_bf16 v[240:255], v[140:143], v[232:239], %[idx]\n\t" "v_exp_f32 v210, v210\n\t" "v_exp_f32 v211, v211\n\t" \
  "v_exp_f32 v212, v212\n\t" "v_mfma_f32_32x32x16_bf16 v[224:239], v[108:111], %[q], v[192:207]\n\t" "v_exp_f32 v213, v213\n\t" "v_exp_f32 v214, v214\n\t" \
  "v_exp_f32 v215, v215\n\t" "v_exp_f32 v216, v216\n\t" "v_exp_f32 v217, v217\n\t" "v_smfmac_f32_32x32x32_bf16 v[240:255], v[144:147], v[208:215], %[idx]\n\t" \
  "v_exp_f32 v218, v218\n\t" "v_exp_f32 v219, v219\n\t" "v_exp_f32 v220, v220\n\t" "v_exp_f32 v221, v221\n\t" "v_exp_f32 v222, v222\n\t" "v_exp_f32 v223, v223\n\t" \
  "v_exp_f32 v224, v224\n\t" "v_exp_f32 v225, v225\n\t" "v_smfmac_f32_32x32x32_bf16 v[240:255], v[148:151], v[216:223], %[idx]\n\t" "v_exp_f32 v226, v226\n\t" \
  "v_exp_f32 v227, v227\n\t" "v_exp_f32 v228, v228\n\t" "v_mfma_f32_32x32x16_bf16 v[208:223], v[112:115], %[q], v[192:207]\n\t" "v_exp_f32 v229, v229\n\t" \
  "v_exp_f32 v230, v230\n\t" "v_exp_f32 v231, v231\n\t" "v_exp_f32 v232, v232\n\t" "v_exp_f32 v233, v233\n\t" \
  "v_smfmac_f32_32x32x32_bf16 v[240:255], v[152:155], v[224:231], %[idx]\n\t" "v_exp_f32 v234, v234\n\t" "v_exp_f32 v235, v235\n\t" "v_exp_f32 v236, v236\n\t" \
  "v_exp_f32 v237, v237\n\t" "v_exp_f32 v238, v238\n\t" "v_exp_f32 v239, v239\n\t" "v_exp_f32 v208, v208\n\t" "v_exp_f32 v209, v209\n\t" \
  "v_smfmac_f32_32x32x32_bf16 v[240:255], v[156:159], v[232:239], %[idx]\n\t" "v_exp_f32 v210, v210\n\t" "v_exp_f32 v211, v211\n\t" "v_exp_f32 v212, v212\n\t" \
  "v_mfma_f32_32x32x16_bf16 v[224:239], v[116:119], %[q], v[192:207]\n\t" "v_exp_f32 v213, v213\n\t" "v_exp_f32 v214, v214\n\t" "v_exp_f32 v215, v215\n\t" \
  "v_exp_f32 v216, v216\n\t" "v_exp_f32 v217, v217\n\t" "v_smfmac_f32_32x32x32_bf16 v[240:255], v[160:163], v[208:215], %[idx]\n\t" "v_exp_f32 v218, v218\n\t" \
  "v_exp_f32 v219, v219\n\t" "v_exp_f32 v220, v220\n\t" "v_exp_f32 v221, v221\n\t" "v_exp_f32 v222, v222\n\t" "v_exp_f32 v223, v223\n\t" "v_exp_f32 v224, v224\n\t" \
  "v_exp_f32 v225, v225\n\t" "v_smfmac_f32_32x32x32_bf16 v[240:255], v[164:167], v[216:223], %[idx]\n\t" "v_exp_f32 v226, v226\n\t" "v_exp_f32 v227, v227\n\t" \
  "v_exp_f32 v228, v228\n\t" "v_mfma_f32_32x32x16_bf16 v[208:223], v[120:123], %[q], v[192:207]\n\t" "v_exp_f32 v229, v229\n\t" "v_exp_f32 v230, v230\n\t" \
  "v_exp_f32 v231, v231\n\t" "v_exp_f32 v232, v232\n\t" "v_exp_f32 v233, v233\n\t" "v_smfmac_f32_32x32x32_bf16 v[240:255], v[168:171], v[224:231], %[idx]\n\t" \
  "v_exp_f32 v234, v234\n\t" "v_exp_f32 v235, v235\n\t" "v_exp_f32 v236, v236\n\t" "v_exp_f32 v237, v237\n\t" "v_exp_f32 v238, v238\n\t" "v_exp_f32 v239, v239\n\t" \
  "v_exp_f32 v208, v208\n\t" "v_exp_f32 v209, v209\n\t" "v_smfmac_f32_32x32x32_bf16 v[240:255], v[172:175], v[232:239], %[idx]\n\t" "v_exp_f32 v210, v210\n\t" \
  "v_exp_f32 v211, v211\n\t" "v_exp_f32 v212, v212\n\t" "v_mfma_f32_32x32x16_bf16 v[224:239], v[124:127], %[q], v[192:207]\n\t" "ds_read_b128 %[q], %[qa]\n\t" \
  "v_exp_f32 v213, v213\n\t" "v_exp_f32 v214, v214\n\t" "v_exp_f32 v215, v215\n\t" "v_exp_f32 v216, v216\n\t" "v_exp_f32 v217, v217\n\t" \
  "v_smfmac_f32_32x32x32_bf16 v[240:255], v[176:179], v[208:215], %[idx]\n\t" "v_exp_f32 v218, v218\n\t" "v_exp_f32 v219, v219\n\t" "v_exp_f32 v220, v220\n\t" \
  "v_exp_f32 v221, v221\n\t" "v_exp_f32 v222, v222\n\t" "v_exp_f32 v223, v223\n\t" "v_exp_f32 v224, v224\n\t" "v_exp_f32 v225, v225\n\t" \
  "v_smfmac_f32_32x32x32_bf16 v[240:255], v[180:183], v[216:223], %[idx]\n\t" "v_exp_f32 v226, v226\n\t" "v_exp_f32 v227, v227\n\t" "v_exp_f32 v228, v228\n\t" \
  "s_waitcnt lgkmcnt(0)\n\t" "v_mfma_f32_32x32x16_bf16 v[208:223], v[96:99], %[q], v[192:207]\n\t" "v_exp_f32 v229, v229\n\t" "v_exp_f32 v230, v230\n\t" \
  "v_exp_f32 v231, v231\n\t" "v_exp_f32 v232, v232\n\t" "v_exp_f32 v233, v233\n\t" "v_smfmac_f32_32x32x32_bf16 v[240:255], v[184:187], v[224:231], %[idx]\n\t" \
  "v_exp_f32 v234, v234\n\t" "v_exp_f32 v235, v235\n\t" "v_exp_f32 v236, v236\n\t" "v_exp_f32 v237, v237\n\t" "v_exp_f32 v238, v238\n\t" "v_exp_f32 v239, v239\n\t" \
  "v_exp_f32 v208, v208\n\t" "v_exp_f32 v209, v209\n\t" "v_smfmac_f32_32x32x32_bf16 v[240:255], v[188:191], v[232:239], %[idx]\n\t" "v_exp_f32 v210, v210\n\t" \
  "v_exp_f32 v211, v211\n\t" "v_exp_f32 v212, v212\n\t" "v_mfma_f32_32x32x16_bf16 v[224:239], v[100:103], %[q], v[192:207]\n\t" "v_exp_f32 v213, v213\n\t" \
  "v_exp_f32 v214, v214\n\t" "v_exp_f32 v215, v215\n\t" "v_exp_f32 v216, v216\n\t" "v_exp_f32 v217, v217\n\t" "v_exp_f32 v218, v218\n\t" "v_exp_f32 v219, v219\n\t" \
  "v_exp_f32 v220, v220\n\t" ""
#define ROWMAX_ASM \
  "s_nop 1\n\t" "v_mfma_f32_32x32x16_bf16 v[224:239], v[96:99], %[q], 0\n\t" "s_nop 11\n\t" "v_max3_f32 %[m], %[m], v224, v225\n\t" \
  "v_max3_f32 %[m], %[m], v226, v227\n\t" "v_max3_f32 %[m], %[m], v228, v229\n\t" "v_max3_f32 %[m], %[m], v230, v231\n\t" "v_max3_f32 %[m], %[m], v232, v233\n\t" \
  "v_max3_f32 %[m], %[m], v234, v235\n\t" "v_max3_f32 %[m], %[m], v236, v237\n\t" "v_max3_f32 %[m], %[m], v238, v239\n\t" \
  "v_mfma_f32_32x32x16_bf16 v[224:239], v[100:103], %[q], 0\n\t" "s_nop 11\n\t" "v_max3_f32 %[m], %[m], v224, v225\n\t" "v_max3_f32 %[m], %[m], v226, v227\n\t" \
  "v_max3_f32 %[m], %[m], v228, v229\n\t" "v_max3_f32 %[m], %[m], v230, v231\n\t" "v_max3_f32 %[m], %[m], v232, v233\n\t" "v_max3_f32 %[m], %[m], v234, v235\n\t" \
  "v_max3_f32 %[m], %[m], v236, v237\n\t" "v_max3_f32 %[m], %[m], v238, v239\n\t" "v_mfma_f32_32x32x16_bf16 v[224:239], v[104:107], %[q], 0\n\t" "s_nop 11\n\t" \
  "v_max3_f32 %[m], %[m], v224, v225\n\t" "v_max3_f32 %[m], %[m], v226, v227\n\t" "v_max3_f32 %[m], %[m], v228, v229\n\t" "v_max3_f32 %[m], %[m], v230, v231\n\t" \
  "v_max3_f32 %[m], %[m], v232, v233\n\t" "v_max3_f32 %[m], %[m], v234, v235\n\t" "v_max3_f32 %[m], %[m], v236, v237\n\t" "v_max3_f32 %[m], %[m], v238, v239\n\t" \
  "v_mfma_f32_32x32x16_bf16 v[224:239], v[108:111], %[q], 0\n\t" "s_nop 11\n\t" "v_max3_f32 %[m], %[m], v224, v225\n\t" "v_max3_f32 %[m], %[m], v226, v227\n\t" \
  "v_max3_f32 %[m], %[m], v228, v229\n\t" "v_max3_f32 %[m], %[m], v230, v231\n\t" "v_max3_f32 %[m], %[m], v232, v233\n\t" "v_max3_f32 %[m], %[m], v234, v235\n\t" \
  "v_max3_f32 %[m], %[m], v236, v237\n\t" "v_max3_f32 %[m], %[m], v238, v239\n\t" "v_mfma_f32_32x32x16_bf16 v[224:239], v[112:115], %[q], 0\n\t" "s_nop 11\n\t" \
  "v_max3_f32 %[m], %[m], v224, v225\n\t" "v_max3_f32 %[m], %[m], v226, v227\n\t" "v_max3_f32 %[m], %[m], v228, v229\n\t" "v_max3_f32 %[m], %[m], v230, v231\n\t" \
  "v_max3_f32 %[m], %[m], v232, v233\n\t" "v_max3_f32 %[m], %[m], v234, v235\n\t" "v_max3_f32 %[m], %[m], v236, v237\n\t" "v_max3_f32 %[m], %[m], v238, v239\n\t" \
  "v_mfma_f32_32x32x16_bf16 v[224:239], v[116:119], %[q], 0\n\t" "s_nop 11\n\t" "v_max3_f32 %[m], %[m], v224, v225\n\t" "v_max3_f32 %[m], %[m], v226, v227\n\t" \
  "v_max3_f32 %[m], %[m], v228, v229\n\t" "v_max3_f32 %[m], %[m], v230, v231\n\t" "v_max3_f32 %[m], %[m], v232, v233\n\t" "v_max3_f32 %[m], %[m], v234, v235\n\t" \
  "v_max3_f32 %[m], %[m], v236, v237\n\t" "v_max3_f32 %[m], %[m], v238, v239\n\t" "v_mfma_f32_32x32x16_bf16 v[224:239], v[120:123], %[q], 0\n\t" "s_nop 11\n\t" \
  "v_max3_f32 %[m], %[m], v224, v225\n\t" "v_max3_f32 %[m], %[m], v226, v227\n\t" "v_max3_f32 %[m], %[m], v228, v229\n\t" "v_max3_f32 %[m], %[m], v230, v231\n\t" \
  "v_max3_f32 %[m], %[m], v232, v233\n\t" "v_max3_f32 %[m], %[m], v234, v235\n\t" "v_max3_f32 %[m], %[m], v236, v237\n\t" "v_max3_f32 %[m], %[m], v238, v239\n\t" \
  "v_mfma_f32_32x32x16_bf16 v[224:239], v[124:127], %[q], 0\n\t" "s_nop 11\n\t" "v_max3_f32 %[m], %[m], v224, v225\n\t" "v_max3_f32 %[m], %[m], v226, v227\n\t" \
  "v_max3_f32 %[m], %[m], v228, v229\n\t" "v_max3_f32 %[m], %[m], v230, v231\n\t" "v_max3_f32 %[m], %[m], v232, v233\n\t" "v_max3_f32 %[m], %[m], v234, v235\n\t" \
  "v_max3_f32 %[m], %[m], v236, v237\n\t" "v_max3_f32 %[m], %[m], v238, v239\n\t" ""
#define POST_ASM \
  "s_add_u32 %[i], %[i], 1\n\t" "s_cmp_eq_u32 %[i], 8\n\t" "s_cbranch_scc1 .Lfb_out%=\n\t" "v_cndmask_b32 %[t1], %[sh], v248, %[lo]\n\t" \
  "v_add_u32 %[t2], %[so], %[l16]\n\t" "ds_write_b128 %[t2], v[240:243]\n\t" "ds_write_b128 %[t2], v[244:247] offset:1024\n\t" "v_add_u32 %[t2], %[so], %[l4]\n\t" \
  "ds_write_b32 %[t2], %[t1] offset:2048\n\t" "s_add_u32 %[so], %[so], 14080\n\t" "s_and_b32 %[so], %[so], 0x1ffff\n\t" "s_add_u32 %[qo], %[qo], 1024\n\t" \
  "s_and_b32 %[qo], %[qo], 0x1fff\n\t" "v_add_u32 %[qa], %[qo], %[qb]\n\t" ""
#define WOLOAD_ASM \
  "s_cmp_lg_u32 %[i], %[iw]\n\t" "s_cbranch_scc1 .Lfb_skip%=\n\t" "s_mov_b64 %[wb2], %[wb]\n\t" "global_load_dwordx4 v[64:67], %[ro], %[wb2]\n\t" \
  "v_add_u32 %[ro], 0x2000, %[ro]\n\t" "global_load_dwordx4 v[68:71], %[ro], %[wb2]\n\t" "v_add_u32 %[ro], 0x2000, %[ro]\n\t" \
  "global_load_dwordx4 v[72:75], %[ro], %[wb2]\n\t" "v_add_u32 %[ro], 0x2000, %[ro]\n\t" "global_load_dwordx4 v[76:79], %[ro], %[wb2]\n\t" \
  "v_add_u32 %[ro], 0x2000, %[ro]\n\t" "global_load_dwordx4 v[80:83], %[ro], %[wb2]\n\t" "v_add_u32 %[ro], 0x2000, %[ro]\n\t" \
  "global_load_dwordx4 v[84:87], %[ro], %[wb2]\n\t" "v_add_u32 %[ro], 0x2000, %[ro]\n\t" "global_load_dwordx4 v[88:91], %[ro], %[wb2]\n\t" \
  "v_add_u32 %[ro], 0x2000, %[ro]\n\t" "global_load_dwordx4 v[92:95], %[ro], %[wb2]\n\t" ""

#define KV_OUT "=&{v[96:127]}"(kv0), "=&{v[128:159]}"(kv1), "=&{v[160:191]}"(kv2)
#define KV_IO "+{v[96:127]}"(kv0), "+{v[128:159]}"(kv1), "+{v[160:191]}"(kv2)
#define KV_IN "{v[96:127]}"(kv0), "{v[128:159]}"(kv1), "{v[160:191]}"(kv2)
#define KV_ADDR [ka0] "v"(kb), [ka1] "v"(kb + 4096), [va0] "v"(vp), [va1] "v"(vp + 4096), [rs] "s"(rs)

__global__ __launch_bounds__(512, 2) void fused_block(const u16* __restrict__ x, const u16* __restrict__ wq, const u16* __restrict__ wk,
                                                      const u16* __restrict__ wv, const u16* __restrict__ wo, const u16* __restrict__ wr,
                                                      const u16* __restrict__ wg, const u16* __restrict__ wu, const u16* __restrict__ wd,
                                                      char* ws, u16* __restrict__ out) {
  __shared__ __attribute__((aligned(16))) char smem[L_TOTAL];
  unsigned pf1, pf2;
  {
    unsigned long long pc; unsigned t;
    asm volatile("s_getpc_b64 %[pc]\n.Lfb_begin:\n\tv_readfirstlane_b32 %[t], %[tid]\n\ts_cmp_lg_u32 %[t], 0\n\ts_cbranch_scc1 .Lfb_nopf\n\t"
                 "v_and_b32 %[a], 15, %[tid]\n\tv_lshlrev_b32 %[b], 7, %[a]\n\tglobal_load_dword %[b], %[b], %[pc]\n\t"
                 "s_lshr_b32 %[t], %[bx], 3\n\ts_mul_i32 %[t], %[t], 1152\n\ts_add_u32 %[t], %[t], 2048\n\t"
                 "v_min_u32 %[a], 8, %[a]\n\tv_lshl_add_u32 %[a], %[a], 7, %[t]\n\tv_min_u32 %[a], .Lfb_end-.Lfb_begin, %[a]\n\t"
                 "global_load_dword %[a], %[a], %[pc]\n.Lfb_nopf:"
                 : [pc] "=&s"(pc), [t] "=&s"(t), [a] "=&v"(pf1), [b] "=&v"(pf2) : [tid] "v"(threadIdx.x), [bx] "s"(blockIdx.x) : "scc", "memory");
  }
  char* const XI = smem + L_XI;
  char* const WI = smem + L_WI;
  char* const KI = smem + L_KI;
  char* const CTL = smem + L_CTL;
  const int tid = threadIdx.x, lane = tid & 63;
  const int w = __builtin_amdgcn_readfirstlane(tid >> 6);
  const int b = blockIdx.x, g8 = b & 7, tb = b & 31, hg = b >> 5;
  const int rr = lane >> 3, c = lane & 7;
  const int h = lane >> 5, l31 = lane & 31;
  const int isV = hg & 1, lr0 = (hg >> 1) * 16;
  const int pid = tb * 8 + hg;
  const int u = (b & 7) * 28 + (b >> 3);
  const rsrc_t rs = __builtin_amdgcn_make_buffer_rsrc(ws, 0, (int)WS_END, 0x00020000);

  unsigned tag;
  asm volatile("s_load_dword %0, %1, 0x0" : "=s"(tag) : "s"(ws) : "memory");
  const bool kvw = (w == (hg & 6));
  const bool ldw = (w == (hg | 1));
  u32x4 xo0, xo1, wr0, wr1, xe, xv[8];
  f32x32 wov;
  unsigned pf0 = 0;
  {
    const u16* p = x + (size_t)(tb * 64 + lr0 + 2 * w) * D_;
    GLD128(xo0, h * 1024 + l31 * 16, p);
    GLD128(xo1, h * 1024 + l31 * 16 + 512, p);
  }
#pragma unroll
  for (int i = 0; i < 2; ++i) glds16((isV ? wv : wk) + (w * 2 + i) * 512, lane * 16, KI + (w * 2 + i) * 1024);
  const unsigned row_off = rr * 1024 + c * 16;
  const unsigned wq_off = rr * 1024 + ((c ^ (((rr >> 1) & 1) << 2)) << 4);
  const u16* xrows = x + (size_t)(tb * 64 + w * 8) * D_;
  if (kvw) {
    __builtin_amdgcn_s_setprio(3);
  {
    unsigned long long ta = (unsigned long long)ws + (lane == 0 ? WS_YP + b * 5376 : (lane == 1 ? WS_K : WS_KVF));
    if (lane == 3) ta = (unsigned long long)(out + (size_t)tb * 64 * D_);
    asm volatile("global_load_dword %0, %1, off" : "=v"(pf0) : "v"(ta) : "memory");
  }
#pragma unroll
    for (int j = 0; j < 8; ++j)
      st128(rs, WS_O + (unsigned)((tb * 2 + (j >> 2)) * 32 + hg * 4 + (j & 3)) * 1024 + lane * 16, u32x4{0xffffffffu, 0xffffffffu, 0xffffffffu, 0xffffffffu});
  } else if (ldw) {
#pragma unroll
    for (int pc = 0; pc < 8; ++pc) glds16(wq + pc * 8 * D_ + hg * 64, wq_off, WI + pc * 1024);
  } else {
#pragma unroll
    for (int i = 0; i < 8; ++i) asm volatile("global_load_dwordx4 %0, %1, %2 offset:%3" : "=v"(xv[i]) : "v"(row_off), "s"(xrows), "i"(i * 128) : "memory");
  }
  VMWAIT(10);
  asm volatile("" : "+v"(xo0), "+v"(xo1), "+v"(pf1), "+v"(pf2));
  {
    float mean, var;
    stats32(xo0, xo1, mean, var);
    asm volatile("" : "+v"(xo0), "+v"(xo1));
    const float mb = rbf(mean), sd = ln_sd(var), inv = __builtin_amdgcn_rcpf(sd);
#pragma unroll
    for (int j = 0; j < 4; ++j) { xo0[j] = ln_pair(xo0[j], mb, inv); xo1[j] = ln_pair(xo1[j], mb, inv); }
    const int lrow = lr0 + 2 * w + h;
    char* xr = XI + lrow * 1024 + ((l31 ^ (lrow & 15)) << 4);
    *(u32x4*)xr = xo0; *(u32x4*)(xr + 512) = xo1;
    if (l31 == 0) *(f32x2*)(CTL + C_STAT + lrow * 8) = f32x2{mb, sd};
    if (tid < 2) *(unsigned*)(CTL + tid * 4) = 0u;
    if (kvw) VMWAIT(9); else VMWAIT(8);
    asm volatile("s_waitcnt lgkmcnt(0)" : "+s"(tag) :: "memory");
    __builtin_amdgcn_s_barrier();
    CBAR();
  }
  tag += 1u;
  if ((w >> 2) == isV) { GLD128(wr0, l31 * 16, wr); GLD128(wr1, l31 * 16 + 512, wr); }
  if (kvw) {
    const int g = lane >> 4, q = (lane & 15) >> 2, p = lane & 3;
    const char* xb = XI + (lr0 + (lane & 15)) * 1024;
    const char* wb = opq(KI + (8 * g + q) * 32 + p * 8);
    f32x4 acc = {}, act = {};
    bf16x8 fx[2][4], fw[2][4];
#define KV_LD(bf, k0) _Pragma("unroll") for (int i = 0; i < 4; ++i) { const int ks = (k0) + i; \
      fx[bf][i] = *(const bf16x8*)(xb + ((((4 * i + g) ^ (lane & 15)) << 4) + (ks >> 2) * 256)); \
      fw[bf][i] = tr_frag(wb + ks * 1024, wb + ks * 1024 + 128); }
    KV_LD(0, 0)
#pragma unroll
    for (int kb = 0; kb < 4; ++kb) {
      if (kb < 3) { KV_LD((kb + 1) & 1, 4 * kb + 4) }
      SB();
#pragma unroll
      for (int i = 0; i < 4; ++i) {
        acc = __builtin_amdgcn_mfma_f32_16x16x32_bf16(fw[kb & 1][i], fx[kb & 1][i], acc, 0, 0, 0);
        act = __builtin_amdgcn_mfma_f32_16x16x32_bf16(fx[kb & 1][i], fw[kb & 1][i], act, 0, 0, 0);
      }
      SB();
    }
    if (isV) acc = act;
    const u32x2 pk = {pack_bf(acc[0], acc[1]), pack_bf(acc[2], acc[3])};
    float n = 0.f;
    if (!isV) {
      st64(rs, WS_K + (unsigned)(tb * 64 + lr0 + (lane & 15)) * 32 + (((g & 1) << 1) | (g >> 1)) * 8, pk);
      n = wave_maxpos(add32(add16(acc[0] * acc[0] + acc[1] * acc[1] + acc[2] * acc[2] + acc[3] * acc[3])));
    } else {
      st64(rs, WS_V + (unsigned)(pid - 1 + (g >> 1)) * VHALF + (lane & 15) * 16 + (g & 1) * 8, pk);
    }
    DRAIN();
    if (lane < 8) st64(rs, WS_KVF + (unsigned)(lane * 256 + pid) * 8, u32x2{__float_as_uint(n), tag});
    __builtin_amdgcn_s_setprio(0);
  } else if (ldw) {
#pragma unroll 4
    for (int pc = 8; pc < 28; ++pc) glds16(wq + pc * 8 * D_ + hg * 64, wq_off, WI + pc * 1024);
  } else {
    const int w6 = w - (w > hg ? 2 : 0);
#pragma unroll
    for (int i = 0; i < 6; ++i) { const int pc = 28 + i * 6 + w6; glds16(wq + pc * 8 * D_ + hg * 64, wq_off, WI + pc * 1024); }
    asm volatile("s_waitcnt vmcnt(6)" : "+v"(xv[0]), "+v"(xv[1]), "+v"(xv[2]), "+v"(xv[3]), "+v"(xv[4]), "+v"(xv[5]), "+v"(xv[6]), "+v"(xv[7]) :: "memory");
    float s = 0.f, q = 0.f;
    unsigned ones = 0x3f803f80u; asm volatile("" : "+v"(ones));
#pragma unroll
    for (int i = 0; i < 8; ++i)
#pragma unroll
      for (int j = 0; j < 4; ++j) { s = dot2(xv[i][j], ones, s); q = dot2(xv[i][j], xv[i][j], q); }
    const float mean = sum8(s) * (1.0f / 512.0f);
    float var = fmaf(-mean, mean, sum8(q) * (1.0f / 512.0f));
    if (__builtin_expect(__any(!(var >= mean * mean * 0.015625f)), 0)) {
      float v = 0.f;
#pragma unroll
      for (int i = 0; i < 8; ++i)
#pragma unroll
        for (int j = 0; j < 4; ++j) { const float a = bf_lo(xv[i][j]) - mean, bb = bf_hi(xv[i][j]) - mean; v = fmaf(a, a, v); v = fmaf(bb, bb, v); }
      var = sum8(v) * (1.0f / 512.0f);
    }
    asm volatile("" : "+v"(xv[0]), "+v"(xv[1]), "+v"(xv[2]), "+v"(xv[3]), "+v"(xv[4]), "+v"(xv[5]), "+v"(xv[6]), "+v"(xv[7]));
    const float st_m = rbf(mean), st_s = ln_sd(var);
    const float inv = __builtin_amdgcn_rcpf(st_s), nb = -st_m * inv;
    const int lrow = w * 8 + rr;
#pragma unroll
    for (int i = 0; i < 8; ++i) {
      u32x4 o;
#pragma unroll
      for (int j = 0; j < 4; ++j) o[j] = pack_bf(fmaf(bf_lo(xv[i][j]), inv, nb), fmaf(bf_hi(xv[i][j]), inv, nb));
      *(u32x4*)(XI + lrow * 1024 + (((i * 8 + c) ^ (lrow & 15)) << 4)) = o;
    }
    if (c == 0) *(f32x2*)(CTL + C_STAT + lrow * 8) = f32x2{st_m, st_s};
  }
  VMWAIT(20);
  asm volatile("" : "+v"(pf0), "+v"(wr0), "+v"(wr1));
  if ((w >> 2) == isV) {
    float r = 0.f;
#pragma unroll
    for (int j = 0; j < 4; ++j) { r = dot2(xo0[j], wr0[j], r); r = dot2(xo1[j], wr1[j], r); }
    const unsigned rkey = make_key(rbf(sum32(r)), pid * 8 + 2 * (w & 3) + h);
    if (l31 == 0) *(unsigned*)(CTL + C_R + (2 * (w & 3) + h) * 4) = rkey;
  }
  if (!kvw) DRAIN();
  RAW_BARRIER();
  if (w < 4) __builtin_amdgcn_s_setprio(2); else __builtin_amdgcn_s_setprio(1);
  if (w == 4) {
    const unsigned key = lane < 8 ? *(const unsigned*)(CTL + C_R + lane * 4) : 0u;
    const unsigned k1 = wave_maxu(key), k2 = wave_maxu(key == k1 ? 0u : key);
    if (lane < 8) st128(rs, WS_CAND + (unsigned)(lane * 256 + pid) * 16, u32x4{k1, tag, k2, tag});
  }
  const int blk = (lane >> 4) & 1, q4 = (lane & 15) >> 2, p4 = lane & 3;
  if (w < 4) {
    const int nt = w & 1, tt = w >> 1;
    const int tok = tt * 32 + l31;
    const char* xb = XI + tok * 1024;
    const char* wb = WI + (8 * h + q4) * 128 + (((4 * nt + 2 * blk + (p4 >> 1)) ^ (((q4 >> 1) & 1) << 2)) << 4) + 8 * (p4 & 1);
    f32x16 acc = {};
    bf16x8 fa[2][4], fb[2][4];
#define Q_LD(bf, k0) _Pragma("unroll") for (int i = 0; i < 4; ++i) { const int ks = (k0) + i; \
      fa[bf][i] = tr_frag(wb + ks * 2048, wb + ks * 2048 + 512); \
      fb[bf][i] = *(const bf16x8*)(xb + ((((2 * (ks & 7) + h) ^ (tok & 15)) << 4) + (ks >> 3) * 256)); }
    Q_LD(0, 0)
    SB();
#pragma unroll
    for (int kb = 0; kb < 8; ++kb) {
      if (kb < 7) { Q_LD((kb + 1) & 1, 4 * kb + 4) }
#pragma unroll
      for (int i = 0; i < 4; ++i) acc = __builtin_amdgcn_mfma_f32_32x32x16_bf16(fa[kb & 1][i], fb[kb & 1][i], acc, 0, 0, 0);
#pragma unroll
      for (int i = 0; i < 4; ++i) { SGB(0x8, 1); SGB(0x100, 3); }
      SB();
    }
    const float cs = 0.25f * 1.4426950408889634f;
    float n0 = 0.f, n1 = 0.f;
#pragma unroll
    for (int r = 0; r < 8; ++r) { acc[r] *= cs; acc[r + 8] *= cs; n0 = fmaf(acc[r], acc[r], n0); n1 = fmaf(acc[r + 8], acc[r + 8], n1); }
    char* qi = smem + L_QI + (tt * 4 + nt * 2) * 1024 + lane * 16;
    *(u32x4*)qi = u32x4{pack_bf(acc[0], acc[1]), pack_bf(acc[2], acc[3]), pack_bf(acc[4], acc[5]), pack_bf(acc[6], acc[7])};
    *(u32x4*)(qi + 1024) = u32x4{pack_bf(acc[8], acc[9]), pack_bf(acc[10], acc[11]), pack_bf(acc[12], acc[13]), pack_bf(acc[14], acc[15])};
    const float nm = wave_maxpos(fmaxf(add32(n0), add32(n1)));
    if (lane == 0) {
      *(float*)(CTL + C_QMAX + w * 4) = nm;
      asm volatile("s_waitcnt lgkmcnt(0)\n\tds_add_u32 %0, %1" :: "v"((unsigned)(uintptr_t)(LAS char*)(CTL + C_CNT)), "v"(1u) : "memory");
    }
  }

  xe = *(const u32x4*)((const char*)(xrows + hg * 64) + row_off);
  f32x32 kv0, kv1, kv2;
  asm volatile(VCONST_ASM : KV_OUT : [cst] "v"((lane & 15) ? 0u : 0x3f803f80u));
  float kmax;
  {
    u32x2 f;
    SPIN {
      f = ld64(rs, WS_KVF + (unsigned)(g8 * 256 + w * 32 + l31) * 8);
      if (__all(f[1] == tag)) break;
      SLEEP(1);
    }
    CBAR();
    kmax = __uint_as_float(f[0]);
  }
  {
    const unsigned kb = WS_K + (unsigned)(w * 256 + l31) * 32 + h * 16;
    const unsigned vp = WS_V + (unsigned)(w * 32 + 2 * blk + h) * VHALF + (lane & 15) * 16;
    asm volatile(LOADS_ASM : KV_IO : KV_ADDR : "memory");
  }
  kmax = wave_maxpos(kmax);
  u32x4 gatep;
  {
    const f32x2 st = *(const f32x2*)(CTL + C_STAT + (w * 8 + rr) * 8);
    const float inv = __builtin_amdgcn_rcpf(st[1]);
#pragma unroll
    for (int j = 0; j < 4; ++j) gatep[j] = silu_pair(ln_pair(xe[j], st[0], inv));
    asm volatile("" : "+v"(gatep));
  }
  lds_wait_ge(CTL + C_CNT, 4u);
  const f32x4 qn = *(const f32x4*)(CTL + C_QMAX);
  const float qmax = fmaxf(fmaxf(qn[0], qn[1]), fmaxf(qn[2], qn[3]));
  const float bound = __builtin_amdgcn_sqrtf(qmax * kmax) * 1.02f + 0.01f;
  int fasti = __builtin_amdgcn_readfirstlane(bound <= 48.0f ? 1 : 0);
  asm volatile("" : "+s"(fasti));
  const bool fast = fasti != 0;
  const int idx = (int)0xDDDDDDDDu;
  bf16x8 zf = {};
  asm volatile("" : "+v"(zf));
  const f32x16 z16 = {};
  const char* qbase = smem + L_QI + lane * 16;
  f32x16 o;
  float shift = bound;
  bf16x8 qf = *(const bf16x8*)(qbase + ((w + 1) & 7) * 1024);
  const unsigned lds0 = (unsigned)(uintptr_t)(LAS char*)smem;
  unsigned qa = lds0 + L_QI + lane * 16 + ((w + 2) & 7) * 1024;
  f32x16 cc = splat16(-bound);
  f32x32 sab;
  int i = 0;
  unsigned so = ((w + 1) & 7) * PREG + 6 * PSLOT, qo = ((w + 2) & 7) * 1024; float t1_; unsigned t2_;
#define RUN_OUT "=&{v[240:255]}"(o), "+{v[192:207]}"(cc), [q] "+v"(qf), [qa] "+v"(qa), [i] "+s"(i), [so] "+s"(so), [qo] "+s"(qo), [t1] "=&v"(t1_), [t2] "=&v"(t2_)
#define RUN_IN [z] "v"(zf), [idx] "v"(idx), [sh] "v"(shift), [lo] "s"(0xffffffffull), [l16] "v"(lds0 + lane * 16), [l4] "v"(lds0 + lane * 4), \
               [qb] "v"(lds0 + L_QI + lane * 16)
  asm volatile("s_cmp_eq_u32 %[fast], 0\n\ts_cbranch_scc1 .Lfb_slow0\n\t" PROLOG0_ASM PASS0_ASM POST_ASM
               "s_branch .Lfb_out%=\n.Lfb_slow0:\n\ts_waitcnt vmcnt(0)\n\t" SWAPS_ASM "\n.Lfb_out%=:"
               : "=&{v[208:239]}"(sab), RUN_OUT, KV_IO : RUN_IN, [fast] "s"(fasti) : "scc", "memory");

  if (w == 2) {
        int lane = tid & 63; asm volatile("" : "+v"(lane));
        unsigned m1 = 0, m2 = 0;
        SPIN {
          bool ok = true; m1 = 0; m2 = 0;
#pragma unroll
          for (int j = 0; j < 4; ++j) {
            const u32x4 cd = ld128(rs, WS_CAND + (unsigned)(g8 * 256 + j * 64 + lane) * 16);
            ok = ok && cd[1] == tag && cd[3] == tag;
            m2 = max(max(m2, cd[2]), min(m1, cd[0])); m1 = max(m1, cd[0]);
          }
          if (__all(ok)) break;
          SLEEP(1);
        }
        CBAR();
        const unsigned k1 = __builtin_amdgcn_readfirstlane(wave_maxu(m1));
        const unsigned k2 = __builtin_amdgcn_readfirstlane(wave_maxu(m1 == k1 ? m2 : m1));
        if (lane == 0) *(u32x2*)(CTL + C_K1) = u32x2{k1, k2};
        if (b < NU) {
          const int h = lane >> 5, l31 = lane & 31;
          const u16* xr = x + key_tok(h ? k2 : k1) * D_ + l31 * 8;
        {
          const u32x4 xs0 = *(const u32x4*)xr, xs1 = *(const u32x4*)(xr + 256);
          float mean, var;
          stats32(xs0, xs1, mean, var);
          const float mb = rbf(mean), inv = __builtin_amdgcn_rcpf(ln_sd(var));
          u32x4 o0;
#pragma unroll
          for (int j = 0; j < 4; ++j) o0[j] = ln_pair((u & 4) ? xs1[j] : xs0[j], mb, inv);
          *(u32x4*)(smem + L_XS + h * 1024 + l31 * 16) = o0;
          asm volatile("s_waitcnt lgkmcnt(0)" ::: "memory");
          if (lane == 0) *(volatile LAS unsigned*)(CTL + C_SEL) = 1u;
        }
        }
  }
  if (w == 0 && b < NU) {
    int ln = tid; asm volatile("" : "+v"(ln));
    const unsigned go = (ln >> 3) * (F_ * 2) + (((ln & 7) ^ (((ln >> 4) & 1) << 2)) << 4);
#pragma unroll
    for (int j = 0; j < 16; ++j) glds16((j < 8 ? wg : wu) + ((u & 7) * 64 + (j & 7) * 8) * F_ + (u >> 3) * 64, go, smem + L_GU + j * 1024);
  }
  if (w == 1 && b < NU) {
#pragma unroll
    for (int f = 0; f < 8; ++f) glds16(wd + (u * 8 + f) * D_, lane * 16, smem + (f < 6 ? L_DN + f * 1088 : L_DN2 + (f - 6) * 1088));
  }
  asm volatile("" : "=&{v[64:95]}"(wov));
  const int iw = (0x33222453 >> (4 * w)) & 15;
  unsigned ro = row_off;
  unsigned long long wb2_;

#pragma nounroll
  while (i < 8) {
    if (b < NU && ((w == 0 && i == 2) || (w == 1 && i == 4))) {
      int lane = tid & 63, u = (b & 7) * 28 + (b >> 3);
      asm volatile("" : "+v"(lane), "+s"(u));
      const int h = lane >> 5, l31 = lane & 31, blk = (lane >> 4) & 1, q4 = (lane & 15) >> 2, p4 = lane & 3;
      if (i == 2) {
        DRAIN();
        lds_wait_ge(CTL + C_SEL, 1u);
        const char* xb = opq(smem + L_XS + (lane & 1) * 1024 + (u & 3) * 128 + h * 16);
        bf16x8 fx[4];
#pragma unroll
        for (int ks = 0; ks < 4; ++ks) fx[ks] = *(const bf16x8*)(xb + ks * 32);
#pragma unroll
        for (int nt = 0; nt < 2; ++nt) {
          const char* wb = opq(smem + L_GU + (8 * h + q4) * 128 + (((4 * nt + 2 * blk + (p4 >> 1)) ^ (((q4 >> 1) & 1) << 2)) << 4) + 8 * (p4 & 1));
#pragma unroll
          for (int mat = 0; mat < 2; ++mat) {
            f32x16 z = z16;
#pragma unroll
            for (int ks = 0; ks < 4; ++ks) z = QK(fx[ks], tr_frag(wb + mat * 8192 + ks * 2048, wb + mat * 8192 + ks * 2048 + 512), z);
            if (lane < 32) st128(rs, WS_P1 + (unsigned)u * 2048 + (mat * 64 + nt * 32 + lane) * 16, u32x4{__float_as_uint(z[0]), tag, __float_as_uint(z[1]), tag});
            SB();
          }
        }
      } else {
        const unsigned po = WS_P1 + (unsigned)((u & ~7) + (lane >> 3)) * 2048 + ((u & 7) * 8 + (lane & 7)) * 16;
        u32x4 pg, pu;
        SPIN {
          pg = ld128(rs, po); pu = ld128(rs, po + 1024);
          if (__all(pg[1] == tag && pg[3] == tag && pu[1] == tag && pu[3] == tag)) break;
          SLEEP(2);
        }
        DRAIN();
        float t[4] = {__uint_as_float(pg[0]), __uint_as_float(pg[2]), __uint_as_float(pu[0]), __uint_as_float(pu[2])};
#pragma unroll
        for (int j = 0; j < 4; ++j) { t[j] += dpp_f<0x128>(t[j]); t[j] = add32(add16(t[j])); }
        const unsigned hp = pack_bf(rbf(silu_f(rbf(t[0]))) * rbf(t[2]), rbf(silu_f(rbf(t[1]))) * rbf(t[3]));
        if (lane < 8) { *(u16*)(smem + L_HS + lane * 2) = (u16)hp; *(u16*)(smem + L_HS + 16 + lane * 2) = (u16)(hp >> 16); }
        u32x4 ha = *(const u32x4*)(smem + L_HS + (lane & 1) * 16);
        if (lane >= 2) ha = u32x4{0u, 0u, 0u, 0u};
        const bf16x8 A = __builtin_bit_cast(bf16x8, ha);
        const int co = (2 * blk + (p4 >> 1)) * 16 + 8 * (p4 & 1);
        const char* db0 = opq(smem + L_DN + q4 * 1088 + co);
        const char* db1 = opq(smem + (q4 < 2 ? L_DN + (4 + q4) * 1088 : L_DN2 + (q4 - 2) * 1088) + co);
#pragma unroll 2
        for (int ct = 0; ct < 16; ++ct) {
          const bf16x8 bw = tr_frag(db0 + ct * 64, db1 + ct * 64);
          const f32x16 z = QK(A, bw, z16);
          auto sw = __builtin_amdgcn_permlane32_swap(__float_as_uint(z[0]), __float_as_uint(z[1]), false, false);
          st32(rs, WS_YP + (unsigned)(h * 64 + ct * 4 + (l31 >> 3)) * (NU * 32) + u * 32 + (l31 & 7) * 4, sw[0]);
        }
        DRAIN();
        if (lane < 8) st32(rs, WS_YF + lane * 1024 + u * 4, tag);
      }
    }
  if (w < 4 && i == 7 && b < NU) __builtin_amdgcn_s_setprio(0);
    int stop = 8;
    if (b < NU) {
      if (w < 4 && i < 7) stop = 7;
      if (w == 0 && i < 2) stop = 2;
      if (w == 1 && i < 4) stop = 4;
    }
    if (!fast) {
      stop = i + 1;
      float m = -3.0e38f;
      asm volatile(ROWMAX_ASM : [m] "+v"(m), "=&{v[208:239]}"(sab) : [q] "v"(qf), KV_IN);
      shift = max32(m);
      cc = splat16(-shift);
      asm volatile(PROLOG_ASM : "=&{v[208:239]}"(sab), "+{v[192:207]}"(cc) : [q] "v"(qf), KV_IN);
    }
    asm volatile(".Lfb_loop%=:\n\t" PASSN_ASM POST_ASM WOLOAD_ASM "\n.Lfb_skip%=:\n\ts_cmp_lt_u32 %[i], %[stop]\n\ts_cbranch_scc1 .Lfb_loop%=\n.Lfb_out%=:"
                 : "+{v[208:239]}"(sab), RUN_OUT, "+{v[64:95]}"(wov), [ro] "+v"(ro), [wb2] "=&s"(wb2_)
                 : RUN_IN, [stop] "s"(stop), [iw] "s"(iw), [wb] "s"(wo + (size_t)(w * 64) * D_ + hg * 64), KV_IN : "scc", "memory");
  }
  {
  int tid = threadIdx.x; asm volatile("" : "+v"(tid));
  const int lane = tid & 63, rr = lane >> 3, c = lane & 7, h = lane >> 5, l31 = lane & 31, blk = (lane >> 4) & 1, q4 = (lane & 15) >> 2, p4 = lane & 3;
  if (w < 4 && b >= NU) {
    SPIN {
      const u32x4 f = ld128(rs, WS_YF + g8 * 1024 + min(lane, 55) * 16);
      if (__all(f[0] == tag && f[1] == tag && f[2] == tag && f[3] == tag)) break;
      SLEEP(8);
    }
    CBAR();
    const unsigned rw = (unsigned)((b - NU) * 4 + w);
    const unsigned yo = WS_YP + rw * (NU * 32) + lane * 16;
    f32x4 y = {};
#pragma unroll
    for (int j = 0; j < 7; ++j) y += __builtin_bit_cast(f32x4, ld128(rs, yo + j * 1024));
#pragma unroll
    for (int j = 0; j < 4; ++j) { float t = y[j]; t += dpp_f<0x4E>(t); t += dpp_f<0x124>(t); t += dpp_f<0x128>(t); y[j] = add32(add16(t)); }
    if (lane < 2) {
      st128(rs, WS_YG + rw * 64 + lane * 32, u32x4{__float_as_uint(y[0]), tag, __float_as_uint(y[1]), tag});
      st128(rs, WS_YG + rw * 64 + lane * 32 + 16, u32x4{__float_as_uint(y[2]), tag, __float_as_uint(y[3]), tag});
    }
  }
  __builtin_amdgcn_s_setprio(0);
  DRAIN();
  const u32x4 xe = *(const u32x4*)(x + (size_t)(tb * 64 + (tid >> 3)) * D_ + hg * 64 + c * 8);
  RAW_BARRIER();
  const unsigned fr = (unsigned)((tb * 2 + (w >> 2)) * 32 + hg * 4 + (w & 3));
  {
    float a[8], pl[7], pm[7];
    auto sw = __builtin_amdgcn_permlane32_swap(__float_as_uint(o[8]), __float_as_uint(o[8]), false, false);
    float l = __uint_as_float(sw[0]);
    float mx = shift;
#pragma unroll
    for (int sidx = 0; sidx < 7; ++sidx) {
      const char* sl = smem + w * PREG + sidx * PSLOT + 2048 + l31 * 4;
      pl[sidx] = *(const float*)sl; pm[sidx] = *(const float*)(sl + 128);
      mx = fmaxf(mx, pm[sidx]);
    }
    const float f0 = __builtin_amdgcn_exp2f(shift - mx);
    l *= f0;
#pragma unroll
    for (int r = 0; r < 8; ++r) a[r] = o[r] * f0;
#pragma unroll
    for (int sidx = 0; sidx < 7; ++sidx) {
      const char* sl = smem + w * PREG + sidx * PSLOT;
      const f32x4 p0 = *(const f32x4*)(sl + lane * 16), p1 = *(const f32x4*)(sl + 1024 + lane * 16);
      const float f = __builtin_amdgcn_exp2f(pm[sidx] - mx);
#pragma unroll
      for (int r = 0; r < 4; ++r) { a[r] = fmaf(p0[r], f, a[r]); a[r + 4] = fmaf(p1[r], f, a[r + 4]); }
      l = fmaf(pl[sidx], f, l);
    }
    const float inv = __builtin_amdgcn_rcpf(l);
    const u32x4 of = {min(pack_bf(a[0] * inv, a[1] * inv), 0xfffffffeu), min(pack_bf(a[2] * inv, a[3] * inv), 0xfffffffeu),
                      min(pack_bf(a[4] * inv, a[5] * inv), 0xfffffffeu), min(pack_bf(a[6] * inv, a[7] * inv), 0xfffffffeu)};
    st128(rs, WS_O + fr * 1024 + lane * 16, of);
    *(u32x4*)(smem + L_QI + w * 1024 + lane * 16) = of;
  }
  asm volatile("s_waitcnt lgkmcnt(0)\n\tds_write_b128 %0, v[64:67]\n\tds_write_b128 %0, v[68:71] offset:1024\n\tds_write_b128 %0, v[72:75] offset:2048\n\t"
               "ds_write_b128 %0, v[76:79] offset:3072\n\tds_write_b128 %0, v[80:83] offset:4096\n\tds_write_b128 %0, v[84:87] offset:5120\n\t"
               "ds_write_b128 %0, v[88:91] offset:6144\n\tds_write_b128 %0, v[92:95] offset:7168"
               :: "v"((unsigned)(uintptr_t)(LAS char*)smem + w * PREG + rr * 128 + ((c ^ (((rr >> 1) & 1) << 2)) << 4)), "{v[64:95]}"(wov) : "memory");
  if (b == 0 && tid == 0) st32(rs, WS_EPOCH, tag);
  RAW_BARRIER();
  const int etok = tid >> 3, gtok = tb * 64 + etok;
  {
    const int tt = w >> 2, kq = w & 3;
    bf16x8 fa[2][8];
#pragma unroll
    for (int nt = 0; nt < 2; ++nt) {
      const char* wb = opq(smem + 2 * kq * PREG + (4 * h + q4) * 128 + (((4 * nt + 2 * blk + (p4 >> 1)) ^ (((q4 >> 1) & 1) << 2)) << 4) + 8 * (p4 & 1));
#pragma unroll
      for (int i = 0; i < 8; ++i) fa[nt][i] = tr_frag(wb + (i >> 2) * PREG + (i & 3) * 2048, wb + (i >> 2) * PREG + (i & 3) * 2048 + 1024);
    }
    SB();
    const unsigned fo = WS_O + (unsigned)((tb * 2 + tt) * 32 + kq * 8) * 1024 + lane * 16;
    u32x4 fb[8];
    if (hg != 2 * kq) {
#pragma unroll
      for (int i = 0; i < 4; ++i) fb[i] = ld128(rs, fo + i * 1024);
    } else {
#pragma unroll
      for (int i = 0; i < 4; ++i) fb[i] = *(const u32x4*)(smem + L_QI + (tt * 4 + i) * 1024 + lane * 16);
    }
    if (hg != 2 * kq + 1) {
#pragma unroll
      for (int i = 4; i < 8; ++i) fb[i] = ld128(rs, fo + i * 1024);
    } else {
#pragma unroll
      for (int i = 0; i < 4; ++i) fb[4 + i] = *(const u32x4*)(smem + L_QI + (tt * 4 + i) * 1024 + lane * 16);
    }
    SB();
    {
      const unsigned k1 = *(const unsigned*)(CTL + C_K1), k2 = *(const unsigned*)(CTL + C_K2);
      const int sel = (gtok == key_tok(k1)) ? 0 : ((gtok == key_tok(k2)) ? 1 : -1);
      if (__builtin_expect(__any(sel >= 0), 0)) {
        const f32x2 st = *(const f32x2*)(CTL + C_STAT + etok * 8);
        const float inv = __builtin_amdgcn_rcpf(st[1]);
        const float rsg = rbf(sigm_f(key_val(sel > 0 ? k2 : k1)));
        const unsigned go = WS_YG + (unsigned)((sel > 0 ? 512 : 0) + hg * 64 + c * 8) * 8;
        u32x4 gq[4];
        SPIN {
          bool ok = true;
#pragma unroll
          for (int j = 0; j < 4; ++j) { gq[j] = ld128(rs, go + j * 16); ok = ok && gq[j][1] == tag && gq[j][3] == tag; }
          if (__all(ok)) break;
          SLEEP(1);
        }
#pragma unroll
        for (int j = 0; j < 4; ++j) {
          const unsigned xn = ln_pair(xe[j], st[0], inv);
          const float u0 = rbf(rbf(__uint_as_float(gq[j][0])) * rsg), u1 = rbf(rbf(__uint_as_float(gq[j][2])) * rsg);
          if (sel >= 0) gatep[j] = silu_pair(pack_bf(bf_lo(xn) + u0, bf_hi(xn) + u1));
        }
      }
    }
    SB();
    f32x16 acc0 = {}, acc1 = {};
#define FB_MAX(i) max(max(fb[i][0], fb[i][1]), max(fb[i][2], fb[i][3]))
#pragma unroll
    for (int hf = 0; hf < 2; ++hf) {
      if (__builtin_expect(__any(max(max(FB_MAX(4 * hf), FB_MAX(4 * hf + 1)), max(FB_MAX(4 * hf + 2), FB_MAX(4 * hf + 3))) == 0xffffffffu), 0)) {
        _Pragma("nounroll") for (int tries = 0; tries < 64; ++tries) {
          unsigned bad = 0;
#pragma unroll
          for (int i = 4 * hf; i < 8; ++i) bad |= __any(FB_MAX(i) == 0xffffffffu) ? 1u << i : 0u;
          const unsigned need = bad & (15u << (4 * hf));
          if (!need) break;
          SPIN {
            const unsigned pv = ld32(rs, fo - lane * 16 + (lane & 7) * 1024 + 1020);
            if (!__any(pv == 0xffffffffu && ((need >> (lane & 7)) & 1u))) break;
            SLEEP(1);
          }
#pragma unroll
          for (int i = 4 * hf; i < 8; ++i) if ((bad >> i) & 1u) fb[i] = ld128(rs, fo + i * 1024);
        }
      }
#pragma unroll
      for (int i = 4 * hf; i < 4 * hf + 4; ++i) {
        acc0 = QK(fa[0][i], __builtin_bit_cast(bf16x8, fb[i]), acc0);
        acc1 = QK(fa[1][i], __builtin_bit_cast(bf16x8, fb[i]), acc1);
      }
      SB();
    }
    char* pr = smem + w * PREG + 8192 + l31 * 256;
#pragma unroll
    for (int g = 0; g < 4; ++g) {
      *(f32x4*)(pr + (((2 * g + h) ^ (l31 & 7)) << 4)) = f32x4{acc0[4 * g], acc0[4 * g + 1], acc0[4 * g + 2], acc0[4 * g + 3]};
      *(f32x4*)(pr + 128 + (((2 * g + h) ^ (l31 & 7)) << 4)) = f32x4{acc1[4 * g], acc1[4 * g + 1], acc1[4 * g + 2], acc1[4 * g + 3]};
    }
  }
  RAW_BARRIER();
  {
    f32x4 s0v = {}, s1v = {};
#pragma unroll
    for (int kq = 0; kq < 4; ++kq) {
      const char* pr = smem + ((w >> 2) * 4 + kq) * PREG + 8192 + (etok & 31) * 256 + (c >> 2) * 128;
      s0v += *(const f32x4*)(pr + ((((2 * c) & 7) ^ rr) << 4)); s1v += *(const f32x4*)(pr + ((((2 * c + 1) & 7) ^ rr) << 4));
    }
    u32x4 ov;
#pragma unroll
    for (int j = 0; j < 4; ++j) {
      const unsigned sv = j < 2 ? pack_bf(s0v[2 * j], s0v[2 * j + 1]) : pack_bf(s1v[2 * j - 4], s1v[2 * j - 3]);
      const unsigned pr2 = pack_bf(bf_lo(sv) * bf_lo(gatep[j]), bf_hi(sv) * bf_hi(gatep[j]));
      ov[j] = pack_bf(bf_lo(xe[j]) + bf_lo(pr2), bf_hi(xe[j]) + bf_hi(pr2));
    }
    *(u32x4*)(out + (size_t)gtok * D_ + hg * 64 + c * 8) = ov;
  }
  }
  asm volatile(".Lfb_end:");
}

extern "C" void kernel_launch(void* const* d_in, const int* in_sizes, int n_in, void* d_out, int out_size, void* d_ws, size_t ws_size,
                              hipStream_t stream) {
  fused_block<<<256, 512, 0, stream>>>((const u16*)d_in[0], (const u16*)d_in[1], (const u16*)d_in[2], (const u16*)d_in[3], (const u16*)d_in[4],
                                       (const u16*)d_in[6], (const u16*)d_in[7], (const u16*)d_in[8], (const u16*)d_in[9], (char*)d_ws, (u16*)d_out);
}
```
